```python
import torch

def _softmax(x, axis=-1):
    return torch.softmax(x, dim=axis)
def _gelu(x):
    return torch.nn.functional.gelu(x, approximate="tanh")
def _silu(x):
    return torch.nn.functional.silu(x)
def _sigmoid(x):
    return torch.sigmoid(x)
def _layernorm(x, axis=-1, eps=1e-5):
    m = x.mean(dim=axis, keepdim=True)
    v = x.var(dim=axis, keepdim=True, unbiased=False)
    return (x - m) / torch.sqrt(v + eps)
def _scatter_add(tgt, idx, src, axis=0):
    return tgt.clone().index_add_(axis, idx, src)
def _segment_sum(src, seg, n):
    out = torch.zeros((n,) + src.shape[1:], dtype=src.dtype)
    return out.index_add_(0, seg, src)
def _pad(x, pw):
    flat = tuple(v for pair in reversed(pw) for v in pair)
    return torch.nn.functional.pad(x, flat)

kernel_name = 'interleave_layers__grouped_kv_mqa__sliding_block__prompt'

def setup_inputs():

    _w = (lambda a: (lambda t: t.to(torch.bfloat16) if t.is_floating_point() else t)(torch.as_tensor(a)))
    def _A_setup():
        B, S, D, H = 1, 2048, 512, 32
        Dh = D // H
        return {
            "x": _w((lambda *s: torch.randn(*s, dtype=torch.bfloat16) * 0.05)(B, S, D)),
            "wq": _w((lambda *s: torch.randn(*s, dtype=torch.bfloat16) * 0.05)(D, H * Dh)),
            "wk": _w((lambda *s: torch.randn(*s, dtype=torch.bfloat16) * 0.05)(D, Dh)),
            "wv": _w((lambda *s: torch.randn(*s, dtype=torch.bfloat16) * 0.05)(D, Dh)),
            "wo": _w((lambda *s: torch.randn(*s, dtype=torch.bfloat16) * 0.05)(H * Dh, D)),
        }
    _A_in = _A_setup()

    def _B_setup():
        B, S, D = 1, 2048, 512
        return {
            "x": _w((lambda *s: torch.randn(*s, dtype=torch.bfloat16) * 0.05)(B, S, D)),
            "wqkv": _w((lambda *s: torch.randn(*s, dtype=torch.bfloat16) * 0.05)(D, 3 * D)),
            "wo": _w((lambda *s: torch.randn(*s, dtype=torch.bfloat16) * 0.05)(D, D)),
        }
    _B_in = _B_setup()

    return {
        "A_x": _A_in["x"],
        "A_wq": _A_in["wq"],
        "A_wk": _A_in["wk"],
        "A_wv": _A_in["wv"],
        "A_wo": _A_in["wo"],
        "B_x": _B_in["x"],
        "B_wqkv": _B_in["wqkv"],
        "B_wo": _B_in["wo"],
    }

def reference(A_x, A_wq, A_wk, A_wv, A_wo, B_x, B_wqkv, B_wo):
    def _A_ref(x, wq, wk, wv, wo):
        B, S, D = x.shape
        H = 32
        Dh = D // H
        q = (lambda x, *d: x.permute(*d))((x @ wq).reshape(B, S, H, Dh), 0, 2, 1, 3)
        k = x @ wk
        v = x @ wv
        scores = torch.einsum("bhqd,bkd->bhqk", q, k) * (Dh ** -0.5)
        p = _softmax(scores, axis=-1)
        o = torch.einsum("bhqk,bkd->bhqd", p, v)
        return (lambda x, *d: x.permute(*d))(o, 0, 2, 1, 3).reshape(B, S, H * Dh) @ wo

    def _B_ref(x, wqkv, wo):
        B, S, D = x.shape
        H = 32; Dh = D // H; W = 256
        qkv = (lambda x, *d: x.permute(*d))((x @ wqkv).reshape(B, S, 3, H, Dh), 2, 0, 3, 1, 4)
        q, k, v = qkv[0], qkv[1], qkv[2]
        scores = torch.einsum("bhqd,bhkd->bhqk", q, k) * (Dh ** -0.5)
        idx = torch.arange(S)
        mask = (idx[:, None] - idx[None, :] >= 0) & (idx[:, None] - idx[None, :] < W)
        scores = torch.where(mask[None, None], scores, -1e9)
        p = _softmax(scores, axis=-1)
        o = torch.einsum("bhqk,bhkd->bhqd", p, v)
        return (lambda x, *d: x.permute(*d))(o, 0, 2, 1, 3).reshape(B, S, D) @ wo

    x = A_x
    x = x + _A_ref(_layernorm(x, axis=-1), A_wq, A_wk, A_wv, A_wo)
    x = x + _B_ref(_layernorm(x, axis=-1), B_wqkv, B_wo)
    x = x + _A_ref(_layernorm(x, axis=-1), A_wq, A_wk, A_wv, A_wo)
    x = x + _B_ref(_layernorm(x, axis=-1), B_wqkv, B_wo)
    return x
```

```cpp
#include <hip/hip_runtime.h>
#include <hip/hip_bf16.h>
#include <stdint.h>

typedef float f32x16 __attribute__((ext_vector_type(16)));
typedef float f32x8 __attribute__((ext_vector_type(8)));
typedef float f32x4 __attribute__((ext_vector_type(4)));
typedef float f32x2 __attribute__((ext_vector_type(2)));
typedef unsigned u32x4 __attribute__((ext_vector_type(4)));
typedef unsigned u32x2 __attribute__((ext_vector_type(2)));
typedef short s16x4 __attribute__((ext_vector_type(4)));
typedef __bf16 bf16x2 __attribute__((ext_vector_type(2)));
typedef __bf16 bf16x8 __attribute__((ext_vector_type(8)));
typedef __bf16 bf16x16 __attribute__((ext_vector_type(16)));
#define LAS __attribute__((address_space(3)))
#define GAS __attribute__((address_space(1)))
#define DEV __device__ __forceinline__
typedef LAS char* lptr;

constexpr int WS_E = 0, WS_RDY = 4096, WS_KAF = WS_RDY + 65536, WS_KBF = WS_KAF + 32768, WS_WT = 262144;
constexpr int WT_WOA = 0, WT_KVB = 512, WT_QB = 1536, WT_WOB = 2048, WT_QA = 2560, WT_KA = 3072, WT_VA = 3104, WT_N = 3136;
constexpr int WS_KVA = WS_WT + WT_N * 1024, WS_O = WS_KVA + 262144, WS_X = WS_O + 4 * 2097152, WS_ST = WS_X + 3 * 2097152;
constexpr int WS_KVB = WS_ST + 3 * 262144, WS_END = WS_KVB + 2 * 4194304;
constexpr int L_AT = 0, L_KA = 0, L_VA = 65536, L_KVB = 65536, L_WQ = 67584, L_WK = 133120;
constexpr int L_CONST = 149504, L_QX = 153600, L_MEAN = 161792, L_RSTD = 162048, L_KMA = 162304, L_KMOWN = 162320, L_KMNB = 162336, L_CNT = 162352;
constexpr int LDS_BYTES = 163840;
constexpr int HFS = 1056, HHS = 528, HTS = 32 * HFS;
constexpr unsigned SENT = 0xFFFFFFFFu, SPIN_CAP = 1u << 15;
constexpr float QSCALE = 0.25f * 1.4426950408889634f, LN_EPS = 1e-5f, MBOUND = 50.f, NEG_BIG = -1e30f;

#define BAR() asm volatile("s_waitcnt lgkmcnt(0)\n\ts_barrier" ::: "memory")
#define VM0() asm volatile("s_waitcnt vmcnt(0)" ::: "memory")
DEV u32x4 ldsr(lptr p) { return *(const LAS u32x4*)p; }
DEV void ldsw(lptr p, u32x4 v) { *(LAS u32x4*)p = v; }
DEV f32x4 ldsrf(lptr p) { return *(const LAS f32x4*)p; }
DEV f32x16 mfma(u32x4 a, u32x4 b, f32x16 c) { return __builtin_amdgcn_mfma_f32_32x32x16_bf16(__builtin_bit_cast(bf16x8, a), __builtin_bit_cast(bf16x8, b), c, 0, 0, 0); }
DEV f32x16 smf(u32x4 a, f32x8 b, f32x16 c) { return __builtin_amdgcn_smfmac_f32_32x32x32_bf16(__builtin_bit_cast(bf16x8, a), __builtin_bit_cast(bf16x16, b), c, 0xDDDD, 0, 0); }
DEV f32x8 lo8(f32x16 v) { return __builtin_shufflevector(v, v, 0, 1, 2, 3, 4, 5, 6, 7); }
DEV f32x8 hi8(f32x16 v) { return __builtin_shufflevector(v, v, 8, 9, 10, 11, 12, 13, 14, 15); }
DEV unsigned pk2(float a, float b) { f32x2 f = {a, b}; return __builtin_bit_cast(unsigned, __builtin_convertvector(f, bf16x2)); }
DEV unsigned pks(float a, float b) { return min(pk2(a, b), SENT - 1u); }
DEV float bflo(unsigned u) { return __builtin_bit_cast(float, u << 16); }
DEV float bfhi(unsigned u) { return __builtin_bit_cast(float, u & 0xFFFF0000u); }
DEV float dot2(unsigned a, unsigned b, float c) { return __builtin_amdgcn_fdot2_f32_bf16(__builtin_bit_cast(bf16x2, a), __builtin_bit_cast(bf16x2, b), c, false); }
DEV float sq4(u32x4 a, float c) { return dot2(a[3], a[3], dot2(a[2], a[2], dot2(a[1], a[1], dot2(a[0], a[0], c)))); }
DEV float sm4(u32x4 a, float c) { const unsigned o = 0x3F803F80u; return dot2(a[3], o, dot2(a[2], o, dot2(a[1], o, dot2(a[0], o, c)))); }
template <int CTRL> DEV float dpp(float x) { return __builtin_bit_cast(float, __builtin_amdgcn_update_dpp(0, __builtin_bit_cast(int, x), CTRL, 0xF, 0xF, true)); }
DEV float sum8(float x) { x += dpp<0xB1>(x); x += dpp<0x4E>(x); x += dpp<0x141>(x); return x; }
DEV float sum16(float x) { x = sum8(x); x += dpp<0x140>(x); return x; }
DEV float max16(float x) { x = fmaxf(x, dpp<0xB1>(x)); x = fmaxf(x, dpp<0x4E>(x)); x = fmaxf(x, dpp<0x141>(x)); x = fmaxf(x, dpp<0x140>(x)); return x; }
DEV float max32(float x) { x = max16(x); auto r = __builtin_amdgcn_permlane16_swap(__float_as_uint(x), __float_as_uint(x), false, false); return fmaxf(__uint_as_float(r[0]), __uint_as_float(r[1])); }
DEV float addx32(float x) { auto r = __builtin_amdgcn_permlane32_swap(__float_as_uint(x), __float_as_uint(x), false, false); return __uint_as_float(r[0]) + __uint_as_float(r[1]); }
DEV float maxx32(float x) { auto r = __builtin_amdgcn_permlane32_swap(__float_as_uint(x), __float_as_uint(x), false, false); return fmaxf(__uint_as_float(r[0]), __uint_as_float(r[1])); }
DEV unsigned mx4(u32x4 a) { return max(max(a[0], a[1]), max(a[2], a[3])); }
DEV int slot_of(int t) { return (t & 0x13) | ((t & 4) << 1) | ((t & 8) >> 1); }
DEV u32x4 trfrag(lptr p, int second) {
    s16x4 a = __builtin_amdgcn_ds_read_tr16_b64_v4i16((LAS s16x4*)p), b = __builtin_amdgcn_ds_read_tr16_b64_v4i16((LAS s16x4*)(p + second));
    u32x2 x = __builtin_bit_cast(u32x2, a), y = __builtin_bit_cast(u32x2, b);
    return (u32x4){x[0], x[1], y[0], y[1]};
}
typedef __amdgpu_buffer_rsrc_t rsrc_t;
DEV void st16(rsrc_t r, int off, u32x4 v) { __builtin_amdgcn_raw_buffer_store_b128(v, r, off, 0, 16); }
DEV void st8(rsrc_t r, int off, u32x2 v) { __builtin_amdgcn_raw_buffer_store_b64(v, r, off, 0, 16); }
DEV u32x4 ld16(rsrc_t r, int off) { return __builtin_amdgcn_raw_buffer_load_b128(r, off, 0, 16); }
DEV u32x2 poll8(const char* p) { unsigned long long x = __hip_atomic_load((GAS unsigned long long*)p, __ATOMIC_RELAXED, __HIP_MEMORY_SCOPE_AGENT); return (u32x2){(unsigned)x, (unsigned)(x >> 32)}; }
#define UNI(x) __builtin_amdgcn_readfirstlane(x)
DEV void gld1(u32x4& a, unsigned vo, u32x4 rd, int so) { asm volatile("s_nop 4\n\tbuffer_load_dwordx4 %0, %1, %2, %3 offen sc1" : "=&v"(a) : "v"(vo), "s"(rd), "s"(UNI(so)) : "memory"); }
DEV void gld2(u32x4& a, u32x4& b, unsigned vo, u32x4 rd, int so) {
    asm volatile("s_nop 4\n\tbuffer_load_dwordx4 %0, %2, %3, %4 offen sc1\n\tbuffer_load_dwordx4 %1, %2, %3, %4 offen offset:1024 sc1" : "=&v"(a), "=&v"(b) : "v"(vo), "s"(rd), "s"(UNI(so)) : "memory"); }
DEV void gld4(u32x4& a, u32x4& b, u32x4& c, u32x4& d, unsigned vo, u32x4 rd, int so) {
    asm volatile("s_nop 4\n\tbuffer_load_dwordx4 %0, %4, %5, %6 offen sc1\n\tbuffer_load_dwordx4 %1, %4, %5, %6 offen offset:1024 sc1\n\t"
                 "buffer_load_dwordx4 %2, %4, %5, %6 offen offset:2048 sc1\n\tbuffer_load_dwordx4 %3, %4, %5, %6 offen offset:3072 sc1"
                 : "=&v"(a), "=&v"(b), "=&v"(c), "=&v"(d) : "v"(vo), "s"(rd), "s"(UNI(so)) : "memory"); }
template <int N> DEV void wait2(u32x4& a, u32x4& b) { asm volatile("s_waitcnt vmcnt(%2)" : "+v"(a), "+v"(b) : "i"(N) : "memory"); }
template <int N> DEV void wait4(u32x4& a, u32x4& b, u32x4& c, u32x4& d) { asm volatile("s_waitcnt vmcnt(%4)" : "+v"(a), "+v"(b), "+v"(c), "+v"(d) : "i"(N) : "memory"); }
DEV void dma1(unsigned vo, const char* sb, unsigned ldsb) {
    unsigned keep;
    asm volatile("s_mov_b32 %0, m0\n\ts_mov_b32 m0, %3\n\ts_nop 0\n\tglobal_load_lds_dwordx4 %1, %2\n\ts_mov_b32 m0, %0" : "=&s"(keep) : "v"(vo), "s"(sb), "s"(ldsb) : "memory"); }
DEV void dma4s(unsigned vo, u32x4 rd, int so, unsigned ldsb) {
    unsigned keep;
    asm volatile("s_mov_b32 %0, m0\n\ts_mov_b32 m0, %4\n\ts_nop 4\n\t"
                 "buffer_load_dwordx4 %1, %2, %3 offen sc1 lds\n\tbuffer_load_dwordx4 %1, %2, %3 offen offset:1024 sc1 lds\n\t"
                 "buffer_load_dwordx4 %1, %2, %3 offen offset:2048 sc1 lds\n\tbuffer_load_dwordx4 %1, %2, %3 offen offset:3072 sc1 lds\n\ts_mov_b32 m0, %0"
                 : "=&s"(keep) : "v"(vo), "s"(rd), "s"(UNI(so)), "s"(UNI(ldsb)) : "memory"); }

template <int N> DEV void stage2(u32x4& a, u32x4& b, unsigned vo, u32x4 rd, int sb, lptr dst) {
    wait2<N>(a, b);
    _Pragma("clang loop unroll(disable)") for (unsigned sp = 0; !__all(max(mx4(a), mx4(b)) != SENT) && sp < SPIN_CAP; ++sp) { gld2(a, b, vo, rd, sb); wait2<0>(a, b); }
    ldsw(dst, a); ldsw(dst + 1024, b);
}
template <int N> DEV void stage4(u32x4& a, u32x4& b, u32x4& c, u32x4& d, unsigned vo, u32x4 rd, int sb, lptr dst) {
    wait4<N>(a, b, c, d);
    _Pragma("clang loop unroll(disable)") for (unsigned sp = 0; !__all(max(max(mx4(a), mx4(b)), max(mx4(c), mx4(d))) != SENT) && sp < SPIN_CAP; ++sp) { gld4(a, b, c, d, vo, rd, sb); wait4<0>(a, b, c, d); }
    ldsw(dst, a); ldsw(dst + 1024, b); ldsw(dst + 2048, c); ldsw(dst + 3072, d);
}
template <int N> DEV void stage_stats(u32x4& a, u32x4& b, unsigned vo, u32x4 rd, int sb, lptr lds, int u) {
    wait2<N>(a, b);
    _Pragma("clang loop unroll(disable)") for (unsigned sp = 0; !__all(max(mx4(a), mx4(b)) != SENT) && sp < SPIN_CAP; ++sp) { gld1(a, vo, rd, sb); gld1(b, vo, rd, sb + 4096); wait2<0>(a, b); }
#pragma unroll
    for (int tb = 0; tb < 2; ++tb) {
        const u32x4 v = tb ? b : a;
        const float s0 = sum16(__uint_as_float(v[0])), q0 = sum16(__uint_as_float(v[1])), s1 = sum16(__uint_as_float(v[2])), q1 = sum16(__uint_as_float(v[3]));
        const float s = (u & 1) ? s1 : s0, q = (u & 1) ? q1 : q0;
        const float mean = s * (1.f / 512.f), var = fmaxf(q * (1.f / 512.f) - mean * mean, 0.f);
        if ((u & 15) < 2) { const int tok = tb * 32 + 2 * (u >> 4) + (u & 1); *(LAS float*)(lds + L_MEAN + tok * 4) = mean; *(LAS float*)(lds + L_RSTD + tok * 4) = __builtin_amdgcn_rsqf(var + LN_EPS); }
    }
}

#define ATT_BODY \
    "v_exp_f32 v200, v200\n v_exp_f32 v201, v201\n s_waitcnt lgkmcnt(1)\n v_smfmac_f32_32x32x32_bf16 v[168:183], v[244:247], v[224:231], v251\n" \
    "v_exp_f32 v202, v202\n v_exp_f32 v203, v203\n v_exp_f32 v204, v204\n v_mfma_f32_32x32x16_bf16 v[216:231], v[236:239], v[232:235], v[184:199]\n" \
    "ds_read_b128 v[236:239], v248 offset:2048\n" \
    "ds_read_b128 v[244:247], v249 offset:512\n" \
    "v_exp_f32 v205, v205\n v_exp_f32 v206, v206\n v_exp_f32 v207, v207\n v_exp_f32 v208, v208\n" \
    "v_exp_f32 v209, v209\n s_waitcnt lgkmcnt(2)\n v_smfmac_f32_32x32x32_bf16 v[168:183], v[240:243], v[200:207], v251\n" \
    "ds_read_b128 v[240:243], v249 offset:1024\n" \
    "v_exp_f32 v210, v210\n v_exp_f32 v211, v211\n v_exp_f32 v212, v212\n v_exp_f32 v213, v213\n" \
    "v_exp_f32 v214, v214\n v_exp_f32 v215, v215\n v_exp_f32 v216, v216\n v_exp_f32 v217, v217\n" \
    "s_waitcnt lgkmcnt(1)\n v_smfmac_f32_32x32x32_bf16 v[168:183], v[244:247], v[208:215], v251\n" \
    "v_exp_f32 v218, v218\n v_exp_f32 v219, v219\n v_exp_f32 v220, v220\n v_mfma_f32_32x32x16_bf16 v[200:215], v[236:239], v[232:235], v[184:199]\n" \
    "ds_read_b128 v[236:239], v248 offset:3072\n" \
    "ds_read_b128 v[244:247], v249 offset:1536\n" \
    "v_exp_f32 v221, v221\n v_exp_f32 v222, v222\n v_exp_f32 v223, v223\n v_exp_f32 v224, v224\n" \
    "v_exp_f32 v225, v225\n s_waitcnt lgkmcnt(2)\n v_smfmac_f32_32x32x32_bf16 v[168:183], v[240:243], v[216:223], v251\n" \
    "ds_read_b128 v[240:243], v249 offset:2048\n" \
    "v_exp_f32 v226, v226\n v_exp_f32 v227, v227\n v_exp_f32 v228, v228\n v_exp_f32 v229, v229\n" \
    "v_exp_f32 v230, v230\n v_exp_f32 v231, v231\n v_exp_f32 v200, v200\n v_exp_f32 v201, v201\n" \
    "s_waitcnt lgkmcnt(1)\n v_smfmac_f32_32x32x32_bf16 v[168:183], v[244:247], v[224:231], v251\n" \
    "v_exp_f32 v202, v202\n v_exp_f32 v203, v203\n v_exp_f32 v204, v204\n v_mfma_f32_32x32x16_bf16 v[216:231], v[236:239], v[232:235], v[184:199]\n" \
    "ds_read_b128 v[236:239], v248 offset:4096\n" \
    "ds_read_b128 v[244:247], v249 offset:2560\n" \
    "v_exp_f32 v205, v205\n v_exp_f32 v206, v206\n v_exp_f32 v207, v207\n v_exp_f32 v208, v208\n" \
    "v_exp_f32 v209, v209\n s_waitcnt lgkmcnt(2)\n v_smfmac_f32_32x32x32_bf16 v[168:183], v[240:243], v[200:207], v251\n" \
    "ds_read_b128 v[240:243], v249 offset:3072\n" \
    "v_exp_f32 v210, v210\n v_exp_f32 v211, v211\n v_exp_f32 v212, v212\n v_exp_f32 v213, v213\n" \
    "v_exp_f32 v214, v214\n v_exp_f32 v215, v215\n v_exp_f32 v216, v216\n v_exp_f32 v217, v217\n" \
    "s_waitcnt lgkmcnt(1)\n v_smfmac_f32_32x32x32_bf16 v[168:183], v[244:247], v[208:215], v251\n" \
    "v_exp_f32 v218, v218\n v_exp_f32 v219, v219\n v_exp_f32 v220, v220\n v_mfma_f32_32x32x16_bf16 v[200:215], v[236:239], v[232:235], v[184:199]\n" \
    "ds_read_b128 v[236:239], v248 offset:5120\n" \
    "ds_read_b128 v[244:247], v249 offset:3584\n" \
    "v_exp_f32 v221, v221\n v_exp_f32 v222, v222\n v_exp_f32 v223, v223\n v_exp_f32 v224, v224\n" \
    "v_exp_f32 v225, v225\n s_waitcnt lgkmcnt(2)\n v_smfmac_f32_32x32x32_bf16 v[168:183], v[240:243], v[216:223], v251\n" \
    "v_add_u32 v249, v249, v250\n v_add_u32 v248, 4096, v248\n ds_read_b128 v[240:243], v249\n" \
    "v_exp_f32 v226, v226\n v_exp_f32 v227, v227\n v_exp_f32 v228, v228\n v_exp_f32 v229, v229\n" \
    "v_exp_f32 v230, v230\n v_exp_f32 v231, v231\n" \
    ""
struct AttState { f32x16 o, c, a, b; u32x4 q, k, v0, v1; unsigned ka, va, vs, idx; };
#define ATT_STATE_OPS(s) "+{v[168:183]}"(s.o), "+{v[200:215]}"(s.a), "+{v[216:231]}"(s.b), "+{v[236:239]}"(s.k), "+{v[240:243]}"(s.v0), "+{v[244:247]}"(s.v1), "+{v248}"(s.ka), "+{v249}"(s.va)
#define ATT_CONST_OPS(s) "{v[184:199]}"(s.c), "{v[232:235]}"(s.q), "{v250}"(s.vs), "{v251}"(s.idx)
DEV void att_begin(AttState& s) {
    asm volatile("ds_read_b128 v[236:239], v248\n\ts_waitcnt lgkmcnt(0)\n\t"
                 "v_mfma_f32_32x32x16_bf16 v[200:215], v[236:239], v[232:235], v[184:199]\n\t"
                 "ds_read_b128 v[236:239], v248 offset:1024\n\tds_read_b128 v[244:247], v249 offset:512\n\tds_read_b128 v[240:243], v249\n\t"
                 "s_waitcnt lgkmcnt(0)\n\ts_nop 7\n\ts_nop 7"
                 : ATT_STATE_OPS(s) : ATT_CONST_OPS(s) : "memory");
}
DEV void att_run(AttState& s, int groups) {
    int n = groups;
    asm volatile("s_nop 1\n1:\n" ATT_BODY "s_sub_u32 %8, %8, 1\n\ts_cmp_lg_u32 %8, 0\n\ts_cbranch_scc1 1b\n\ts_waitcnt lgkmcnt(0)\n\ts_nop 7\n\ts_nop 7"
                 : ATT_STATE_OPS(s), "+s"(n) : ATT_CONST_OPS(s) : "scc", "memory");
}
DEV void att_end(AttState& s) {
    asm volatile("s_nop 1\n\tv_smfmac_f32_32x32x32_bf16 v[168:183], v[244:247], v[224:231], v251\n\ts_nop 7\n\ts_nop 7" : ATT_STATE_OPS(s) : ATT_CONST_OPS(s) : "memory");
}
DEV f32x16 splat16(float v) { f32x16 r; for (int i = 0; i < 16; ++i) r[i] = v; return r; }
DEV float rowmax16(f32x16 s, float m) { for (int i = 0; i < 16; ++i) m = fmaxf(m, s[i]); return m; }
DEV u32x4 norm_pack(f32x16 o) { const float r = __builtin_amdgcn_rcpf(o[8]); return (u32x4){pks(o[0] * r, o[1] * r), pks(o[2] * r, o[3] * r), pks(o[4] * r, o[5] * r), pks(o[6] * r, o[7] * r)}; }
constexpr unsigned long long diag_mask(int r) {
    unsigned long long m = 0;
    for (int l = 0; l < 64; ++l) { const int k = (r & 3) + 8 * (r >> 2) + 4 * (l >> 5), key = (k & 0x13) | ((k & 4) << 1) | ((k & 8) >> 1); if (key <= (l & 31)) m |= 1ull << l; }
    return m;
}
template <int R> DEV void mask_fill(f32x16& c, float on, float off) { c[R] = __builtin_amdgcn_inverse_ballot_w64(diag_mask(R)) ? on : off; if constexpr (R < 15) mask_fill<R + 1>(c, on, off); }

DEV void fold_t(const f32x16& acc, const f32x16& cs, float mean, float scale, u32x4& f0, u32x4& f1) {
    float y[16];
#pragma unroll
    for (int r = 0; r < 16; ++r) y[r] = (acc[r] - mean * cs[r]) * scale;
    f0 = (u32x4){pks(y[0], y[1]), pks(y[2], y[3]), pks(y[4], y[5]), pks(y[6], y[7])};
    f1 = (u32x4){pks(y[8], y[9]), pks(y[10], y[11]), pks(y[12], y[13]), pks(y[14], y[15])};
}
DEV void fold_n(const f32x16& acc, float cs, lptr lds, int tb, int h, float (&y)[16]) {
#pragma unroll
    for (int g = 0; g < 4; ++g) {
        const f32x4 m = ldsrf(lds + L_MEAN + (tb * 32 + 8 * g + 4 * h) * 4), r = ldsrf(lds + L_RSTD + (tb * 32 + 8 * g + 4 * h) * 4);
#pragma unroll
        for (int e = 0; e < 4; ++e) y[4 * g + e] = (acc[4 * g + e] - m[e] * cs) * r[e];
    }
}

DEV void kv_partial(const f32x16& acc, const f32x16& cs, bool isV, int tb, lptr lds, lptr red, int lane) {
    const int n = lane & 31, h = lane >> 5;
    if (!isV) {
        const float mean = *(LAS float*)(lds + L_MEAN + (tb * 32 + n) * 4), rstd = *(LAS float*)(lds + L_RSTD + (tb * 32 + n) * 4);
        f32x4 a, b;
#pragma unroll
        for (int e = 0; e < 4; ++e) { a[e] = (acc[e] - mean * cs[e]) * rstd; b[e] = (acc[4 + e] - mean * cs[4 + e]) * rstd; }
        lptr p = red + (h * 32 + slot_of(n)) * 32;
        *(LAS f32x4*)p = a; *(LAS f32x4*)(p + 16) = b;
    } else {
        float y[16]; fold_n(acc, cs[0], lds, tb, h, y);
        if (n < 16) {
#pragma unroll
            for (int s = 0; s < 2; ++s) { lptr p = red + ((s * 2 + h) * 16 + n) * 32;
                *(LAS f32x4*)p = (f32x4){y[8 * s], y[8 * s + 1], y[8 * s + 2], y[8 * s + 3]}; *(LAS f32x4*)(p + 16) = (f32x4){y[8 * s + 4], y[8 * s + 5], y[8 * s + 6], y[8 * s + 7]}; }
        }
    }
}
DEV float kv_final(lptr red0, rsrc_t rw, int dst, int lane) {
    f32x4 a = ldsrf(red0 + lane * 32), b = ldsrf(red0 + lane * 32 + 16);
#pragma unroll
    for (int i = 1; i < 4; ++i) { a += ldsrf(red0 + i * 4096 + lane * 32); b += ldsrf(red0 + i * 4096 + lane * 32 + 16); }
    const u32x4 f = {pks(a[0], a[1]), pks(a[2], a[3]), pks(b[0], b[1]), pks(b[2], b[3])};
    st16(rw, dst + lane * 16, f);
    return max32(addx32(sq4(f, 0.f)));
}

template <bool g0>
DEV void program(const char* __restrict__ x0, const char* __restrict__ wq, const char* __restrict__ wk, const char* __restrict__ wv, const char* __restrict__ woa,
                 const char* __restrict__ wqkv, const char* __restrict__ wob, char* __restrict__ out, char* ws, lptr lds_, const int w_) {
#define FRESH() lptr lds = lds_; asm volatile("" : "+s"(lds)); const unsigned lds0 = (unsigned)(size_t)lds; (void)lds0; int w = w_, b = blockIdx.x, lane = __builtin_amdgcn_mbcnt_hi(~0u, __builtin_amdgcn_mbcnt_lo(~0u, 0u)); asm volatile("" : "+s"(w), "+s"(b), "+v"(lane)); \
    const int wi = w & 3, tid = w * 64 + lane, rb = 4 * (b & 7) + (b >> 6), cg = (b >> 3) & 7, n = lane & 31, h = lane >> 5, tbo = cg & 1, fg = cg >> 1; \
    unsigned one_ = 0x3F803F80u, tag = tag_; float zero_ = 0.f; asm volatile("" : "+v"(one_), "+v"(zero_), "+s"(tag)); const u32x4 ONES = {one_, one_, one_, one_}; (void)ONES; \
    const bool prod = cg < 4, isV = (cg >> 1) & 1; (void)wi; (void)tid; (void)rb; (void)n; (void)h; (void)tbo; (void)fg; (void)prod; (void)isV
    const rsrc_t rw = __builtin_amdgcn_make_buffer_rsrc(ws, 0, WS_END, 0x00020000);
    const u32x4 rd = {(unsigned)(size_t)ws, (unsigned)((size_t)ws >> 32) & 0xFFFFu, (unsigned)WS_END, 0x00020000u};
    unsigned ev, tag_ = 0;
    asm volatile("global_load_dword %0, %1, %2 sc1" : "=&v"(ev) : "v"(0), "s"(ws + WS_E) : "memory");
    const f32x16 ZERO = splat16(0.f);
    u32x4 W[32];
    u32x4 xr0, xr1;

    {
        FRESH();
        if (g0 && prod) {
            const char* src = (isV ? wv : wk) + wi * 2048;
#pragma unroll
            for (int j = 0; j < 4; ++j) dma1(lane * 16, src + (j >> 1) * 8192 + (j & 1) * 1024, lds0 + L_WK + wi * 4096 + j * 1024);
        }
        u32x4 xv[8];
        const int row = 8 * w + (lane >> 3);
        {
            const char* src = x0 + (size_t)(rb * 64 + 8 * w) * 1024; const unsigned vo = (lane >> 3) * 1024 + (lane & 7) * 16;
            asm volatile("global_load_dwordx4 %0, %8, %9\n\tglobal_load_dwordx4 %1, %8, %9 offset:128\n\tglobal_load_dwordx4 %2, %8, %9 offset:256\n\tglobal_load_dwordx4 %3, %8, %9 offset:384\n\t"
                         "global_load_dwordx4 %4, %8, %9 offset:512\n\tglobal_load_dwordx4 %5, %8, %9 offset:640\n\tglobal_load_dwordx4 %6, %8, %9 offset:768\n\tglobal_load_dwordx4 %7, %8, %9 offset:896"
                         : "=&v"(xv[0]), "=&v"(xv[1]), "=&v"(xv[2]), "=&v"(xv[3]), "=&v"(xv[4]), "=&v"(xv[5]), "=&v"(xv[6]), "=&v"(xv[7]) : "v"(vo), "s"(src) : "memory");
        }
        if constexpr (!g0) {
            const unsigned vo = (lane >> 3) * 1024 + (((lane & 7) ^ (((lane >> 4) & 1) << 2)) * 16);
            const char* src = wq + cg * 128 + (size_t)wi * 131072;
#pragma unroll
            for (int m = 0; m < 16; ++m) dma1(vo, src + m * 8192, lds0 + L_WQ + wi * 16384 + m * 1024);
        }
        if (tid < 256) { const unsigned v = ((tid & 11) == 0) ? 0x3F803F80u : 0u; ldsw(lds + L_CONST + tid * 16, (u32x4){v, v, v, v}); }
        if (tid == 0) *(LAS unsigned*)(lds + L_CNT) = 0u;
        if constexpr (g0) asm volatile("s_waitcnt vmcnt(0)" : "+v"(xv[0]), "+v"(xv[1]), "+v"(xv[2]), "+v"(xv[3]), "+v"(xv[4]), "+v"(xv[5]), "+v"(xv[6]), "+v"(xv[7]), "+v"(ev) :: "memory");
        else asm volatile("s_waitcnt vmcnt(16)" : "+v"(xv[0]), "+v"(xv[1]), "+v"(xv[2]), "+v"(xv[3]), "+v"(xv[4]), "+v"(xv[5]), "+v"(xv[6]), "+v"(xv[7]), "+v"(ev) :: "memory");
        tag = tag_ = __builtin_amdgcn_readfirstlane(ev) + 1u;
        float s = 0.f, q = 0.f;
        lptr dst = lds + L_AT + (row >> 5) * HTS + ((lane & 7) >> 1) * HFS + (row & 31) * 16 + (lane & 1) * 8;
#pragma unroll
        for (int m = 0; m < 8; ++m) {
            s = sm4(xv[m], s); q = sq4(xv[m], q);
            *(LAS u32x2*)(dst + m * 4 * HFS) = (u32x2){xv[m][0], xv[m][1]};
            *(LAS u32x2*)(dst + m * 4 * HFS + HHS) = (u32x2){xv[m][2], xv[m][3]};
        }
        s = sum8(s); q = sum8(q);
        const float mean = s * (1.f / 512.f), var = fmaxf(q * (1.f / 512.f) - mean * mean, 0.f);
        if ((lane & 7) == 0) { *(LAS float*)(lds + L_MEAN + row * 4) = mean; *(LAS float*)(lds + L_RSTD + row * 4) = __builtin_amdgcn_rsqf(var + LN_EPS); }
        BAR();
        if constexpr (g0) {
            if (prod) {
                lptr wp = lds + L_WK + wi * 4096 + (4 * h + ((lane & 15) >> 2)) * 32 + (lane & 3) * 8;
                f32x16 acc = ZERO, cs = ZERO;
                lptr ap = lds + L_AT + tbo * HTS + h * HHS + n * 16 + wi * 4 * HFS;
                u32x4 xf[8], Wj[8];
#pragma unroll
                for (int j = 0; j < 8; ++j) { Wj[j] = trfrag(wp + j * 512, 256); xf[j] = ldsr(ap + ((j >> 2) * 16 + (j & 3)) * HFS); }
                if (isV) {
#pragma unroll
                    for (int j = 0; j < 8; ++j) { acc = mfma(xf[j], Wj[j], acc); cs = mfma(ONES, Wj[j], cs); }
                } else {
#pragma unroll
                    for (int j = 0; j < 8; ++j) { acc = mfma(Wj[j], xf[j], acc); cs = mfma(Wj[j], ONES, cs); }
                }
                kv_partial(acc, cs, isV, tbo, lds, lds + L_WK + wi * 4096, lane);
            }
            BAR();
            if (prod) {
                if (w == 0) {
                    const float km = kv_final(lds + L_WK, rw, WS_KVA + (isV ? 65536 : 0) + (rb * 2 + tbo) * 1024, lane);
                    VM0();
                    if (lane == 0) st8(rw, WS_KAF + (rb * 4 + cg) * 128, (u32x2){isV ? 0u : __float_as_uint(km), tag});
                }
            } else {
                const int gw = (rb * 4 + (cg & 3)) * 4 + wi;
                lptr sc = lds + L_WK + wi * 4096;
                for (int blk = gw; blk < 768; blk += 512) {
                    const int ks = blk & 31, gi = blk >> 5;
                    const char* src; int ld, id0, idt;
                    if (gi < 4) { src = woa + gi * 256; ld = 1024; id0 = WT_WOA + gi * 128; idt = 0; }
                    else if (gi < 8) { src = wqkv + (gi - 4) * 256; ld = 3072; id0 = WT_QB + (gi - 4) * 128; idt = 0; }
                    else if (gi < 16) { src = wqkv + (gi - 4) * 256; ld = 3072; id0 = WT_KVB + ((gi - 8) & 3) * 256 + ((gi - 8) >> 2) * 64; idt = 1; }
                    else if (gi < 20) { src = wob + (gi - 16) * 256; ld = 1024; id0 = WT_WOB + (gi - 16) * 128; idt = 0; }
                    else { src = wq + (gi - 20) * 256; ld = 1024; id0 = WT_QA + (gi - 20) * 128; idt = 0; }
                    u32x4 t[4];
#pragma unroll
                    for (int m = 0; m < 4; ++m) t[m] = *(const u32x4*)(src + (size_t)(16 * ks + 4 * m + (lane >> 4)) * ld + (lane & 15) * 16);
#pragma unroll
                    for (int m = 0; m < 4; ++m) ldsw(sc + m * 1024 + lane * 16, t[m]);
                    lptr tp = sc + (4 * h + ((lane & 15) >> 2)) * 256 + ((lane >> 4) & 1) * 32 + (lane & 3) * 8;
#pragma unroll
                    for (int j = 0; j < 4; ++j) {
                        const u32x4 f = trfrag(tp + j * 64, 2048);
                        const int id = id0 + (idt ? (j >> 1) * 128 + (j & 1) * 32 : j * 32) + ks;
                        st16(rw, WS_WT + id * 1024 + lane * 16, f);
                    }
                }
                if (gw >= 256 && gw < 320) {
                    const int ks = gw & 31, v = (gw >> 5) & 1;
                    const unsigned short* src = (const unsigned short*)(v ? wv : wk) + (16 * ks + 4 * h) * 16 + (lane & 15);
                    u32x4 f;
#pragma unroll
                    for (int j = 0; j < 4; ++j) { const int r0 = (j >> 1) * 8 + (j & 1) * 2; f[j] = (unsigned)src[r0 * 16] | ((unsigned)src[(r0 + 1) * 16] << 16); }
                    st16(rw, WS_WT + ((v ? WT_VA : WT_KA) + ks) * 1024 + lane * 16, f);
                }
            }
        } else {
            BAR();
            VM0();
            if (lane == 0) __hip_atomic_fetch_add((LAS unsigned*)(lds + L_CNT), 1u, __ATOMIC_RELAXED, __HIP_MEMORY_SCOPE_WORKGROUP);
            while (__hip_atomic_load((LAS unsigned*)(lds + L_CNT), __ATOMIC_RELAXED, __HIP_MEMORY_SCOPE_WORKGROUP) < 4u) __builtin_amdgcn_s_sleep(1);
            asm volatile("" ::: "memory");
            const int f = wi & 1, tb = wi >> 1, qq = (lane & 15) >> 2;
            lptr wp = lds + L_WQ + (4 * h + qq) * 128 + ((f * 64 + ((lane >> 4) & 1) * 32 + (lane & 3) * 8) ^ ((qq >> 1) << 6));
            lptr ap = lds + L_AT + tb * HTS + h * HHS + n * 16;
            f32x16 acc = ZERO, cs = ZERO;
#pragma unroll
            for (int ks = 0; ks < 32; ++ks) { const u32x4 wf = trfrag(wp + ks * 2048, 1024); acc = mfma(wf, ldsr(ap + ks * HFS), acc); cs = mfma(wf, ONES, cs); }
            const float mean = *(LAS float*)(lds + L_MEAN + (tb * 32 + n) * 4), rstd = *(LAS float*)(lds + L_RSTD + (tb * 32 + n) * 4);
            u32x4 q0, q1; fold_t(acc, cs, mean, rstd * QSCALE, q0, q1);
            ldsw(lds + L_QX + ((2 * f) * 2 + tb) * 1024 + lane * 16, q0); ldsw(lds + L_QX + ((2 * f + 1) * 2 + tb) * 1024 + lane * 16, q1);
            lptr rp = lds + L_AT + tbo * HTS + (8 * fg + 2 * wi) * HFS + h * HHS + n * 16;
            xr0 = ldsr(rp); xr1 = ldsr(rp + HFS);
        }
    }

    for (int p = 0; p < 2; ++p) {
        u32x4 Wj[8];
        {
            FRESH();
            if (w == 0) {
                const char* fp = ws + WS_KAF + p * 16384 + lane * 128;
                u32x2 a, c;
                _Pragma("clang loop unroll(disable)") for (unsigned sp = 0; sp < SPIN_CAP; ++sp) { a = poll8(fp); c = poll8(fp + 8192); if (__all(a[1] == tag && c[1] == tag)) break; __builtin_amdgcn_s_sleep(2); }
                const float km = maxx32(max32(fmaxf(__uint_as_float(a[0]), __uint_as_float(c[0]))));
                if (lane == 0) *(LAS float*)(lds + L_KMA) = km;
            }
            BAR();
            const int kv = WS_KVA + p * 131072;
            {
                u32x4 a, c, d, e; const int off = (w >> 2) * 65536 + wi * 4096;
                gld4(a, c, d, e, lane * 16, rd, kv + off); wait4<0>(a, c, d, e);
                lptr dst = lds + off + lane * 16; ldsw(dst, a); ldsw(dst + 1024, c); ldsw(dst + 2048, d); ldsw(dst + 3072, e);
            }
            AttState s;
            s.q = ldsr(lds + L_QX + w * 1024 + lane * 16);
            const float m = __builtin_amdgcn_sqrtf(addx32(sq4(s.q, 0.f)) * *(LAS float*)(lds + L_KMA)) * 1.002f;
            const bool exact = !__all(m <= MBOUND);
            s.c = splat16(-m); s.o = splat16(zero_); s.a = s.o; s.b = s.o; s.k = ONES; s.v0 = ONES; s.v1 = ONES;
            s.ka = lds0 + L_KA + lane * 16; s.va = lds0 + (n < 16 ? L_VA + h * 256 + n * 16 : L_CONST + h * 256 + (n - 16) * 16);
            s.vs = n < 16 ? 4096u : 0u; s.idx = 0xDDDDu; asm volatile("" : "+v"(s.idx));
            BAR();
            if constexpr (g0) {
#pragma unroll
                for (int j = 0; j < 6; ++j) { const int off = (j / 3) * 65536 + 16384 + (j % 3) * 16384 + wi * 4096; dma4s(lane * 16, rd, kv + off, lds0 + off); }
                unsigned fw = SENT; asm volatile("" : "+v"(fw)); const u32x4 F = {fw, fw, fw, fw}; const int u = tid * 16;
#pragma unroll
                for (int j = 0; j < 4; ++j) st16(rw, WS_O + (2 * p + (j >> 1)) * 2097152 + (rb * 64 + (j & 1) * 32 + 4 * cg) * 1024 + u, F);
#pragma unroll
                for (int j = 0; j < 4; ++j) st16(rw, WS_KVB + p * 4194304 + (rb * 8 + cg) * 16384 + j * 4096 + u, F);
#pragma unroll
                for (int j = 0; j < 2; ++j) st16(rw, WS_X + 2 * p * 2097152 + (rb * 64 + tbo * 32 + 8 * fg) * 1024 + j * 4096 + u, F);
                if (p == 0) {
#pragma unroll
                    for (int j = 0; j < 2; ++j) st16(rw, WS_X + 2097152 + (rb * 64 + tbo * 32 + 8 * fg) * 1024 + j * 4096 + u, F);
                }
                if (w < 2 - p) st16(rw, WS_ST + (2 * p + w) * 262144 + rb * 8192 + tbo * 4096 + fg * 1024 + lane * 16, F);
            }
            if (!exact) { att_begin(s); att_run(s, 3); }
            VM0();
            BAR();
            if (tid == 0) st8(rw, WS_RDY + p * 32768 + b * 128, (u32x2){tag, tag});
            if (exact) {
                float mx = NEG_BIG;
                for (int t = 0; t < 64; ++t) mx = rowmax16(mfma(ldsr(lds + L_KA + t * 1024 + lane * 16), s.q, ZERO), mx);
                s.c = splat16(-maxx32(mx));
                att_begin(s); att_run(s, 3);
            }
            att_run(s, 7);
            {
                const char* fp = ws + WS_RDY + p * 32768 + lane * 128;
                _Pragma("clang loop unroll(disable)") for (unsigned sp = 0; sp < SPIN_CAP; ++sp) {
                    const u32x2 a = poll8(fp), c = poll8(fp + 8192), d = poll8(fp + 16384), e = poll8(fp + 24576);
                    if (__all(a[1] == tag && c[1] == tag && d[1] == tag && e[1] == tag)) break;
                    __builtin_amdgcn_s_sleep(4);
                }
                asm volatile("" ::: "memory");
                if (p == 0 && b == 0 && tid == 0) __hip_atomic_store((GAS unsigned*)(ws + WS_E), tag, __ATOMIC_RELAXED, __HIP_MEMORY_SCOPE_AGENT);
                const int wo = WS_WT + (g0 ? WT_KVB + (cg * 4 + wi) * 32 : WT_WOA + (4 * fg + wi) * 32) * 1024 + lane * 16;
#pragma unroll
                for (int i = 0; i < 32; ++i) W[i] = ld16(rw, wo + i * 1024);
            }
            att_run(s, 6);
            att_end(s);
            st16(rw, WS_O + 2 * p * 2097152 + (rb * 64 + (w & 1) * 32 + 4 * cg + (w >> 1)) * 1024 + lane * 16, norm_pack(s.o));
            BAR();
        }
#define OUT_LOAD(OBUF) do { \
            VM0(); \
            const int src = WS_O + (OBUF) * 2097152 + (rb * 64 + tbo * 32 + 2 * wi) * 1024; lptr dst = lds + L_AT + 2 * wi * 1024 + lane * 16; \
            u32x4 r[8]; \
            _Pragma("unroll") for (int c = 0; c < 4; ++c) gld2(r[2 * c], r[2 * c + 1], lane * 16, rd, src + c * 8192); \
            stage2<6>(r[0], r[1], lane * 16, rd, src, dst); BAR(); \
            stage2<4>(r[2], r[3], lane * 16, rd, src + 8192, dst + 8192); BAR(); \
            stage2<2>(r[4], r[5], lane * 16, rd, src + 16384, dst + 16384); BAR(); \
            stage2<0>(r[6], r[7], lane * 16, rd, src + 24576, dst + 24576); BAR(); } while (0)
#define OUT_MMA(PK) do { \
            f32x16 acc; \
            _Pragma("unroll") for (int j = 0; j < 4; ++j) { acc[2 * j] = bflo(xr0[j]); acc[2 * j + 1] = bfhi(xr0[j]); acc[8 + 2 * j] = bflo(xr1[j]); acc[9 + 2 * j] = bfhi(xr1[j]); } \
            _Pragma("unroll") for (int c = 0; c < 4; ++c) { BAR(); \
                _Pragma("unroll") for (int k = 0; k < 8; ++k) acc = mfma(W[8 * c + k], ldsr(lds + L_AT + (8 * c + k) * 1024 + lane * 16), acc); } \
            _Pragma("unroll") for (int j = 0; j < 8; ++j) PK[j] = pks(acc[2 * j], acc[2 * j + 1]); } while (0)
#define OUT_PUBLISH(PK, XBUF) do { \
            const u32x4 f0 = {PK[0], PK[1], PK[2], PK[3]}, f1 = {PK[4], PK[5], PK[6], PK[7]}; \
            const int xo = WS_X + (XBUF) * 2097152 + (rb * 64 + tbo * 32 + 8 * fg + 2 * wi) * 1024 + lane * 16; \
            st16(rw, xo, f0); st16(rw, xo + 1024, f1); \
            const float s1 = addx32(sm4(f1, sm4(f0, 0.f))), s2 = addx32(sq4(f1, sq4(f0, 0.f))); \
            if (lane < 32) st8(rw, WS_ST + (XBUF) * 262144 + rb * 8192 + tbo * 4096 + (4 * fg + wi) * 256 + lane * 8, (u32x2){min(__float_as_uint(s1), SENT - 1u), min(__float_as_uint(s2), SENT - 1u)}); } while (0)

        if constexpr (g0) {
            FRESH();
            OUT_LOAD(2 * p);
            BAR();
            f32x16 cs = ZERO, a0 = ZERO, a1 = ZERO;
            lptr ap = lds + L_AT + lane * 16;
            const int kvo = WS_KVB + p * 4194304 + (rb * 8 + cg) * 16384;
            if (wi < 2) {
#pragma unroll
                for (int ks = 0; ks < 32; ++ks) cs = mfma(W[ks], ONES, cs);
#pragma unroll
                for (int c = 0; c < 4; ++c) { BAR();
#pragma unroll
                    for (int k = 0; k < 8; ++k) { a0 = mfma(W[8 * c + k], ldsr(ap + (8 * c + k) * 1024), a0); a1 = mfma(W[8 * c + k], ldsr(ap + 32768 + (8 * c + k) * 1024), a1); } }
                asm volatile("" : "+{v[168:183]}"(cs), "+{v[184:199]}"(a0), "+{v[200:215]}"(a1));
                float k0 = 0.f, k1 = 0.f;
#pragma unroll
                for (int tb = 0; tb < 2; ++tb) {
                    const float mean = *(LAS float*)(lds + L_MEAN + (tb * 32 + n) * 4), rstd = *(LAS float*)(lds + L_RSTD + (tb * 32 + n) * 4);
                    u32x4 f0, f1; fold_t(tb ? a1 : a0, cs, mean, rstd, f0, f1);
                    const int o = (tb * 4 + 2 * wi) * 2048 + h * 512 + slot_of(n) * 16;
                    st16(rw, kvo + o, f0); st16(rw, kvo + o + 2048, f1);
                    ldsw(lds + L_KVB + 65536 + o, f0); ldsw(lds + L_KVB + 65536 + o + 2048, f1);
                    k0 = fmaxf(k0, addx32(sq4(f0, 0.f))); k1 = fmaxf(k1, addx32(sq4(f1, 0.f)));
                }
                k0 = max32(k0); k1 = max32(k1);
                if (lane < 2) { const float km = lane ? k1 : k0; st8(rw, WS_KBF + p * 32768 + (rb * 8 + cg) * 128 + (2 * wi + lane) * 8, (u32x2){__float_as_uint(km), tag});
                    *(LAS float*)(lds + L_KMOWN + (2 * wi + lane) * 4) = km; }
            } else {
#pragma unroll
                for (int ks = 0; ks < 32; ++ks) cs = mfma(ONES, W[ks], cs);
#pragma unroll
                for (int c = 0; c < 4; ++c) { BAR();
#pragma unroll
                    for (int k = 0; k < 8; ++k) { a0 = mfma(ldsr(ap + (8 * c + k) * 1024), W[8 * c + k], a0); a1 = mfma(ldsr(ap + 32768 + (8 * c + k) * 1024), W[8 * c + k], a1); } }
                asm volatile("" : "+{v[168:183]}"(cs), "+{v[184:199]}"(a0), "+{v[200:215]}"(a1));
#pragma unroll
                for (int tb = 0; tb < 2; ++tb) {
                    float y[16]; fold_n(tb ? a1 : a0, cs[0], lds, tb, h, y);
                    const int o = (tb * 4 + 2 * (wi - 2) + (n >> 4)) * 2048 + 1024 + h * 256 + (n & 15) * 16;
                    const u32x4 f0 = {pks(y[0], y[1]), pks(y[2], y[3]), pks(y[4], y[5]), pks(y[6], y[7])}, f1 = {pks(y[8], y[9]), pks(y[10], y[11]), pks(y[12], y[13]), pks(y[14], y[15])};
                    st16(rw, kvo + o, f0); st16(rw, kvo + o + 512, f1);
                    ldsw(lds + L_KVB + 65536 + o, f0); ldsw(lds + L_KVB + 65536 + o + 512, f1);
                }
            }
            xr0 = ldsr(ap + (tbo * 32 + 8 * fg + 2 * wi) * 1024); xr1 = ldsr(ap + (tbo * 32 + 8 * fg + 2 * wi + 1) * 1024);
            VM0();
            {
                const int nb = rb < 4 ? rb : 4;
                const int src = WS_KVB + p * 4194304 + ((rb - 4) * 8 + cg) * 16384 + wi * 4096; lptr dst = lds + L_KVB + wi * 4096 + lane * 16;
                u32x4 r[16];
                if (nb == 4) {
#pragma unroll
                    for (int c = 0; c < 4; ++c) gld4(r[4 * c], r[4 * c + 1], r[4 * c + 2], r[4 * c + 3], lane * 16, rd, src + c * 131072);
                    stage4<12>(r[0], r[1], r[2], r[3], lane * 16, rd, src, dst);
                    stage4<8>(r[4], r[5], r[6], r[7], lane * 16, rd, src + 131072, dst + 16384);
                    stage4<4>(r[8], r[9], r[10], r[11], lane * 16, rd, src + 262144, dst + 32768);
                    stage4<0>(r[12], r[13], r[14], r[15], lane * 16, rd, src + 393216, dst + 49152);
                } else {
                    for (int c = 4 - nb; c < 4; ++c) { gld4(r[0], r[1], r[2], r[3], lane * 16, rd, src + c * 131072); stage4<0>(r[0], r[1], r[2], r[3], lane * 16, rd, src + c * 131072, dst + c * 16384); }
                }
                const int wo = WS_WT + (WT_WOB + (4 * fg + wi) * 32) * 1024 + lane * 16;
                if (w == 0) {
                    const bool ex = lane < 16 && (lane >> 2) >= 4 - nb;
                    const char* fp = ws + WS_KBF + p * 32768 + (ex ? ((rb - 4 + (lane >> 2)) * 8 + cg) * 128 + (lane & 3) * 8 : (rb * 8 + cg) * 128);
                    u32x2 a;
                    _Pragma("clang loop unroll(disable)") for (unsigned sp = 0; sp < SPIN_CAP; ++sp) { a = poll8(fp); if (__all(!ex || a[1] == tag)) break; __builtin_amdgcn_s_sleep(1); }
                    float km = ex ? __uint_as_float(a[0]) : 0.f;
                    km = fmaxf(km, dpp<0x124>(km)); km = fmaxf(km, dpp<0x128>(km));
                    if (lane < 4) *(LAS float*)(lds + L_KMNB + lane * 4) = km;
                }
#pragma unroll
                for (int i = 0; i < 32; ++i) W[i] = ld16(rw, wo + i * 1024);
            }
        } else {
            FRESH();
            unsigned pk[8];
            OUT_MMA(pk);
            OUT_PUBLISH(pk, 2 * p);
            BAR();
            VM0();
            {
                const int src = WS_X + 2 * p * 2097152 + (rb * 64 + (wi >> 1) * 32 + (wi & 1) * 4) * 1024; lptr dst = lds + L_AT + ((wi >> 1) * 32 + (wi & 1) * 4) * 1024 + lane * 16;
                const int ssrc = WS_ST + 2 * p * 262144 + rb * 8192; const int u = tid - 256; const unsigned svo = (u & 15) * 256 + (u >> 4) * 16;
                u32x4 r[16], sa, sb;
#pragma unroll
                for (int c = 0; c < 4; ++c) gld4(r[4 * c], r[4 * c + 1], r[4 * c + 2], r[4 * c + 3], lane * 16, rd, src + c * 8192);
                gld1(sa, svo, rd, ssrc); gld1(sb, svo, rd, ssrc + 4096);
                stage4<14>(r[0], r[1], r[2], r[3], lane * 16, rd, src, dst); BAR();
                stage4<10>(r[4], r[5], r[6], r[7], lane * 16, rd, src + 8192, dst + 8192); BAR();
                stage4<6>(r[8], r[9], r[10], r[11], lane * 16, rd, src + 16384, dst + 16384); BAR();
                stage4<2>(r[12], r[13], r[14], r[15], lane * 16, rd, src + 24576, dst + 24576);
                stage_stats<0>(sa, sb, svo, rd, ssrc, lds, u); BAR();
            }
            const int f = wi & 1, tb = wi >> 1;
            const int wo = WS_WT + (WT_QB + (2 * cg + f) * 32) * 1024 + lane * 16;
#pragma unroll
            for (int i = 0; i < 32; ++i) W[i] = ld16(rw, wo + i * 1024);
            f32x16 acc = ZERO, cs = ZERO;
#pragma unroll
            for (int ks = 0; ks < 32; ++ks) { acc = mfma(W[ks], ldsr(lds + L_AT + (tb * 32 + ks) * 1024 + lane * 16), acc); cs = mfma(W[ks], ONES, cs); }
            const float mean = *(LAS float*)(lds + L_MEAN + (tb * 32 + n) * 4), rstd = *(LAS float*)(lds + L_RSTD + (tb * 32 + n) * 4);
            u32x4 q0, q1; fold_t(acc, cs, mean, rstd * QSCALE, q0, q1);
            ldsw(lds + L_QX + ((2 * f) * 2 + tb) * 1024 + lane * 16, q0); ldsw(lds + L_QX + ((2 * f + 1) * 2 + tb) * 1024 + lane * 16, q1);
            asm volatile("" ::: "memory");
            if (p == 0) {
                const int wa = WS_WT + (WT_QA + (2 * cg + f) * 32) * 1024 + lane * 16;
#pragma unroll
                for (int i = 0; i < 32; ++i) W[i] = ld16(rw, wa + i * 1024);
            }
        }
        BAR();
        {
            FRESH();
            const int tb = w & 1, hh = w >> 1, kt = 2 * rb + tb, nmid = kt < 7 ? kt : 7;
            const u32x4 q = ldsr(lds + L_QX + w * 1024 + lane * 16);
            const float km = fmaxf(*(LAS float*)(lds + L_KMOWN + hh * 4), *(LAS float*)(lds + L_KMNB + hh * 4));
            float m = __builtin_amdgcn_sqrtf(addx32(sq4(q, 0.f)) * km) * 1.002f;
            lptr kp = lds + L_KVB + (8 + tb) * 8192 + hh * 2048 + lane * 16;
            if (!__all(m <= MBOUND)) {
                f32x16 c; mask_fill<0>(c, 0.f, NEG_BIG);
                float mx = rowmax16(mfma(ldsr(kp), q, c), NEG_BIG);
                for (int t = 1; t <= nmid; ++t) mx = rowmax16(mfma(ldsr(kp - t * 8192), q, ZERO), mx);
                if (kt >= 8) { mask_fill<0>(c, NEG_BIG, 0.f); mx = rowmax16(mfma(ldsr(kp - 65536), q, c), mx); }
                m = maxx32(mx);
            }
            lptr vp = lds + (n < 16 ? L_KVB + (8 + tb) * 8192 + hh * 2048 + 1024 + h * 256 + n * 16 : L_CONST + h * 256 + (n - 16) * 16);
            const int vs = n < 16 ? 8192 : 0;
            f32x16 o = splat16(zero_);
#define BTILE(CC) do { f32x16 sc = mfma(ldsr(kp), q, CC); const u32x4 v0 = ldsr(vp), v1 = ldsr(vp + 512); \
                _Pragma("unroll") for (int i = 0; i < 16; ++i) sc[i] = __builtin_amdgcn_exp2f(sc[i]); \
                o = smf(v0, lo8(sc), o); o = smf(v1, hi8(sc), o); kp -= 8192; vp -= vs; } while (0)
            { f32x16 c; mask_fill<0>(c, -m, NEG_BIG); BTILE(c); }
            { const f32x16 c = splat16(-m); for (int t = 0; t < nmid; ++t) BTILE(c); }
            if (kt >= 8) { f32x16 c; mask_fill<0>(c, NEG_BIG, -m); BTILE(c); }
            st16(rw, WS_O + (2 * p + 1) * 2097152 + (rb * 64 + tb * 32 + 4 * cg + hh) * 1024 + lane * 16, norm_pack(o));
            if (p == 0 && g0 && prod) {
                const int wo = WS_WT + ((isV ? WT_VA : WT_KA) + 4 * wi) * 1024 + lane * 16;
#pragma unroll
                for (int j = 0; j < 8; ++j) Wj[j] = ld16(rw, wo + ((j >> 2) * 16 + (j & 3)) * 1024);
            } else if (g0) {
#pragma unroll
                for (int j = 0; j < 8; ++j) asm volatile("" : "=v"(Wj[j]));
            }
            BAR();
        }
        if constexpr (g0) {
            FRESH();
            unsigned pk[8];
            OUT_MMA(pk);
            if (p == 1) {
                char* dst = out + (size_t)(rb * 64 + tbo * 32 + n) * 1024 + (128 * fg + 32 * wi) * 2 + h * 16;
#pragma unroll
                for (int g = 0; g < 2; ++g) {
                    auto r0 = __builtin_amdgcn_permlane32_swap(pk[4 * g], pk[4 * g + 2], false, false), r1 = __builtin_amdgcn_permlane32_swap(pk[4 * g + 1], pk[4 * g + 3], false, false);
                    *(u32x4*)(dst + g * 32) = (u32x4){r0[0], r1[0], r0[1], r1[1]};
                }
            } else {
                OUT_PUBLISH(pk, 1);
                BAR();
                f32x16 acc = ZERO, cs = ZERO;
#define JOB(J0, J1, DST, XF) do { if (isV) { _Pragma("unroll") for (int j = J0; j < J1; ++j) DST = mfma(XF, Wj[j], DST); } \
                                  else { _Pragma("unroll") for (int j = J0; j < J1; ++j) DST = mfma(Wj[j], XF, DST); } } while (0)
                if (prod) JOB(0, 8, cs, ONES);
                VM0();
                const int s0 = WS_X + 2097152 + (rb * 64 + tbo * 32 + wi * 4) * 1024; const int s1 = WS_X + 2097152 + (rb * 64 + (tbo ^ 1) * 32 + wi * 4) * 1024;
                lptr d0 = lds + L_AT + (tbo * 32 + wi * 4) * 1024 + lane * 16, d1 = lds + L_AT + ((tbo ^ 1) * 32 + wi * 4) * 1024 + lane * 16;
                const int ssrc = WS_ST + 262144 + rb * 8192; const unsigned svo = (tid & 15) * 256 + (tid >> 4) * 16;
                u32x4 r[16], sa, sb;
                gld4(r[0], r[1], r[2], r[3], lane * 16, rd, s0); gld4(r[4], r[5], r[6], r[7], lane * 16, rd, s0 + 16384);
                gld1(sa, svo, rd, ssrc); gld1(sb, svo, rd, ssrc + 4096);
                gld4(r[8], r[9], r[10], r[11], lane * 16, rd, s1); gld4(r[12], r[13], r[14], r[15], lane * 16, rd, s1 + 16384);
                stage4<14>(r[0], r[1], r[2], r[3], lane * 16, rd, s0, d0); BAR();
                if (prod) JOB(0, 4, acc, r[j]);
                stage4<10>(r[4], r[5], r[6], r[7], lane * 16, rd, s0 + 16384, d0 + 16384);
                stage_stats<8>(sa, sb, svo, rd, ssrc, lds, tid); BAR();
                if (prod) {
                    JOB(4, 8, acc, r[j]);
                    kv_partial(acc, cs, isV, tbo, lds, lds + L_KVB + wi * 4096, lane);
                }
                stage4<4>(r[8], r[9], r[10], r[11], lane * 16, rd, s1, d1); BAR();
                stage4<0>(r[12], r[13], r[14], r[15], lane * 16, rd, s1 + 16384, d1 + 16384);
                float km = 0.f;
                if (prod && w == 0) km = kv_final(lds + L_KVB, rw, WS_KVA + 131072 + (isV ? 65536 : 0) + (rb * 2 + tbo) * 1024, lane);
                BAR();
                if (prod && w == 0) { VM0(); if (lane == 0) st8(rw, WS_KAF + 16384 + (rb * 4 + cg) * 128, (u32x2){isV ? 0u : __float_as_uint(km), tag}); }
            }
        } else {
            FRESH();
            OUT_LOAD(2 * p + 1);
            if (p == 0) {
                BAR();
                const int f = wi & 1, tb = wi >> 1;
                f32x16 acc = ZERO, cs = ZERO;
                lptr ap = lds + L_AT + tb * 32768 + lane * 16;
#pragma unroll
                for (int ks = 0; ks < 32; ++ks) cs = mfma(W[ks], ONES, cs);
                if (tb != tbo) { BAR(); BAR(); }
                BAR();
#pragma unroll
                for (int ks = 0; ks < 16; ++ks) acc = mfma(W[ks], ldsr(ap + ks * 1024), acc);
                BAR();
#pragma unroll
                for (int ks = 16; ks < 32; ++ks) acc = mfma(W[ks], ldsr(ap + ks * 1024), acc);
                const float mean = *(LAS float*)(lds + L_MEAN + (tb * 32 + n) * 4), rstd = *(LAS float*)(lds + L_RSTD + (tb * 32 + n) * 4);
                u32x4 q0, q1; fold_t(acc, cs, mean, rstd * QSCALE, q0, q1);
                ldsw(lds + L_QX + ((2 * f) * 2 + tb) * 1024 + lane * 16, q0); ldsw(lds + L_QX + ((2 * f + 1) * 2 + tb) * 1024 + lane * 16, q1);
                if (tb == tbo) { BAR(); BAR(); }
                xr0 = ldsr(lds + L_AT + (tbo * 32 + 8 * fg + 2 * wi) * 1024 + lane * 16); xr1 = ldsr(lds + L_AT + (tbo * 32 + 8 * fg + 2 * wi + 1) * 1024 + lane * 16);
            }
        }
    }
}

__global__ void __launch_bounds__(512, 2) il_fwd(const char* __restrict__ x0, const char* __restrict__ wq, const char* __restrict__ wk, const char* __restrict__ wv,
                                                 const char* __restrict__ woa, const char* __restrict__ wqkv, const char* __restrict__ wob, char* __restrict__ out, char* ws) {
    extern __shared__ __attribute__((aligned(16))) char smem[];
    const int w = __builtin_amdgcn_readfirstlane(threadIdx.x >> 6);
    if (w < 4) program<true>(x0, wq, wk, wv, woa, wqkv, wob, out, ws, (lptr)smem, w);
    else program<false>(x0, wq, wk, wv, woa, wqkv, wob, out, ws, (lptr)smem, w);
}

extern "C" void kernel_launch(void* const* d_in, const int* in_sizes, int n_in, void* d_out, int out_size, void* d_ws, size_t ws_size, hipStream_t stream) {
    (void)in_sizes; (void)n_in; (void)out_size; (void)ws_size;
    (void)hipFuncSetAttribute((const void*)il_fwd, hipFuncAttributeMaxDynamicSharedMemorySize, LDS_BYTES);
    hipLaunchKernelGGL(il_fwd, dim3(256), dim3(512), LDS_BYTES, stream, (const char*)d_in[0], (const char*)d_in[1], (const char*)d_in[2], (const char*)d_in[3], (const char*)d_in[4],
                       (const char*)d_in[6], (const char*)d_in[7], (char*)d_out, (char*)d_ws);
}
```

```cpp
#include <hip/hip_runtime.h>
#include <hip/hip_bf16.h>
#include <stdint.h>

typedef float f32x16 __attribute__((ext_vector_type(16)));
typedef float f32x8 __attribute__((ext_vector_type(8)));
typedef float f32x4 __attribute__((ext_vector_type(4)));
typedef float f32x2 __attribute__((ext_vector_type(2)));
typedef unsigned u32x4 __attribute__((ext_vector_type(4)));
typedef unsigned u32x2 __attribute__((ext_vector_type(2)));
typedef short s16x4 __attribute__((ext_vector_type(4)));
typedef __bf16 bf16x2 __attribute__((ext_vector_type(2)));
typedef __bf16 bf16x8 __attribute__((ext_vector_type(8)));
typedef __bf16 bf16x16 __attribute__((ext_vector_type(16)));
#define LAS __attribute__((address_space(3)))
#define GAS __attribute__((address_space(1)))
#define DEV __device__ __forceinline__
typedef LAS char* lptr;

constexpr int WS_E = 0, WS_RDY = 4096, WS_KAF = WS_RDY + 65536, WS_KBF = WS_KAF + 32768, WS_WT = 262144;
constexpr int WT_WOA = 0, WT_KVB = 512, WT_QB = 1536, WT_WOB = 2048, WT_QA = 2560, WT_KA = 3072, WT_VA = 3104, WT_N = 3136;
constexpr int WS_KVA = WS_WT + WT_N * 1024, WS_O = WS_KVA + 262144, WS_X = WS_O + 4 * 2097152, WS_ST = WS_X + 3 * 2097152;
constexpr int WS_KVB = WS_ST + 3 * 262144, WS_END = WS_KVB + 2 * 4194304;
constexpr int L_AT = 0, L_KA = 0, L_VA = 65536, L_KVB = 65536, L_WQ = 67584, L_WK = 133120;
constexpr int L_CONST = 149504, L_QX = 153600, L_MEAN = 161792, L_RSTD = 162048, L_KMA = 162304, L_KMOWN = 162320, L_KMNB = 162336, L_CNT = 162352, L_DUMMY = 162368;
constexpr int LDS_BYTES = 163840;
constexpr int HFS = 1056, HHS = 528, HTS = 32 * HFS;
constexpr unsigned SENT = 0xFFFFFFFFu, SPIN_CAP = 1u << 15;
constexpr float QSCALE = 0.25f * 1.4426950408889634f, LN_EPS = 1e-5f, MBOUND = 50.f, NEG_BIG = -1e30f;

#define STAMP(i) do {} while (0)
#define STAMPC(i) do {} while (0)
#define STAMPV(i, v) do {} while (0)
#define STAMP0(i) do {} while (0)
#define NRETRY
#define NRETRYP
#define NINC
#define BAR() asm volatile("s_waitcnt lgkmcnt(0)\n\ts_barrier" ::: "memory")
#define VM0() asm volatile("s_waitcnt vmcnt(0)" ::: "memory")
DEV u32x4 ldsr(lptr p) { return *(const LAS u32x4*)p; }
DEV void ldsw(lptr p, u32x4 v) { *(LAS u32x4*)p = v; }
DEV f32x4 ldsrf(lptr p) { return *(const LAS f32x4*)p; }
DEV f32x16 mfma(u32x4 a, u32x4 b, f32x16 c) { return __builtin_amdgcn_mfma_f32_32x32x16_bf16(__builtin_bit_cast(bf16x8, a), __builtin_bit_cast(bf16x8, b), c, 0, 0, 0); }
DEV f32x16 smf(u32x4 a, f32x8 b, f32x16 c) { return __builtin_amdgcn_smfmac_f32_32x32x32_bf16(__builtin_bit_cast(bf16x8, a), __builtin_bit_cast(bf16x16, b), c, 0xDDDD, 0, 0); }
DEV f32x8 lo8(f32x16 v) { return __builtin_shufflevector(v, v, 0, 1, 2, 3, 4, 5, 6, 7); }
DEV f32x8 hi8(f32x16 v) { return __builtin_shufflevector(v, v, 8, 9, 10, 11, 12, 13, 14, 15); }
DEV unsigned pk2(float a, float b) { f32x2 f = {a, b}; return __builtin_bit_cast(unsigned, __builtin_convertvector(f, bf16x2)); }
DEV unsigned pks(float a, float b) { return min(pk2(a, b), SENT - 1u); }
DEV float bflo(unsigned u) { return __builtin_bit_cast(float, u << 16); }
DEV float bfhi(unsigned u) { return __builtin_bit_cast(float, u & 0xFFFF0000u); }
DEV float dot2(unsigned a, unsigned b, float c) { return __builtin_amdgcn_fdot2_f32_bf16(__builtin_bit_cast(bf16x2, a), __builtin_bit_cast(bf16x2, b), c, false); }
DEV float sq4(u32x4 a, float c) { return dot2(a[3], a[3], dot2(a[2], a[2], dot2(a[1], a[1], dot2(a[0], a[0], c)))); }
DEV float sm4(u32x4 a, float c) { const unsigned o = 0x3F803F80u; return dot2(a[3], o, dot2(a[2], o, dot2(a[1], o, dot2(a[0], o, c)))); }
template <int CTRL> DEV float dpp(float x) { return __builtin_bit_cast(float, __builtin_amdgcn_update_dpp(0, __builtin_bit_cast(int, x), CTRL, 0xF, 0xF, true)); }
DEV float sum8(float x) { x += dpp<0xB1>(x); x += dpp<0x4E>(x); x += dpp<0x141>(x); return x; }
DEV float sum16(float x) { x = sum8(x); x += dpp<0x140>(x); return x; }
DEV float max16(float x) { x = fmaxf(x, dpp<0xB1>(x)); x = fmaxf(x, dpp<0x4E>(x)); x = fmaxf(x, dpp<0x141>(x)); x = fmaxf(x, dpp<0x140>(x)); return x; }
DEV float max32(float x) { x = max16(x); auto r = __builtin_amdgcn_permlane16_swap(__float_as_uint(x), __float_as_uint(x), false, false); return fmaxf(__uint_as_float(r[0]), __uint_as_float(r[1])); }
DEV float addx32(float x) { auto r = __builtin_amdgcn_permlane32_swap(__float_as_uint(x), __float_as_uint(x), false, false); return __uint_as_float(r[0]) + __uint_as_float(r[1]); }
DEV float maxx32(float x) { auto r = __builtin_amdgcn_permlane32_swap(__float_as_uint(x), __float_as_uint(x), false, false); return fmaxf(__uint_as_float(r[0]), __uint_as_float(r[1])); }
DEV unsigned mx4(u32x4 a) { return max(max(a[0], a[1]), max(a[2], a[3])); }
DEV int slot_of(int t) { return (t & 0x13) | ((t & 4) << 1) | ((t & 8) >> 1); }
DEV u32x4 trfrag(lptr p, int second) {
    s16x4 a = __builtin_amdgcn_ds_read_tr16_b64_v4i16((LAS s16x4*)p), b = __builtin_amdgcn_ds_read_tr16_b64_v4i16((LAS s16x4*)(p + second));
    u32x2 x = __builtin_bit_cast(u32x2, a), y = __builtin_bit_cast(u32x2, b);
    return (u32x4){x[0], x[1], y[0], y[1]};
}
DEV int only(bool c, int off) { return c ? off : 0x7FFFF000; }
DEV void ldsw1(lptr lds, bool c, int off, float v) { *(LAS float*)(lds + (c ? off : L_DUMMY)) = v; }
typedef __amdgpu_buffer_rsrc_t rsrc_t;
DEV void st16(rsrc_t r, int off, u32x4 v) { __builtin_amdgcn_raw_buffer_store_b128(v, r, off, 0, 16); }
DEV void st8(rsrc_t r, int off, u32x2 v) { __builtin_amdgcn_raw_buffer_store_b64(v, r, off, 0, 16); }
DEV u32x4 ld16(rsrc_t r, int off) { return __builtin_amdgcn_raw_buffer_load_b128(r, off, 0, 16); }
DEV u32x2 poll8(const char* p) { unsigned long long x = __hip_atomic_load((GAS unsigned long long*)p, __ATOMIC_RELAXED, __HIP_MEMORY_SCOPE_AGENT); return (u32x2){(unsigned)x, (unsigned)(x >> 32)}; }
#define UNI(x) __builtin_amdgcn_readfirstlane(x)
DEV void gld1(u32x4& a, unsigned vo, u32x4 rd, int so) { asm volatile("s_nop 4\n\tbuffer_load_dwordx4 %0, %1, %2, %3 offen sc1" : "=&v"(a) : "v"(vo), "s"(rd), "s"(UNI(so)) : "memory"); }
DEV void gld2(u32x4& a, u32x4& b, unsigned vo, u32x4 rd, int so) {
    asm volatile("s_nop 4\n\tbuffer_load_dwordx4 %0, %2, %3, %4 offen sc1\n\tbuffer_load_dwordx4 %1, %2, %3, %4 offen offset:1024 sc1" : "=&v"(a), "=&v"(b) : "v"(vo), "s"(rd), "s"(UNI(so)) : "memory"); }
DEV void gld4(u32x4& a, u32x4& b, u32x4& c, u32x4& d, unsigned vo, u32x4 rd, int so) {
    asm volatile("s_nop 4\n\tbuffer_load_dwordx4 %0, %4, %5, %6 offen sc1\n\tbuffer_load_dwordx4 %1, %4, %5, %6 offen offset:1024 sc1\n\t"
                 "buffer_load_dwordx4 %2, %4, %5, %6 offen offset:2048 sc1\n\tbuffer_load_dwordx4 %3, %4, %5, %6 offen offset:3072 sc1"
                 : "=&v"(a), "=&v"(b), "=&v"(c), "=&v"(d) : "v"(vo), "s"(rd), "s"(UNI(so)) : "memory"); }
DEV const char* uptr(const char* p) { const size_t a = (size_t)p; return (const char*)(((size_t)(unsigned)UNI((int)(a >> 32)) << 32) | (unsigned)UNI((int)a)); }
template <int N> DEV void wait2(u32x4& a, u32x4& b) { asm volatile("s_waitcnt vmcnt(%2)" : "+v"(a), "+v"(b) : "i"(N) : "memory"); }
template <int N> DEV void wait4(u32x4& a, u32x4& b, u32x4& c, u32x4& d) { asm volatile("s_waitcnt vmcnt(%4)" : "+v"(a), "+v"(b), "+v"(c), "+v"(d) : "i"(N) : "memory"); }
DEV void dma1(unsigned vo, const char* sb, unsigned ldsb) {
    unsigned keep;
    asm volatile("s_mov_b32 %0, m0\n\ts_mov_b32 m0, %3\n\ts_nop 3\n\tglobal_load_lds_dwordx4 %1, %2\n\ts_mov_b32 m0, %0" : "=&s"(keep) : "v"(vo), "s"(uptr(sb)), "s"(UNI(ldsb)) : "memory"); }
DEV void dma4s(unsigned vo, u32x4 rd, int so, unsigned ldsb) {
    unsigned keep;
    asm volatile("s_mov_b32 %0, m0\n\ts_mov_b32 m0, %4\n\ts_nop 4\n\t"
                 "buffer_load_dwordx4 %1, %2, %3 offen sc1 lds\n\tbuffer_load_dwordx4 %1, %2, %3 offen offset:1024 sc1 lds\n\t"
                 "buffer_load_dwordx4 %1, %2, %3 offen offset:2048 sc1 lds\n\tbuffer_load_dwordx4 %1, %2, %3 offen offset:3072 sc1 lds\n\ts_mov_b32 m0, %0"
                 : "=&s"(keep) : "v"(vo), "s"(rd), "s"(UNI(so)), "s"(UNI(ldsb)) : "memory"); }

template <int N> DEV void stage2(u32x4& a, u32x4& b, unsigned vo, u32x4 rd, int sb, lptr dst NRETRYP) {
    wait2<N>(a, b);
    _Pragma("clang loop unroll(disable)") for (unsigned sp = 0; !__all(max(mx4(a), mx4(b)) != SENT) && sp < SPIN_CAP; ++sp) { NINC gld2(a, b, vo, rd, sb); wait2<0>(a, b); }
    ldsw(dst, a); ldsw(dst + 1024, b);
}
template <int N> DEV void stage4(u32x4& a, u32x4& b, u32x4& c, u32x4& d, unsigned vo, u32x4 rd, int sb, lptr dst NRETRYP) {
    wait4<N>(a, b, c, d);
    _Pragma("clang loop unroll(disable)") for (unsigned sp = 0; !__all(max(max(mx4(a), mx4(b)), max(mx4(c), mx4(d))) != SENT) && sp < SPIN_CAP; ++sp) { NINC gld4(a, b, c, d, vo, rd, sb); wait4<0>(a, b, c, d); }
    ldsw(dst, a); ldsw(dst + 1024, b); ldsw(dst + 2048, c); ldsw(dst + 3072, d);
}
template <int N> DEV void stage_stats(u32x4& a, u32x4& b, unsigned vo, u32x4 rd, int sb, lptr lds, int u) {
    wait2<N>(a, b);
    _Pragma("clang loop unroll(disable)") for (unsigned sp = 0; !__all(max(mx4(a), mx4(b)) != SENT) && sp < SPIN_CAP; ++sp) { gld1(a, vo, rd, sb); gld1(b, vo, rd, sb + 4096); wait2<0>(a, b); }
#pragma unroll
    for (int tb = 0; tb < 2; ++tb) {
        const u32x4 v = tb ? b : a;
        const float s0 = sum16(__uint_as_float(v[0])), q0 = sum16(__uint_as_float(v[1])), s1 = sum16(__uint_as_float(v[2])), q1 = sum16(__uint_as_float(v[3]));
        const float s = (u & 1) ? s1 : s0, q = (u & 1) ? q1 : q0;
        const float mean = s * (1.f / 512.f), var = fmaxf(q * (1.f / 512.f) - mean * mean, 0.f);
        const int tok = tb * 32 + 2 * (u >> 4) + (u & 1);
        *(LAS float*)(lds + L_MEAN + tok * 4) = mean; *(LAS float*)(lds + L_RSTD + tok * 4) = __builtin_amdgcn_rsqf(var + LN_EPS);
    }
}

#define GS_B8_0 \
    "ds_read_b128 %[t0], %[a] offset:%c[s]*0\n ds_read_b128 %[t1], %[a] offset:%c[s]*1\n ds_read_b128 %[t2], %[a] offset:%c[s]*2\n" \
    "ds_read_b128 %[t3], %[a] offset:%c[s]*3\n ds_read_b128 %[t4], %[a] offset:%c[s]*4\n ds_read_b128 %[t5], %[a] offset:%c[s]*5\n" \
    "s_waitcnt lgkmcnt(5)\n v_mfma_f32_32x32x16_bf16 %[c], %[w0], %[t0], %[c]\n ds_read_b128 %[t0], %[a] offset:%c[s]*6\n" \
    "s_waitcnt lgkmcnt(5)\n v_mfma_f32_32x32x16_bf16 %[c], %[w1], %[t1], %[c]\n ds_read_b128 %[t1], %[a] offset:%c[s]*7\n" \
    "s_waitcnt lgkmcnt(5)\n v_mfma_f32_32x32x16_bf16 %[c], %[w2], %[t2], %[c]\n s_barrier\n" \
    "ds_read_b128 %[t2], %[a] offset:%c[s]*8\n s_waitcnt lgkmcnt(5)\n v_mfma_f32_32x32x16_bf16 %[c], %[w3], %[t3], %[c]\n" \
    "ds_read_b128 %[t3], %[a] offset:%c[s]*9\n s_waitcnt lgkmcnt(5)\n v_mfma_f32_32x32x16_bf16 %[c], %[w4], %[t4], %[c]\n" \
    "ds_read_b128 %[t4], %[a] offset:%c[s]*10\n s_waitcnt lgkmcnt(5)\n v_mfma_f32_32x32x16_bf16 %[c], %[w5], %[t5], %[c]\n" \
    "ds_read_b128 %[t5], %[a] offset:%c[s]*11\n s_waitcnt lgkmcnt(5)\n v_mfma_f32_32x32x16_bf16 %[c], %[w6], %[t0], %[c]\n" \
    "ds_read_b128 %[t0], %[a] offset:%c[s]*12\n s_waitcnt lgkmcnt(5)\n v_mfma_f32_32x32x16_bf16 %[c], %[w7], %[t1], %[c]\n" \
    "ds_read_b128 %[t1], %[a] offset:%c[s]*13\n" \
    ""
#define GS_B8_1 \
    "s_waitcnt lgkmcnt(5)\n v_mfma_f32_32x32x16_bf16 %[c], %[w0], %[t2], %[c]\n ds_read_b128 %[t2], %[a] offset:%c[s]*14\n" \
    "s_waitcnt lgkmcnt(5)\n v_mfma_f32_32x32x16_bf16 %[c], %[w1], %[t3], %[c]\n ds_read_b128 %[t3], %[a] offset:%c[s]*15\n" \
    "s_waitcnt lgkmcnt(5)\n v_mfma_f32_32x32x16_bf16 %[c], %[w2], %[t4], %[c]\n s_barrier\n" \
    "ds_read_b128 %[t4], %[a] offset:%c[s]*16\n s_waitcnt lgkmcnt(5)\n v_mfma_f32_32x32x16_bf16 %[c], %[w3], %[t5], %[c]\n" \
    "ds_read_b128 %[t5], %[a] offset:%c[s]*17\n s_waitcnt lgkmcnt(5)\n v_mfma_f32_32x32x16_bf16 %[c], %[w4], %[t0], %[c]\n" \
    "ds_read_b128 %[t0], %[a] offset:%c[s]*18\n s_waitcnt lgkmcnt(5)\n v_mfma_f32_32x32x16_bf16 %[c], %[w5], %[t1], %[c]\n" \
    "ds_read_b128 %[t1], %[a] offset:%c[s]*19\n s_waitcnt lgkmcnt(5)\n v_mfma_f32_32x32x16_bf16 %[c], %[w6], %[t2], %[c]\n" \
    "ds_read_b128 %[t2], %[a] offset:%c[s]*20\n s_waitcnt lgkmcnt(5)\n v_mfma_f32_32x32x16_bf16 %[c], %[w7], %[t3], %[c]\n" \
    "ds_read_b128 %[t3], %[a] offset:%c[s]*21\n" \
    ""
#define GS_B8_2 \
    "s_waitcnt lgkmcnt(5)\n v_mfma_f32_32x32x16_bf16 %[c], %[w0], %[t4], %[c]\n ds_read_b128 %[t4], %[a] offset:%c[s]*22\n" \
    "s_waitcnt lgkmcnt(5)\n v_mfma_f32_32x32x16_bf16 %[c], %[w1], %[t5], %[c]\n ds_read_b128 %[t5], %[a] offset:%c[s]*23\n" \
    "s_waitcnt lgkmcnt(5)\n v_mfma_f32_32x32x16_bf16 %[c], %[w2], %[t0], %[c]\n s_barrier\n" \
    "ds_read_b128 %[t0], %[a] offset:%c[s]*24\n s_waitcnt lgkmcnt(5)\n v_mfma_f32_32x32x16_bf16 %[c], %[w3], %[t1], %[c]\n" \
    "ds_read_b128 %[t1], %[a] offset:%c[s]*25\n s_waitcnt lgkmcnt(5)\n v_mfma_f32_32x32x16_bf16 %[c], %[w4], %[t2], %[c]\n" \
    "ds_read_b128 %[t2], %[a] offset:%c[s]*26\n s_waitcnt lgkmcnt(5)\n v_mfma_f32_32x32x16_bf16 %[c], %[w5], %[t3], %[c]\n" \
    "ds_read_b128 %[t3], %[a] offset:%c[s]*27\n s_waitcnt lgkmcnt(5)\n v_mfma_f32_32x32x16_bf16 %[c], %[w6], %[t4], %[c]\n" \
    "ds_read_b128 %[t4], %[a] offset:%c[s]*28\n s_waitcnt lgkmcnt(5)\n v_mfma_f32_32x32x16_bf16 %[c], %[w7], %[t5], %[c]\n" \
    "ds_read_b128 %[t5], %[a] offset:%c[s]*29\n" \
    ""
#define GS_B8_3 \
    "s_waitcnt lgkmcnt(5)\n v_mfma_f32_32x32x16_bf16 %[c], %[w0], %[t0], %[c]\n ds_read_b128 %[t0], %[a] offset:%c[s]*30\n" \
    "s_waitcnt lgkmcnt(5)\n v_mfma_f32_32x32x16_bf16 %[c], %[w1], %[t1], %[c]\n ds_read_b128 %[t1], %[a] offset:%c[s]*31\n" \
    "s_waitcnt lgkmcnt(5)\n v_mfma_f32_32x32x16_bf16 %[c], %[w2], %[t2], %[c]\n s_waitcnt lgkmcnt(4)\n" \
    "v_mfma_f32_32x32x16_bf16 %[c], %[w3], %[t3], %[c]\n s_waitcnt lgkmcnt(3)\n v_mfma_f32_32x32x16_bf16 %[c], %[w4], %[t4], %[c]\n" \
    "s_waitcnt lgkmcnt(2)\n v_mfma_f32_32x32x16_bf16 %[c], %[w5], %[t5], %[c]\n s_waitcnt lgkmcnt(1)\n" \
    "v_mfma_f32_32x32x16_bf16 %[c], %[w6], %[t0], %[c]\n s_waitcnt lgkmcnt(0)\n v_mfma_f32_32x32x16_bf16 %[c], %[w7], %[t1], %[c]\n" \
    ""
#define GS_B16_0 \
    "ds_read_b128 %[t0], %[a] offset:%c[s]*0\n ds_read_b128 %[t1], %[a] offset:%c[s]*1\n ds_read_b128 %[t2], %[a] offset:%c[s]*2\n" \
    "ds_read_b128 %[t3], %[a] offset:%c[s]*3\n ds_read_b128 %[t4], %[a] offset:%c[s]*4\n ds_read_b128 %[t5], %[a] offset:%c[s]*5\n" \
    "s_waitcnt lgkmcnt(5)\n v_mfma_f32_32x32x16_bf16 %[c], %[w0], %[t0], 0\n ds_read_b128 %[t0], %[a] offset:%c[s]*6\n" \
    "s_waitcnt lgkmcnt(5)\n v_mfma_f32_32x32x16_bf16 %[c], %[w1], %[t1], %[c]\n ds_read_b128 %[t1], %[a] offset:%c[s]*7\n" \
    "s_waitcnt lgkmcnt(5)\n v_mfma_f32_32x32x16_bf16 %[c], %[w2], %[t2], %[c]\n ds_read_b128 %[t2], %[a] offset:%c[s]*8\n" \
    "s_waitcnt lgkmcnt(5)\n v_mfma_f32_32x32x16_bf16 %[c], %[w3], %[t3], %[c]\n ds_read_b128 %[t3], %[a] offset:%c[s]*9\n" \
    "s_waitcnt lgkmcnt(5)\n v_mfma_f32_32x32x16_bf16 %[c], %[w4], %[t4], %[c]\n ds_read_b128 %[t4], %[a] offset:%c[s]*10\n" \
    "s_waitcnt lgkmcnt(5)\n v_mfma_f32_32x32x16_bf16 %[c], %[w5], %[t5], %[c]\n ds_read_b128 %[t5], %[a] offset:%c[s]*11\n" \
    "s_waitcnt lgkmcnt(5)\n v_mfma_f32_32x32x16_bf16 %[c], %[w6], %[t0], %[c]\n ds_read_b128 %[t0], %[a] offset:%c[s]*12\n" \
    "s_waitcnt lgkmcnt(5)\n v_mfma_f32_32x32x16_bf16 %[c], %[w7], %[t1], %[c]\n ds_read_b128 %[t1], %[a] offset:%c[s]*13\n" \
    ""
#define GS_B16_1 \
    "s_waitcnt lgkmcnt(5)\n v_mfma_f32_32x32x16_bf16 %[c], %[w0], %[t2], %[c]\n ds_read_b128 %[t2], %[a] offset:%c[s]*14\n" \
    "s_waitcnt lgkmcnt(5)\n v_mfma_f32_32x32x16_bf16 %[c], %[w1], %[t3], %[c]\n ds_read_b128 %[t3], %[a] offset:%c[s]*15\n" \
    "s_waitcnt lgkmcnt(5)\n v_mfma_f32_32x32x16_bf16 %[c], %[w2], %[t4], %[c]\n s_barrier\n" \
    "ds_read_b128 %[t4], %[a] offset:%c[s]*16\n s_waitcnt lgkmcnt(5)\n v_mfma_f32_32x32x16_bf16 %[c], %[w3], %[t5], %[c]\n" \
    "ds_read_b128 %[t5], %[a] offset:%c[s]*17\n s_waitcnt lgkmcnt(5)\n v_mfma_f32_32x32x16_bf16 %[c], %[w4], %[t0], %[c]\n" \
    "ds_read_b128 %[t0], %[a] offset:%c[s]*18\n s_waitcnt lgkmcnt(5)\n v_mfma_f32_32x32x16_bf16 %[c], %[w5], %[t1], %[c]\n" \
    "ds_read_b128 %[t1], %[a] offset:%c[s]*19\n s_waitcnt lgkmcnt(5)\n v_mfma_f32_32x32x16_bf16 %[c], %[w6], %[t2], %[c]\n" \
    "ds_read_b128 %[t2], %[a] offset:%c[s]*20\n s_waitcnt lgkmcnt(5)\n v_mfma_f32_32x32x16_bf16 %[c], %[w7], %[t3], %[c]\n" \
    "ds_read_b128 %[t3], %[a] offset:%c[s]*21\n" \
    ""
#define GS_B16_2 \
    "s_waitcnt lgkmcnt(5)\n v_mfma_f32_32x32x16_bf16 %[c], %[w0], %[t4], %[c]\n ds_read_b128 %[t4], %[a] offset:%c[s]*22\n" \
    "s_waitcnt lgkmcnt(5)\n v_mfma_f32_32x32x16_bf16 %[c], %[w1], %[t5], %[c]\n ds_read_b128 %[t5], %[a] offset:%c[s]*23\n" \
    "s_waitcnt lgkmcnt(5)\n v_mfma_f32_32x32x16_bf16 %[c], %[w2], %[t0], %[c]\n ds_read_b128 %[t0], %[a] offset:%c[s]*24\n" \
    "s_waitcnt lgkmcnt(5)\n v_mfma_f32_32x32x16_bf16 %[c], %[w3], %[t1], %[c]\n ds_read_b128 %[t1], %[a] offset:%c[s]*25\n" \
    "s_waitcnt lgkmcnt(5)\n v_mfma_f32_32x32x16_bf16 %[c], %[w4], %[t2], %[c]\n ds_read_b128 %[t2], %[a] offset:%c[s]*26\n" \
    "s_waitcnt lgkmcnt(5)\n v_mfma_f32_32x32x16_bf16 %[c], %[w5], %[t3], %[c]\n ds_read_b128 %[t3], %[a] offset:%c[s]*27\n" \
    "s_waitcnt lgkmcnt(5)\n v_mfma_f32_32x32x16_bf16 %[c], %[w6], %[t4], %[c]\n ds_read_b128 %[t4], %[a] offset:%c[s]*28\n" \
    "s_waitcnt lgkmcnt(5)\n v_mfma_f32_32x32x16_bf16 %[c], %[w7], %[t5], %[c]\n ds_read_b128 %[t5], %[a] offset:%c[s]*29\n" \
    ""
#define GS_B16_3 \
    "s_waitcnt lgkmcnt(5)\n v_mfma_f32_32x32x16_bf16 %[c], %[w0], %[t0], %[c]\n ds_read_b128 %[t0], %[a] offset:%c[s]*30\n" \
    "s_waitcnt lgkmcnt(5)\n v_mfma_f32_32x32x16_bf16 %[c], %[w1], %[t1], %[c]\n ds_read_b128 %[t1], %[a] offset:%c[s]*31\n" \
    "s_waitcnt lgkmcnt(5)\n v_mfma_f32_32x32x16_bf16 %[c], %[w2], %[t2], %[c]\n s_waitcnt lgkmcnt(4)\n" \
    "v_mfma_f32_32x32x16_bf16 %[c], %[w3], %[t3], %[c]\n s_waitcnt lgkmcnt(3)\n v_mfma_f32_32x32x16_bf16 %[c], %[w4], %[t4], %[c]\n" \
    "s_waitcnt lgkmcnt(2)\n v_mfma_f32_32x32x16_bf16 %[c], %[w5], %[t5], %[c]\n s_waitcnt lgkmcnt(1)\n" \
    "v_mfma_f32_32x32x16_bf16 %[c], %[w6], %[t0], %[c]\n s_waitcnt lgkmcnt(0)\n v_mfma_f32_32x32x16_bf16 %[c], %[w7], %[t1], %[c]\n" \
    ""
#define GS_NB_0 \
    "ds_read_b128 %[t0], %[a] offset:%c[s]*0\n ds_read_b128 %[t1], %[a] offset:%c[s]*1\n ds_read_b128 %[t2], %[a] offset:%c[s]*2\n" \
    "ds_read_b128 %[t3], %[a] offset:%c[s]*3\n ds_read_b128 %[t4], %[a] offset:%c[s]*4\n ds_read_b128 %[t5], %[a] offset:%c[s]*5\n" \
    "s_waitcnt lgkmcnt(5)\n v_mfma_f32_32x32x16_bf16 %[c], %[w0], %[t0], 0\n ds_read_b128 %[t0], %[a] offset:%c[s]*6\n" \
    "s_waitcnt lgkmcnt(5)\n v_mfma_f32_32x32x16_bf16 %[c], %[w1], %[t1], %[c]\n ds_read_b128 %[t1], %[a] offset:%c[s]*7\n" \
    "s_waitcnt lgkmcnt(5)\n v_mfma_f32_32x32x16_bf16 %[c], %[w2], %[t2], %[c]\n ds_read_b128 %[t2], %[a] offset:%c[s]*8\n" \
    "s_waitcnt lgkmcnt(5)\n v_mfma_f32_32x32x16_bf16 %[c], %[w3], %[t3], %[c]\n ds_read_b128 %[t3], %[a] offset:%c[s]*9\n" \
    "s_waitcnt lgkmcnt(5)\n v_mfma_f32_32x32x16_bf16 %[c], %[w4], %[t4], %[c]\n ds_read_b128 %[t4], %[a] offset:%c[s]*10\n" \
    "s_waitcnt lgkmcnt(5)\n v_mfma_f32_32x32x16_bf16 %[c], %[w5], %[t5], %[c]\n ds_read_b128 %[t5], %[a] offset:%c[s]*11\n" \
    "s_waitcnt lgkmcnt(5)\n v_mfma_f32_32x32x16_bf16 %[c], %[w6], %[t0], %[c]\n ds_read_b128 %[t0], %[a] offset:%c[s]*12\n" \
    "s_waitcnt lgkmcnt(5)\n v_mfma_f32_32x32x16_bf16 %[c], %[w7], %[t1], %[c]\n ds_read_b128 %[t1], %[a] offset:%c[s]*13\n" \
    ""
#define GS_NB_1 \
    "s_waitcnt lgkmcnt(5)\n v_mfma_f32_32x32x16_bf16 %[c], %[w0], %[t2], %[c]\n ds_read_b128 %[t2], %[a] offset:%c[s]*14\n" \
    "s_waitcnt lgkmcnt(5)\n v_mfma_f32_32x32x16_bf16 %[c], %[w1], %[t3], %[c]\n ds_read_b128 %[t3], %[a] offset:%c[s]*15\n" \
    "s_waitcnt lgkmcnt(5)\n v_mfma_f32_32x32x16_bf16 %[c], %[w2], %[t4], %[c]\n ds_read_b128 %[t4], %[a] offset:%c[s]*16\n" \
    "s_waitcnt lgkmcnt(5)\n v_mfma_f32_32x32x16_bf16 %[c], %[w3], %[t5], %[c]\n ds_read_b128 %[t5], %[a] offset:%c[s]*17\n" \
    "s_waitcnt lgkmcnt(5)\n v_mfma_f32_32x32x16_bf16 %[c], %[w4], %[t0], %[c]\n ds_read_b128 %[t0], %[a] offset:%c[s]*18\n" \
    "s_waitcnt lgkmcnt(5)\n v_mfma_f32_32x32x16_bf16 %[c], %[w5], %[t1], %[c]\n ds_read_b128 %[t1], %[a] offset:%c[s]*19\n" \
    "s_waitcnt lgkmcnt(5)\n v_mfma_f32_32x32x16_bf16 %[c], %[w6], %[t2], %[c]\n ds_read_b128 %[t2], %[a] offset:%c[s]*20\n" \
    "s_waitcnt lgkmcnt(5)\n v_mfma_f32_32x32x16_bf16 %[c], %[w7], %[t3], %[c]\n ds_read_b128 %[t3], %[a] offset:%c[s]*21\n" \
    ""
#define GS_NB_2 \
    "s_waitcnt lgkmcnt(5)\n v_mfma_f32_32x32x16_bf16 %[c], %[w0], %[t4], %[c]\n ds_read_b128 %[t4], %[a] offset:%c[s]*22\n" \
    "s_waitcnt lgkmcnt(5)\n v_mfma_f32_32x32x16_bf16 %[c], %[w1], %[t5], %[c]\n ds_read_b128 %[t5], %[a] offset:%c[s]*23\n" \
    "s_waitcnt lgkmcnt(5)\n v_mfma_f32_32x32x16_bf16 %[c], %[w2], %[t0], %[c]\n ds_read_b128 %[t0], %[a] offset:%c[s]*24\n" \
    "s_waitcnt lgkmcnt(5)\n v_mfma_f32_32x32x16_bf16 %[c], %[w3], %[t1], %[c]\n ds_read_b128 %[t1], %[a] offset:%c[s]*25\n" \
    "s_waitcnt lgkmcnt(5)\n v_mfma_f32_32x32x16_bf16 %[c], %[w4], %[t2], %[c]\n ds_read_b128 %[t2], %[a] offset:%c[s]*26\n" \
    "s_waitcnt lgkmcnt(5)\n v_mfma_f32_32x32x16_bf16 %[c], %[w5], %[t3], %[c]\n ds_read_b128 %[t3], %[a] offset:%c[s]*27\n" \
    "s_waitcnt lgkmcnt(5)\n v_mfma_f32_32x32x16_bf16 %[c], %[w6], %[t4], %[c]\n ds_read_b128 %[t4], %[a] offset:%c[s]*28\n" \
    "s_waitcnt lgkmcnt(5)\n v_mfma_f32_32x32x16_bf16 %[c], %[w7], %[t5], %[c]\n ds_read_b128 %[t5], %[a] offset:%c[s]*29\n" \
    ""
#define GS_NB_3 \
    "s_waitcnt lgkmcnt(5)\n v_mfma_f32_32x32x16_bf16 %[c], %[w0], %[t0], %[c]\n ds_read_b128 %[t0], %[a] offset:%c[s]*30\n" \
    "s_waitcnt lgkmcnt(5)\n v_mfma_f32_32x32x16_bf16 %[c], %[w1], %[t1], %[c]\n ds_read_b128 %[t1], %[a] offset:%c[s]*31\n" \
    "s_waitcnt lgkmcnt(5)\n v_mfma_f32_32x32x16_bf16 %[c], %[w2], %[t2], %[c]\n s_waitcnt lgkmcnt(4)\n" \
    "v_mfma_f32_32x32x16_bf16 %[c], %[w3], %[t3], %[c]\n s_waitcnt lgkmcnt(3)\n v_mfma_f32_32x32x16_bf16 %[c], %[w4], %[t4], %[c]\n" \
    "s_waitcnt lgkmcnt(2)\n v_mfma_f32_32x32x16_bf16 %[c], %[w5], %[t5], %[c]\n s_waitcnt lgkmcnt(1)\n" \
    "v_mfma_f32_32x32x16_bf16 %[c], %[w6], %[t0], %[c]\n s_waitcnt lgkmcnt(0)\n v_mfma_f32_32x32x16_bf16 %[c], %[w7], %[t1], %[c]\n" \
    ""
#define GD_T_0 \
    "ds_read_b128 %[t0], %[a] offset:0\n ds_read_b128 %[t1], %[b] offset:0\n ds_read_b128 %[t2], %[a] offset:1024\n" \
    "ds_read_b128 %[t3], %[b] offset:1024\n ds_read_b128 %[t4], %[a] offset:2048\n ds_read_b128 %[t5], %[b] offset:2048\n" \
    "ds_read_b128 %[t6], %[a] offset:3072\n ds_read_b128 %[t7], %[b] offset:3072\n s_waitcnt lgkmcnt(7)\n" \
    "v_mfma_f32_32x32x16_bf16 %[c], %[w0], %[t0], 0\n ds_read_b128 %[t0], %[a] offset:4096\n s_waitcnt lgkmcnt(7)\n" \
    "v_mfma_f32_32x32x16_bf16 %[d], %[w0], %[t1], 0\n ds_read_b128 %[t1], %[b] offset:4096\n s_waitcnt lgkmcnt(7)\n" \
    "v_mfma_f32_32x32x16_bf16 %[c], %[w1], %[t2], %[c]\n ds_read_b128 %[t2], %[a] offset:5120\n s_waitcnt lgkmcnt(7)\n" \
    "v_mfma_f32_32x32x16_bf16 %[d], %[w1], %[t3], %[d]\n ds_read_b128 %[t3], %[b] offset:5120\n s_waitcnt lgkmcnt(7)\n" \
    "v_mfma_f32_32x32x16_bf16 %[c], %[w2], %[t4], %[c]\n ds_read_b128 %[t4], %[a] offset:6144\n s_waitcnt lgkmcnt(7)\n" \
    "v_mfma_f32_32x32x16_bf16 %[d], %[w2], %[t5], %[d]\n ds_read_b128 %[t5], %[b] offset:6144\n s_waitcnt lgkmcnt(7)\n" \
    "v_mfma_f32_32x32x16_bf16 %[c], %[w3], %[t6], %[c]\n ds_read_b128 %[t6], %[a] offset:7168\n s_waitcnt lgkmcnt(7)\n" \
    "v_mfma_f32_32x32x16_bf16 %[d], %[w3], %[t7], %[d]\n ds_read_b128 %[t7], %[b] offset:7168\n s_waitcnt lgkmcnt(7)\n" \
    "v_mfma_f32_32x32x16_bf16 %[c], %[w4], %[t0], %[c]\n s_barrier\n ds_read_b128 %[t0], %[a] offset:8192\n" \
    "s_waitcnt lgkmcnt(7)\n v_mfma_f32_32x32x16_bf16 %[d], %[w4], %[t1], %[d]\n ds_read_b128 %[t1], %[b] offset:8192\n" \
    "s_waitcnt lgkmcnt(7)\n v_mfma_f32_32x32x16_bf16 %[c], %[w5], %[t2], %[c]\n ds_read_b128 %[t2], %[a] offset:9216\n" \
    "s_waitcnt lgkmcnt(7)\n v_mfma_f32_32x32x16_bf16 %[d], %[w5], %[t3], %[d]\n ds_read_b128 %[t3], %[b] offset:9216\n" \
    "s_waitcnt lgkmcnt(7)\n v_mfma_f32_32x32x16_bf16 %[c], %[w6], %[t4], %[c]\n ds_read_b128 %[t4], %[a] offset:10240\n" \
    "s_waitcnt lgkmcnt(7)\n v_mfma_f32_32x32x16_bf16 %[d], %[w6], %[t5], %[d]\n ds_read_b128 %[t5], %[b] offset:10240\n" \
    "s_waitcnt lgkmcnt(7)\n v_mfma_f32_32x32x16_bf16 %[c], %[w7], %[t6], %[c]\n ds_read_b128 %[t6], %[a] offset:11264\n" \
    "s_waitcnt lgkmcnt(7)\n v_mfma_f32_32x32x16_bf16 %[d], %[w7], %[t7], %[d]\n ds_read_b128 %[t7], %[b] offset:11264\n" \
    ""
#define GD_T_1 \
    "s_waitcnt lgkmcnt(7)\n v_mfma_f32_32x32x16_bf16 %[c], %[w0], %[t0], %[c]\n ds_read_b128 %[t0], %[a] offset:12288\n" \
    "s_waitcnt lgkmcnt(7)\n v_mfma_f32_32x32x16_bf16 %[d], %[w0], %[t1], %[d]\n ds_read_b128 %[t1], %[b] offset:12288\n" \
    "s_waitcnt lgkmcnt(7)\n v_mfma_f32_32x32x16_bf16 %[c], %[w1], %[t2], %[c]\n ds_read_b128 %[t2], %[a] offset:13312\n" \
    "s_waitcnt lgkmcnt(7)\n v_mfma_f32_32x32x16_bf16 %[d], %[w1], %[t3], %[d]\n ds_read_b128 %[t3], %[b] offset:13312\n" \
    "s_waitcnt lgkmcnt(7)\n v_mfma_f32_32x32x16_bf16 %[c], %[w2], %[t4], %[c]\n ds_read_b128 %[t4], %[a] offset:14336\n" \
    "s_waitcnt lgkmcnt(7)\n v_mfma_f32_32x32x16_bf16 %[d], %[w2], %[t5], %[d]\n ds_read_b128 %[t5], %[b] offset:14336\n" \
    "s_waitcnt lgkmcnt(7)\n v_mfma_f32_32x32x16_bf16 %[c], %[w3], %[t6], %[c]\n ds_read_b128 %[t6], %[a] offset:15360\n" \
    "s_waitcnt lgkmcnt(7)\n v_mfma_f32_32x32x16_bf16 %[d], %[w3], %[t7], %[d]\n ds_read_b128 %[t7], %[b] offset:15360\n" \
    "s_waitcnt lgkmcnt(7)\n v_mfma_f32_32x32x16_bf16 %[c], %[w4], %[t0], %[c]\n s_barrier\n" \
    "ds_read_b128 %[t0], %[a] offset:16384\n s_waitcnt lgkmcnt(7)\n v_mfma_f32_32x32x16_bf16 %[d], %[w4], %[t1], %[d]\n" \
    "ds_read_b128 %[t1], %[b] offset:16384\n s_waitcnt lgkmcnt(7)\n v_mfma_f32_32x32x16_bf16 %[c], %[w5], %[t2], %[c]\n" \
    "ds_read_b128 %[t2], %[a] offset:17408\n s_waitcnt lgkmcnt(7)\n v_mfma_f32_32x32x16_bf16 %[d], %[w5], %[t3], %[d]\n" \
    "ds_read_b128 %[t3], %[b] offset:17408\n s_waitcnt lgkmcnt(7)\n v_mfma_f32_32x32x16_bf16 %[c], %[w6], %[t4], %[c]\n" \
    "ds_read_b128 %[t4], %[a] offset:18432\n s_waitcnt lgkmcnt(7)\n v_mfma_f32_32x32x16_bf16 %[d], %[w6], %[t5], %[d]\n" \
    "ds_read_b128 %[t5], %[b] offset:18432\n s_waitcnt lgkmcnt(7)\n v_mfma_f32_32x32x16_bf16 %[c], %[w7], %[t6], %[c]\n" \
    "ds_read_b128 %[t6], %[a] offset:19456\n s_waitcnt lgkmcnt(7)\n v_mfma_f32_32x32x16_bf16 %[d], %[w7], %[t7], %[d]\n" \
    "ds_read_b128 %[t7], %[b] offset:19456\n" \
    ""
#define GD_T_2 \
    "s_waitcnt lgkmcnt(7)\n v_mfma_f32_32x32x16_bf16 %[c], %[w0], %[t0], %[c]\n ds_read_b128 %[t0], %[a] offset:20480\n" \
    "s_waitcnt lgkmcnt(7)\n v_mfma_f32_32x32x16_bf16 %[d], %[w0], %[t1], %[d]\n ds_read_b128 %[t1], %[b] offset:20480\n" \
    "s_waitcnt lgkmcnt(7)\n v_mfma_f32_32x32x16_bf16 %[c], %[w1], %[t2], %[c]\n ds_read_b128 %[t2], %[a] offset:21504\n" \
    "s_waitcnt lgkmcnt(7)\n v_mfma_f32_32x32x16_bf16 %[d], %[w1], %[t3], %[d]\n ds_read_b128 %[t3], %[b] offset:21504\n" \
    "s_waitcnt lgkmcnt(7)\n v_mfma_f32_32x32x16_bf16 %[c], %[w2], %[t4], %[c]\n ds_read_b128 %[t4], %[a] offset:22528\n" \
    "s_waitcnt lgkmcnt(7)\n v_mfma_f32_32x32x16_bf16 %[d], %[w2], %[t5], %[d]\n ds_read_b128 %[t5], %[b] offset:22528\n" \
    "s_waitcnt lgkmcnt(7)\n v_mfma_f32_32x32x16_bf16 %[c], %[w3], %[t6], %[c]\n ds_read_b128 %[t6], %[a] offset:23552\n" \
    "s_waitcnt lgkmcnt(7)\n v_mfma_f32_32x32x16_bf16 %[d], %[w3], %[t7], %[d]\n ds_read_b128 %[t7], %[b] offset:23552\n" \
    "s_waitcnt lgkmcnt(7)\n v_mfma_f32_32x32x16_bf16 %[c], %[w4], %[t0], %[c]\n s_barrier\n" \
    "ds_read_b128 %[t0], %[a] offset:24576\n s_waitcnt lgkmcnt(7)\n v_mfma_f32_32x32x16_bf16 %[d], %[w4], %[t1], %[d]\n" \
    "ds_read_b128 %[t1], %[b] offset:24576\n s_waitcnt lgkmcnt(7)\n v_mfma_f32_32x32x16_bf16 %[c], %[w5], %[t2], %[c]\n" \
    "ds_read_b128 %[t2], %[a] offset:25600\n s_waitcnt lgkmcnt(7)\n v_mfma_f32_32x32x16_bf16 %[d], %[w5], %[t3], %[d]\n" \
    "ds_read_b128 %[t3], %[b] offset:25600\n s_waitcnt lgkmcnt(7)\n v_mfma_f32_32x32x16_bf16 %[c], %[w6], %[t4], %[c]\n" \
    "ds_read_b128 %[t4], %[a] offset:26624\n s_waitcnt lgkmcnt(7)\n v_mfma_f32_32x32x16_bf16 %[d], %[w6], %[t5], %[d]\n" \
    "ds_read_b128 %[t5], %[b] offset:26624\n s_waitcnt lgkmcnt(7)\n v_mfma_f32_32x32x16_bf16 %[c], %[w7], %[t6], %[c]\n" \
    "ds_read_b128 %[t6], %[a] offset:27648\n s_waitcnt lgkmcnt(7)\n v_mfma_f32_32x32x16_bf16 %[d], %[w7], %[t7], %[d]\n" \
    "ds_read_b128 %[t7], %[b] offset:27648\n" \
    ""
#define GD_T_3 \
    "s_waitcnt lgkmcnt(7)\n v_mfma_f32_32x32x16_bf16 %[c], %[w0], %[t0], %[c]\n ds_read_b128 %[t0], %[a] offset:28672\n" \
    "s_waitcnt lgkmcnt(7)\n v_mfma_f32_32x32x16_bf16 %[d], %[w0], %[t1], %[d]\n ds_read_b128 %[t1], %[b] offset:28672\n" \
    "s_waitcnt lgkmcnt(7)\n v_mfma_f32_32x32x16_bf16 %[c], %[w1], %[t2], %[c]\n ds_read_b128 %[t2], %[a] offset:29696\n" \
    "s_waitcnt lgkmcnt(7)\n v_mfma_f32_32x32x16_bf16 %[d], %[w1], %[t3], %[d]\n ds_read_b128 %[t3], %[b] offset:29696\n" \
    "s_waitcnt lgkmcnt(7)\n v_mfma_f32_32x32x16_bf16 %[c], %[w2], %[t4], %[c]\n ds_read_b128 %[t4], %[a] offset:30720\n" \
    "s_waitcnt lgkmcnt(7)\n v_mfma_f32_32x32x16_bf16 %[d], %[w2], %[t5], %[d]\n ds_read_b128 %[t5], %[b] offset:30720\n" \
    "s_waitcnt lgkmcnt(7)\n v_mfma_f32_32x32x16_bf16 %[c], %[w3], %[t6], %[c]\n ds_read_b128 %[t6], %[a] offset:31744\n" \
    "s_waitcnt lgkmcnt(7)\n v_mfma_f32_32x32x16_bf16 %[d], %[w3], %[t7], %[d]\n ds_read_b128 %[t7], %[b] offset:31744\n" \
    "s_waitcnt lgkmcnt(7)\n v_mfma_f32_32x32x16_bf16 %[c], %[w4], %[t0], %[c]\n s_waitcnt lgkmcnt(6)\n" \
    "v_mfma_f32_32x32x16_bf16 %[d], %[w4], %[t1], %[d]\n s_waitcnt lgkmcnt(5)\n v_mfma_f32_32x32x16_bf16 %[c], %[w5], %[t2], %[c]\n" \
    "s_waitcnt lgkmcnt(4)\n v_mfma_f32_32x32x16_bf16 %[d], %[w5], %[t3], %[d]\n s_waitcnt lgkmcnt(3)\n" \
    "v_mfma_f32_32x32x16_bf16 %[c], %[w6], %[t4], %[c]\n s_waitcnt lgkmcnt(2)\n v_mfma_f32_32x32x16_bf16 %[d], %[w6], %[t5], %[d]\n" \
    "s_waitcnt lgkmcnt(1)\n v_mfma_f32_32x32x16_bf16 %[c], %[w7], %[t6], %[c]\n s_waitcnt lgkmcnt(0)\n" \
    "v_mfma_f32_32x32x16_bf16 %[d], %[w7], %[t7], %[d]\n" \
    ""
#define GD_N_0 \
    "ds_read_b128 %[t0], %[a] offset:0\n ds_read_b128 %[t1], %[b] offset:0\n ds_read_b128 %[t2], %[a] offset:1024\n" \
    "ds_read_b128 %[t3], %[b] offset:1024\n ds_read_b128 %[t4], %[a] offset:2048\n ds_read_b128 %[t5], %[b] offset:2048\n" \
    "ds_read_b128 %[t6], %[a] offset:3072\n ds_read_b128 %[t7], %[b] offset:3072\n s_waitcnt lgkmcnt(7)\n" \
    "v_mfma_f32_32x32x16_bf16 %[c], %[t0], %[w0], 0\n ds_read_b128 %[t0], %[a] offset:4096\n s_waitcnt lgkmcnt(7)\n" \
    "v_mfma_f32_32x32x16_bf16 %[d], %[t1], %[w0], 0\n ds_read_b128 %[t1], %[b] offset:4096\n s_waitcnt lgkmcnt(7)\n" \
    "v_mfma_f32_32x32x16_bf16 %[c], %[t2], %[w1], %[c]\n ds_read_b128 %[t2], %[a] offset:5120\n s_waitcnt lgkmcnt(7)\n" \
    "v_mfma_f32_32x32x16_bf16 %[d], %[t3], %[w1], %[d]\n ds_read_b128 %[t3], %[b] offset:5120\n s_waitcnt lgkmcnt(7)\n" \
    "v_mfma_f32_32x32x16_bf16 %[c], %[t4], %[w2], %[c]\n ds_read_b128 %[t4], %[a] offset:6144\n s_waitcnt lgkmcnt(7)\n" \
    "v_mfma_f32_32x32x16_bf16 %[d], %[t5], %[w2], %[d]\n ds_read_b128 %[t5], %[b] offset:6144\n s_waitcnt lgkmcnt(7)\n" \
    "v_mfma_f32_32x32x16_bf16 %[c], %[t6], %[w3], %[c]\n ds_read_b128 %[t6], %[a] offset:7168\n s_waitcnt lgkmcnt(7)\n" \
    "v_mfma_f32_32x32x16_bf16 %[d], %[t7], %[w3], %[d]\n ds_read_b128 %[t7], %[b] offset:7168\n s_waitcnt lgkmcnt(7)\n" \
    "v_mfma_f32_32x32x16_bf16 %[c], %[t0], %[w4], %[c]\n s_barrier\n ds_read_b128 %[t0], %[a] offset:8192\n" \
    "s_waitcnt lgkmcnt(7)\n v_mfma_f32_32x32x16_bf16 %[d], %[t1], %[w4], %[d]\n ds_read_b128 %[t1], %[b] offset:8192\n" \
    "s_waitcnt lgkmcnt(7)\n v_mfma_f32_32x32x16_bf16 %[c], %[t2], %[w5], %[c]\n ds_read_b128 %[t2], %[a] offset:9216\n" \
    "s_waitcnt lgkmcnt(7)\n v_mfma_f32_32x32x16_bf16 %[d], %[t3], %[w5], %[d]\n ds_read_b128 %[t3], %[b] offset:9216\n" \
    "s_waitcnt lgkmcnt(7)\n v_mfma_f32_32x32x16_bf16 %[c], %[t4], %[w6], %[c]\n ds_read_b128 %[t4], %[a] offset:10240\n" \
    "s_waitcnt lgkmcnt(7)\n v_mfma_f32_32x32x16_bf16 %[d], %[t5], %[w6], %[d]\n ds_read_b128 %[t5], %[b] offset:10240\n" \
    "s_waitcnt lgkmcnt(7)\n v_mfma_f32_32x32x16_bf16 %[c], %[t6], %[w7], %[c]\n ds_read_b128 %[t6], %[a] offset:11264\n" \
    "s_waitcnt lgkmcnt(7)\n v_mfma_f32_32x32x16_bf16 %[d], %[t7], %[w7], %[d]\n ds_read_b128 %[t7], %[b] offset:11264\n" \
    ""
#define GD_N_1 \
    "s_waitcnt lgkmcnt(7)\n v_mfma_f32_32x32x16_bf16 %[c], %[t0], %[w0], %[c]\n ds_read_b128 %[t0], %[a] offset:12288\n" \
    "s_waitcnt lgkmcnt(7)\n v_mfma_f32_32x32x16_bf16 %[d], %[t1], %[w0], %[d]\n ds_read_b128 %[t1], %[b] offset:12288\n" \
    "s_waitcnt lgkmcnt(7)\n v_mfma_f32_32x32x16_bf16 %[c], %[t2], %[w1], %[c]\n ds_read_b128 %[t2], %[a] offset:13312\n" \
    "s_waitcnt lgkmcnt(7)\n v_mfma_f32_32x32x16_bf16 %[d], %[t3], %[w1], %[d]\n ds_read_b128 %[t3], %[b] offset:13312\n" \
    "s_waitcnt lgkmcnt(7)\n v_mfma_f32_32x32x16_bf16 %[c], %[t4], %[w2], %[c]\n ds_read_b128 %[t4], %[a] offset:14336\n" \
    "s_waitcnt lgkmcnt(7)\n v_mfma_f32_32x32x16_bf16 %[d], %[t5], %[w2], %[d]\n ds_read_b128 %[t5], %[b] offset:14336\n" \
    "s_waitcnt lgkmcnt(7)\n v_mfma_f32_32x32x16_bf16 %[c], %[t6], %[w3], %[c]\n ds_read_b128 %[t6], %[a] offset:15360\n" \
    "s_waitcnt lgkmcnt(7)\n v_mfma_f32_32x32x16_bf16 %[d], %[t7], %[w3], %[d]\n ds_read_b128 %[t7], %[b] offset:15360\n" \
    "s_waitcnt lgkmcnt(7)\n v_mfma_f32_32x32x16_bf16 %[c], %[t0], %[w4], %[c]\n s_barrier\n" \
    "ds_read_b128 %[t0], %[a] offset:16384\n s_waitcnt lgkmcnt(7)\n v_mfma_f32_32x32x16_bf16 %[d], %[t1], %[w4], %[d]\n" \
    "ds_read_b128 %[t1], %[b] offset:16384\n s_waitcnt lgkmcnt(7)\n v_mfma_f32_32x32x16_bf16 %[c], %[t2], %[w5], %[c]\n" \
    "ds_read_b128 %[t2], %[a] offset:17408\n s_waitcnt lgkmcnt(7)\n v_mfma_f32_32x32x16_bf16 %[d], %[t3], %[w5], %[d]\n" \
    "ds_read_b128 %[t3], %[b] offset:17408\n s_waitcnt lgkmcnt(7)\n v_mfma_f32_32x32x16_bf16 %[c], %[t4], %[w6], %[c]\n" \
    "ds_read_b128 %[t4], %[a] offset:18432\n s_waitcnt lgkmcnt(7)\n v_mfma_f32_32x32x16_bf16 %[d], %[t5], %[w6], %[d]\n" \
    "ds_read_b128 %[t5], %[b] offset:18432\n s_waitcnt lgkmcnt(7)\n v_mfma_f32_32x32x16_bf16 %[c], %[t6], %[w7], %[c]\n" \
    "ds_read_b128 %[t6], %[a] offset:19456\n s_waitcnt lgkmcnt(7)\n v_mfma_f32_32x32x16_bf16 %[d], %[t7], %[w7], %[d]\n" \
    "ds_read_b128 %[t7], %[b] offset:19456\n" \
    ""
#define GD_N_2 \
    "s_waitcnt lgkmcnt(7)\n v_mfma_f32_32x32x16_bf16 %[c], %[t0], %[w0], %[c]\n ds_read_b128 %[t0], %[a] offset:20480\n" \
    "s_waitcnt lgkmcnt(7)\n v_mfma_f32_32x32x16_bf16 %[d], %[t1], %[w0], %[d]\n ds_read_b128 %[t1], %[b] offset:20480\n" \
    "s_waitcnt lgkmcnt(7)\n v_mfma_f32_32x32x16_bf16 %[c], %[t2], %[w1], %[c]\n ds_read_b128 %[t2], %[a] offset:21504\n" \
    "s_waitcnt lgkmcnt(7)\n v_mfma_f32_32x32x16_bf16 %[d], %[t3], %[w1], %[d]\n ds_read_b128 %[t3], %[b] offset:21504\n" \
    "s_waitcnt lgkmcnt(7)\n v_mfma_f32_32x32x16_bf16 %[c], %[t4], %[w2], %[c]\n ds_read_b128 %[t4], %[a] offset:22528\n" \
    "s_waitcnt lgkmcnt(7)\n v_mfma_f32_32x32x16_bf16 %[d], %[t5], %[w2], %[d]\n ds_read_b128 %[t5], %[b] offset:22528\n" \
    "s_waitcnt lgkmcnt(7)\n v_mfma_f32_32x32x16_bf16 %[c], %[t6], %[w3], %[c]\n ds_read_b128 %[t6], %[a] offset:23552\n" \
    "s_waitcnt lgkmcnt(7)\n v_mfma_f32_32x32x16_bf16 %[d], %[t7], %[w3], %[d]\n ds_read_b128 %[t7], %[b] offset:23552\n" \
    "s_waitcnt lgkmcnt(7)\n v_mfma_f32_32x32x16_bf16 %[c], %[t0], %[w4], %[c]\n s_barrier\n" \
    "ds_read_b128 %[t0], %[a] offset:24576\n s_waitcnt lgkmcnt(7)\n v_mfma_f32_32x32x16_bf16 %[d], %[t1], %[w4], %[d]\n" \
    "ds_read_b128 %[t1], %[b] offset:24576\n s_waitcnt lgkmcnt(7)\n v_mfma_f32_32x32x16_bf16 %[c], %[t2], %[w5], %[c]\n" \
    "ds_read_b128 %[t2], %[a] offset:25600\n s_waitcnt lgkmcnt(7)\n v_mfma_f32_32x32x16_bf16 %[d], %[t3], %[w5], %[d]\n" \
    "ds_read_b128 %[t3], %[b] offset:25600\n s_waitcnt lgkmcnt(7)\n v_mfma_f32_32x32x16_bf16 %[c], %[t4], %[w6], %[c]\n" \
    "ds_read_b128 %[t4], %[a] offset:26624\n s_waitcnt lgkmcnt(7)\n v_mfma_f32_32x32x16_bf16 %[d], %[t5], %[w6], %[d]\n" \
    "ds_read_b128 %[t5], %[b] offset:26624\n s_waitcnt lgkmcnt(7)\n v_mfma_f32_32x32x16_bf16 %[c], %[t6], %[w7], %[c]\n" \
    "ds_read_b128 %[t6], %[a] offset:27648\n s_waitcnt lgkmcnt(7)\n v_mfma_f32_32x32x16_bf16 %[d], %[t7], %[w7], %[d]\n" \
    "ds_read_b128 %[t7], %[b] offset:27648\n" \
    ""
#define GD_N_3 \
    "s_waitcnt lgkmcnt(7)\n v_mfma_f32_32x32x16_bf16 %[c], %[t0], %[w0], %[c]\n ds_read_b128 %[t0], %[a] offset:28672\n" \
    "s_waitcnt lgkmcnt(7)\n v_mfma_f32_32x32x16_bf16 %[d], %[t1], %[w0], %[d]\n ds_read_b128 %[t1], %[b] offset:28672\n" \
    "s_waitcnt lgkmcnt(7)\n v_mfma_f32_32x32x16_bf16 %[c], %[t2], %[w1], %[c]\n ds_read_b128 %[t2], %[a] offset:29696\n" \
    "s_waitcnt lgkmcnt(7)\n v_mfma_f32_32x32x16_bf16 %[d], %[t3], %[w1], %[d]\n ds_read_b128 %[t3], %[b] offset:29696\n" \
    "s_waitcnt lgkmcnt(7)\n v_mfma_f32_32x32x16_bf16 %[c], %[t4], %[w2], %[c]\n ds_read_b128 %[t4], %[a] offset:30720\n" \
    "s_waitcnt lgkmcnt(7)\n v_mfma_f32_32x32x16_bf16 %[d], %[t5], %[w2], %[d]\n ds_read_b128 %[t5], %[b] offset:30720\n" \
    "s_waitcnt lgkmcnt(7)\n v_mfma_f32_32x32x16_bf16 %[c], %[t6], %[w3], %[c]\n ds_read_b128 %[t6], %[a] offset:31744\n" \
    "s_waitcnt lgkmcnt(7)\n v_mfma_f32_32x32x16_bf16 %[d], %[t7], %[w3], %[d]\n ds_read_b128 %[t7], %[b] offset:31744\n" \
    "s_waitcnt lgkmcnt(7)\n v_mfma_f32_32x32x16_bf16 %[c], %[t0], %[w4], %[c]\n s_waitcnt lgkmcnt(6)\n" \
    "v_mfma_f32_32x32x16_bf16 %[d], %[t1], %[w4], %[d]\n s_waitcnt lgkmcnt(5)\n v_mfma_f32_32x32x16_bf16 %[c], %[t2], %[w5], %[c]\n" \
    "s_waitcnt lgkmcnt(4)\n v_mfma_f32_32x32x16_bf16 %[d], %[t3], %[w5], %[d]\n s_waitcnt lgkmcnt(3)\n" \
    "v_mfma_f32_32x32x16_bf16 %[c], %[t4], %[w6], %[c]\n s_waitcnt lgkmcnt(2)\n v_mfma_f32_32x32x16_bf16 %[d], %[t5], %[w6], %[d]\n" \
    "s_waitcnt lgkmcnt(1)\n v_mfma_f32_32x32x16_bf16 %[c], %[t6], %[w7], %[c]\n s_waitcnt lgkmcnt(0)\n" \
    "v_mfma_f32_32x32x16_bf16 %[d], %[t7], %[w7], %[d]\n" \
    ""
#define MM_ "v_mfma_f32_32x32x16_bf16 "
#define MT_(c, w, t, ci) MM_ "%[" #c "], %[w" #w "], %[t" #t "], " ci "\n\t"
#define MN_(c, w, t, ci) MM_ "%[" #c "], %[t" #t "], %[w" #w "], " ci "\n\t"
#define WINS_ [w0] "v"(W[0]), [w1] "v"(W[1]), [w2] "v"(W[2]), [w3] "v"(W[3]), [w4] "v"(W[4]), [w5] "v"(W[5]), [w6] "v"(W[6]), [w7] "v"(W[7])
struct Tmp { u32x4 t0, t1, t2, t3, t4, t5, t6, t7; };
#define T6_(m) [t0] m(t.t0), [t1] m(t.t1), [t2] m(t.t2), [t3] m(t.t3), [t4] m(t.t4), [t5] m(t.t5)
#define T8_(m) T6_(m), [t6] m(t.t6), [t7] m(t.t7)
#define GS_(TXT, CM, TM) asm volatile(TXT : [c] CM(c), T6_(TM) : WINS_, [a] "v"(a), [s] "i"(ST) : "memory")
#define GD_(TT, TN, CM, TM) asm volatile("s_cmp_lg_u32 %[f], 0\n\ts_cbranch_scc1 1f\n" TN "s_branch 2f\n1:\n" TT "2:" : [c] CM(c), [d] CM(d), T8_(TM) : WINS_, [a] "v"(a), [b] "v"(b), [f] "s"(T) : "scc", "memory")
template <int MODE, int Q, int ST> DEV void gs(f32x16& c, Tmp& t, const u32x4* W, unsigned a) {
    if constexpr (MODE == 0) { if constexpr (Q == 0) GS_(GS_B8_0, "+v", "=&v"); else if constexpr (Q == 1) GS_(GS_B8_1, "+v", "+v"); else if constexpr (Q == 2) GS_(GS_B8_2, "+v", "+v"); else GS_(GS_B8_3, "+v", "+v"); }
    else if constexpr (MODE == 1) { if constexpr (Q == 0) GS_(GS_B16_0, "=&v", "=&v"); else if constexpr (Q == 1) GS_(GS_B16_1, "+v", "+v"); else if constexpr (Q == 2) GS_(GS_B16_2, "+v", "+v"); else GS_(GS_B16_3, "+v", "+v"); }
    else { if constexpr (Q == 0) GS_(GS_NB_0, "=&v", "=&v"); else if constexpr (Q == 1) GS_(GS_NB_1, "+v", "+v"); else if constexpr (Q == 2) GS_(GS_NB_2, "+v", "+v"); else GS_(GS_NB_3, "+v", "+v"); }
}
template <int Q> DEV void gd(int T, f32x16& c, f32x16& d, Tmp& t, const u32x4* W, unsigned a, unsigned b) {
    if constexpr (Q == 0) GD_(GD_T_0, GD_N_0, "=&v", "=&v"); else if constexpr (Q == 1) GD_(GD_T_1, GD_N_1, "+v", "+v");
    else if constexpr (Q == 2) GD_(GD_T_2, GD_N_2, "+v", "+v"); else GD_(GD_T_3, GD_N_3, "+v", "+v");
}
DEV void wld8(u32x4* W, unsigned vo, u32x4 rd, int so) { gld4(W[0], W[1], W[2], W[3], vo, rd, so); gld4(W[4], W[5], W[6], W[7], vo, rd, so + 4096); }
template <int N> DEV void wwait8(u32x4* W) {
    asm volatile("s_waitcnt vmcnt(%8)" : "+v"(W[0]), "+v"(W[1]), "+v"(W[2]), "+v"(W[3]), "+v"(W[4]), "+v"(W[5]), "+v"(W[6]), "+v"(W[7]) : "i"(N) : "memory"); }
DEV void wwait32(u32x4* W) { wwait8<0>(W); wwait8<0>(W + 8); wwait8<0>(W + 16); wwait8<0>(W + 24); }
#define CS8_(M, C0) "s_nop 1\n\t" M(c, 0, 0, C0) M(c, 1, 0, "%[c]") M(c, 2, 0, "%[c]") M(c, 3, 0, "%[c]") M(c, 4, 0, "%[c]") M(c, 5, 0, "%[c]") M(c, 6, 0, "%[c]") M(c, 7, 0, "%[c]")
template <bool T, bool FIRST> DEV void colsum8(f32x16& c, const u32x4* W, u32x4 ones) {
    if constexpr (T && FIRST) asm volatile(CS8_(MT_, "0") : [c] "=&v"(c) : WINS_, [t0] "v"(ones));
    else if constexpr (T) asm volatile(CS8_(MT_, "%[c]") : [c] "+v"(c) : WINS_, [t0] "v"(ones));
    else if constexpr (FIRST) asm volatile(CS8_(MN_, "0") : [c] "=&v"(c) : WINS_, [t0] "v"(ones));
    else asm volatile(CS8_(MN_, "%[c]") : [c] "+v"(c) : WINS_, [t0] "v"(ones));
}
template <bool T> DEV void colsum32(f32x16& c, const u32x4* W, u32x4 ones) { colsum8<T, true>(c, W, ones); colsum8<T, false>(c, W + 8, ones); colsum8<T, false>(c, W + 16, ones); colsum8<T, false>(c, W + 24, ones); }
template <bool FIRST> DEV void colsum8d(int T, f32x16& c, const u32x4* W, u32x4 ones) {
    if constexpr (FIRST) asm volatile("s_cmp_lg_u32 %[f], 0\n\ts_cbranch_scc1 1f\n\t" CS8_(MN_, "0") "s_branch 2f\n1:\n\t" CS8_(MT_, "0") "2:" : [c] "=&v"(c) : WINS_, [t0] "v"(ones), [f] "s"(T) : "scc");
    else asm volatile("s_cmp_lg_u32 %[f], 0\n\ts_cbranch_scc1 1f\n\t" CS8_(MN_, "%[c]") "s_branch 2f\n1:\n\t" CS8_(MT_, "%[c]") "2:" : [c] "+v"(c) : WINS_, [t0] "v"(ones), [f] "s"(T) : "scc");
}
DEV void settle(f32x16& a) { asm volatile("s_nop 7\n\ts_nop 7" : "+v"(a)); }
DEV void settle(f32x16& a, f32x16& b) { asm volatile("s_nop 7\n\ts_nop 7" : "+v"(a), "+v"(b)); }
DEV void settle(f32x16& a, f32x16& b, f32x16& c) { asm volatile("s_nop 7\n\ts_nop 7" : "+v"(a), "+v"(b), "+v"(c)); }

#define ATT_BODY \
    "v_exp_f32 v200, v200\n v_exp_f32 v201, v201\n s_waitcnt lgkmcnt(1)\n v_smfmac_f32_32x32x32_bf16 v[168:183], v[244:247], v[224:231], v251\n" \
    "v_exp_f32 v202, v202\n v_exp_f32 v203, v203\n v_exp_f32 v204, v204\n v_mfma_f32_32x32x16_bf16 v[216:231], v[236:239], v[232:235], v[184:199]\n" \
    "ds_read_b128 v[236:239], v248 offset:2048\n" \
    "ds_read_b128 v[244:247], v249 offset:512\n" \
    "v_exp_f32 v205, v205\n v_exp_f32 v206, v206\n v_exp_f32 v207, v207\n v_exp_f32 v208, v208\n" \
    "v_exp_f32 v209, v209\n s_waitcnt lgkmcnt(2)\n v_smfmac_f32_32x32x32_bf16 v[168:183], v[240:243], v[200:207], v251\n" \
    "ds_read_b128 v[240:243], v249 offset:1024\n" \
    "v_exp_f32 v210, v210\n v_exp_f32 v211, v211\n v_exp_f32 v212, v212\n v_exp_f32 v213, v213\n" \
    "v_exp_f32 v214, v214\n v_exp_f32 v215, v215\n v_exp_f32 v216, v216\n v_exp_f32 v217, v217\n" \
    "s_waitcnt lgkmcnt(1)\n v_smfmac_f32_32x32x32_bf16 v[168:183], v[244:247], v[208:215], v251\n" \
    "v_exp_f32 v218, v218\n v_exp_f32 v219, v219\n v_exp_f32 v220, v220\n v_mfma_f32_32x32x16_bf16 v[200:215], v[236:239], v[232:235], v[184:199]\n" \
    "ds_read_b128 v[236:239], v248 offset:3072\n" \
    "ds_read_b128 v[244:247], v249 offset:1536\n" \
    "v_exp_f32 v221, v221\n v_exp_f32 v222, v222\n v_exp_f32 v223, v223\n v_exp_f32 v224, v224\n" \
    "v_exp_f32 v225, v225\n s_waitcnt lgkmcnt(2)\n v_smfmac_f32_32x32x32_bf16 v[168:183], v[240:243], v[216:223], v251\n" \
    "ds_read_b128 v[240:243], v249 offset:2048\n" \
    "v_exp_f32 v226, v226\n v_exp_f32 v227, v227\n v_exp_f32 v228, v228\n v_exp_f32 v229, v229\n" \
    "v_exp_f32 v230, v230\n v_exp_f32 v231, v231\n v_exp_f32 v200, v200\n v_exp_f32 v201, v201\n" \
    "s_waitcnt lgkmcnt(1)\n v_smfmac_f32_32x32x32_bf16 v[168:183], v[244:247], v[224:231], v251\n" \
    "v_exp_f32 v202, v202\n v_exp_f32 v203, v203\n v_exp_f32 v204, v204\n v_mfma_f32_32x32x16_bf16 v[216:231], v[236:239], v[232:235], v[184:199]\n" \
    "ds_read_b128 v[236:239], v248 offset:4096\n" \
    "ds_read_b128 v[244:247], v249 offset:2560\n" \
    "v_exp_f32 v205, v205\n v_exp_f32 v206, v206\n v_exp_f32 v207, v207\n v_exp_f32 v208, v208\n" \
    "v_exp_f32 v209, v209\n s_waitcnt lgkmcnt(2)\n v_smfmac_f32_32x32x32_bf16 v[168:183], v[240:243], v[200:207], v251\n" \
    "ds_read_b128 v[240:243], v249 offset:3072\n" \
    "v_exp_f32 v210, v210\n v_exp_f32 v211, v211\n v_exp_f32 v212, v212\n v_exp_f32 v213, v213\n" \
    "v_exp_f32 v214, v214\n v_exp_f32 v215, v215\n v_exp_f32 v216, v216\n v_exp_f32 v217, v217\n" \
    "s_waitcnt lgkmcnt(1)\n v_smfmac_f32_32x32x32_bf16 v[168:183], v[244:247], v[208:215], v251\n" \
    "v_exp_f32 v218, v218\n v_exp_f32 v219, v219\n v_exp_f32 v220, v220\n v_mfma_f32_32x32x16_bf16 v[200:215], v[236:239], v[232:235], v[184:199]\n" \
    "ds_read_b128 v[236:239], v248 offset:5120\n" \
    "ds_read_b128 v[244:247], v249 offset:3584\n" \
    "v_exp_f32 v221, v221\n v_exp_f32 v222, v222\n v_exp_f32 v223, v223\n v_exp_f32 v224, v224\n" \
    "v_exp_f32 v225, v225\n s_waitcnt lgkmcnt(2)\n v_smfmac_f32_32x32x32_bf16 v[168:183], v[240:243], v[216:223], v251\n" \
    "v_add_u32 v249, v249, v250\n v_add_u32 v248, 4096, v248\n ds_read_b128 v[240:243], v249\n" \
    "v_exp_f32 v226, v226\n v_exp_f32 v227, v227\n v_exp_f32 v228, v228\n v_exp_f32 v229, v229\n" \
    "v_exp_f32 v230, v230\n v_exp_f32 v231, v231\n" \
    ""
#define ATT_FIRST \
    "v_exp_f32 v200, v200\n v_exp_f32 v201, v201\n s_waitcnt lgkmcnt(1)\n v_smfmac_f32_32x32x32_bf16 v[168:183], v[244:247], v[224:231], v251\n" \
    "v_exp_f32 v202, v202\n v_exp_f32 v203, v203\n v_exp_f32 v204, v204\n v_mfma_f32_32x32x16_bf16 v[216:231], v[236:239], v[232:235], v[184:199]\n" \
    "ds_read_b128 v[236:239], v248 offset:2048\n" \
    "ds_read_b128 v[244:247], v249 offset:512\n" \
    "s_mov_b32 m0, %[lo]\n s_add_u32 %[lo], %[lo], 4096\n buffer_load_dwordx4 %[vo], %[rd], %[ko] offen offset:0 sc1 lds\n" \
    "v_exp_f32 v205, v205\n v_exp_f32 v206, v206\n v_exp_f32 v207, v207\n v_exp_f32 v208, v208\n" \
    "v_exp_f32 v209, v209\n s_waitcnt lgkmcnt(2)\n v_smfmac_f32_32x32x32_bf16 v[168:183], v[240:243], v[200:207], v251\n" \
    "ds_read_b128 v[240:243], v249 offset:1024\n" \
    "buffer_store_dwordx4 %[fill], %[vo], %[rd], %[r0] offen sc1\n" \
    "v_exp_f32 v210, v210\n v_exp_f32 v211, v211\n v_exp_f32 v212, v212\n v_exp_f32 v213, v213\n" \
    "v_exp_f32 v214, v214\n v_exp_f32 v215, v215\n v_exp_f32 v216, v216\n v_exp_f32 v217, v217\n" \
    "s_waitcnt lgkmcnt(1)\n v_smfmac_f32_32x32x32_bf16 v[168:183], v[244:247], v[208:215], v251\n" \
    "v_exp_f32 v218, v218\n v_exp_f32 v219, v219\n v_exp_f32 v220, v220\n v_mfma_f32_32x32x16_bf16 v[200:215], v[236:239], v[232:235], v[184:199]\n" \
    "ds_read_b128 v[236:239], v248 offset:3072\n" \
    "ds_read_b128 v[244:247], v249 offset:1536\n" \
    "buffer_load_dwordx4 %[vo], %[rd], %[ko] offen offset:1024 sc1 lds\n" \
    "v_exp_f32 v221, v221\n v_exp_f32 v222, v222\n v_exp_f32 v223, v223\n v_exp_f32 v224, v224\n" \
    "v_exp_f32 v225, v225\n s_waitcnt lgkmcnt(2)\n v_smfmac_f32_32x32x32_bf16 v[168:183], v[240:243], v[216:223], v251\n" \
    "ds_read_b128 v[240:243], v249 offset:2048\n" \
    "buffer_store_dwordx4 %[fill], %[vo], %[rd], %[r1] offen sc1\n" \
    "v_exp_f32 v226, v226\n v_exp_f32 v227, v227\n v_exp_f32 v228, v228\n v_exp_f32 v229, v229\n" \
    "v_exp_f32 v230, v230\n v_exp_f32 v231, v231\n v_exp_f32 v200, v200\n v_exp_f32 v201, v201\n" \
    "s_waitcnt lgkmcnt(1)\n v_smfmac_f32_32x32x32_bf16 v[168:183], v[244:247], v[224:231], v251\n" \
    "v_exp_f32 v202, v202\n v_exp_f32 v203, v203\n v_exp_f32 v204, v204\n v_mfma_f32_32x32x16_bf16 v[216:231], v[236:239], v[232:235], v[184:199]\n" \
    "ds_read_b128 v[236:239], v248 offset:4096\n" \
    "ds_read_b128 v[244:247], v249 offset:2560\n" \
    "buffer_load_dwordx4 %[vo], %[rd], %[ko] offen offset:2048 sc1 lds\n" \
    "v_exp_f32 v205, v205\n v_exp_f32 v206, v206\n v_exp_f32 v207, v207\n v_exp_f32 v208, v208\n" \
    "v_exp_f32 v209, v209\n s_waitcnt lgkmcnt(2)\n v_smfmac_f32_32x32x32_bf16 v[168:183], v[240:243], v[200:207], v251\n" \
    "ds_read_b128 v[240:243], v249 offset:3072\n" \
    "buffer_store_dwordx4 %[fill], %[vo], %[rd], %[r2] offen sc1\n" \
    "v_exp_f32 v210, v210\n v_exp_f32 v211, v211\n v_exp_f32 v212, v212\n v_exp_f32 v213, v213\n" \
    "v_exp_f32 v214, v214\n v_exp_f32 v215, v215\n v_exp_f32 v216, v216\n v_exp_f32 v217, v217\n" \
    "s_waitcnt lgkmcnt(1)\n v_smfmac_f32_32x32x32_bf16 v[168:183], v[244:247], v[208:215], v251\n" \
    "v_exp_f32 v218, v218\n v_exp_f32 v219, v219\n v_exp_f32 v220, v220\n v_mfma_f32_32x32x16_bf16 v[200:215], v[236:239], v[232:235], v[184:199]\n" \
    "ds_read_b128 v[236:239], v248 offset:5120\n" \
    "ds_read_b128 v[244:247], v249 offset:3584\n" \
    "buffer_load_dwordx4 %[vo], %[rd], %[ko] offen offset:3072 sc1 lds\n" \
    "s_add_u32 %[ko], %[ko], 4096\n v_exp_f32 v221, v221\n v_exp_f32 v222, v222\n v_exp_f32 v223, v223\n" \
    "v_exp_f32 v224, v224\n v_exp_f32 v225, v225\n s_waitcnt lgkmcnt(2)\n v_smfmac_f32_32x32x32_bf16 v[168:183], v[240:243], v[216:223], v251\n" \
    "v_add_u32 v249, v249, v250\n v_add_u32 v248, 4096, v248\n ds_read_b128 v[240:243], v249\n" \
    "buffer_store_dwordx4 %[fill], %[vo], %[rd], %[r2] offen offset:1024 sc1\n" \
    "v_exp_f32 v226, v226\n v_exp_f32 v227, v227\n v_exp_f32 v228, v228\n v_exp_f32 v229, v229\n" \
    "v_exp_f32 v230, v230\n v_exp_f32 v231, v231\n v_exp_f32 v200, v200\n v_exp_f32 v201, v201\n" \
    "s_waitcnt lgkmcnt(1)\n v_smfmac_f32_32x32x32_bf16 v[168:183], v[244:247], v[224:231], v251\n" \
    "v_exp_f32 v202, v202\n v_exp_f32 v203, v203\n v_exp_f32 v204, v204\n v_mfma_f32_32x32x16_bf16 v[216:231], v[236:239], v[232:235], v[184:199]\n" \
    "ds_read_b128 v[236:239], v248 offset:2048\n" \
    "ds_read_b128 v[244:247], v249 offset:512\n" \
    "s_mov_b32 m0, %[lo]\n s_add_u32 %[lo], %[lo], 4096\n buffer_load_dwordx4 %[vo], %[rd], %[ko] offen offset:0 sc1 lds\n" \
    "v_exp_f32 v205, v205\n v_exp_f32 v206, v206\n v_exp_f32 v207, v207\n v_exp_f32 v208, v208\n" \
    "v_exp_f32 v209, v209\n s_waitcnt lgkmcnt(2)\n v_smfmac_f32_32x32x32_bf16 v[168:183], v[240:243], v[200:207], v251\n" \
    "ds_read_b128 v[240:243], v249 offset:1024\n" \
    "buffer_store_dwordx4 %[fill], %[vo], %[rd], %[r3] offen sc1\n" \
    "v_exp_f32 v210, v210\n v_exp_f32 v211, v211\n v_exp_f32 v212, v212\n v_exp_f32 v213, v213\n" \
    "v_exp_f32 v214, v214\n v_exp_f32 v215, v215\n v_exp_f32 v216, v216\n v_exp_f32 v217, v217\n" \
    "s_waitcnt lgkmcnt(1)\n v_smfmac_f32_32x32x32_bf16 v[168:183], v[244:247], v[208:215], v251\n" \
    "v_exp_f32 v218, v218\n v_exp_f32 v219, v219\n v_exp_f32 v220, v220\n v_mfma_f32_32x32x16_bf16 v[200:215], v[236:239], v[232:235], v[184:199]\n" \
    "ds_read_b128 v[236:239], v248 offset:3072\n" \
    "ds_read_b128 v[244:247], v249 offset:1536\n" \
    "buffer_load_dwordx4 %[vo], %[rd], %[ko] offen offset:1024 sc1 lds\n" \
    "v_exp_f32 v221, v221\n v_exp_f32 v222, v222\n v_exp_f32 v223, v223\n v_exp_f32 v224, v224\n" \
    "v_exp_f32 v225, v225\n s_waitcnt lgkmcnt(2)\n v_smfmac_f32_32x32x32_bf16 v[168:183], v[240:243], v[216:223], v251\n" \
    "ds_read_b128 v[240:243], v249 offset:2048\n" \
    "buffer_store_dwordx4 %[fill], %[vo], %[rd], %[r4] offen sc1\n" \
    "v_exp_f32 v226, v226\n v_exp_f32 v227, v227\n v_exp_f32 v228, v228\n v_exp_f32 v229, v229\n" \
    "v_exp_f32 v230, v230\n v_exp_f32 v231, v231\n v_exp_f32 v200, v200\n v_exp_f32 v201, v201\n" \
    "s_waitcnt lgkmcnt(1)\n v_smfmac_f32_32x32x32_bf16 v[168:183], v[244:247], v[224:231], v251\n" \
    "v_exp_f32 v202, v202\n v_exp_f32 v203, v203\n v_exp_f32 v204, v204\n v_mfma_f32_32x32x16_bf16 v[216:231], v[236:239], v[232:235], v[184:199]\n" \
    "ds_read_b128 v[236:239], v248 offset:4096\n" \
    "ds_read_b128 v[244:247], v249 offset:2560\n" \
    "buffer_load_dwordx4 %[vo], %[rd], %[ko] offen offset:2048 sc1 lds\n" \
    "v_exp_f32 v205, v205\n v_exp_f32 v206, v206\n v_exp_f32 v207, v207\n v_exp_f32 v208, v208\n" \
    "v_exp_f32 v209, v209\n s_waitcnt lgkmcnt(2)\n v_smfmac_f32_32x32x32_bf16 v[168:183], v[240:243], v[200:207], v251\n" \
    "ds_read_b128 v[240:243], v249 offset:3072\n" \
    "buffer_store_dwordx4 %[fill], %[vo], %[rd], %[r5] offen sc1\n" \
    "v_exp_f32 v210, v210\n v_exp_f32 v211, v211\n v_exp_f32 v212, v212\n v_exp_f32 v213, v213\n" \
    "v_exp_f32 v214, v214\n v_exp_f32 v215, v215\n v_exp_f32 v216, v216\n v_exp_f32 v217, v217\n" \
    "s_waitcnt lgkmcnt(1)\n v_smfmac_f32_32x32x32_bf16 v[168:183], v[244:247], v[208:215], v251\n" \
    "v_exp_f32 v218, v218\n v_exp_f32 v219, v219\n v_exp_f32 v220, v220\n v_mfma_f32_32x32x16_bf16 v[200:215], v[236:239], v[232:235], v[184:199]\n" \
    "ds_read_b128 v[236:239], v248 offset:5120\n" \
    "ds_read_b128 v[244:247], v249 offset:3584\n" \
    "buffer_load_dwordx4 %[vo], %[rd], %[ko] offen offset:3072 sc1 lds\n" \
    "s_add_u32 %[ko], %[ko], 4096\n v_exp_f32 v221, v221\n v_exp_f32 v222, v222\n v_exp_f32 v223, v223\n" \
    "v_exp_f32 v224, v224\n v_exp_f32 v225, v225\n s_waitcnt lgkmcnt(2)\n v_smfmac_f32_32x32x32_bf16 v[168:183], v[240:243], v[216:223], v251\n" \
    "v_add_u32 v249, v249, v250\n v_add_u32 v248, 4096, v248\n ds_read_b128 v[240:243], v249\n" \
    "v_exp_f32 v226, v226\n v_exp_f32 v227, v227\n v_exp_f32 v228, v228\n v_exp_f32 v229, v229\n" \
    "v_exp_f32 v230, v230\n v_exp_f32 v231, v231\n v_exp_f32 v200, v200\n v_exp_f32 v201, v201\n" \
    "s_waitcnt lgkmcnt(1)\n v_smfmac_f32_32x32x32_bf16 v[168:183], v[244:247], v[224:231], v251\n" \
    "v_exp_f32 v202, v202\n v_exp_f32 v203, v203\n v_exp_f32 v204, v204\n v_mfma_f32_32x32x16_bf16 v[216:231], v[236:239], v[232:235], v[184:199]\n" \
    "ds_read_b128 v[236:239], v248 offset:2048\n" \
    "ds_read_b128 v[244:247], v249 offset:512\n" \
    "s_mov_b32 m0, %[lo]\n s_add_u32 %[lo], %[lo], 4096\n buffer_load_dwordx4 %[vo], %[rd], %[ko] offen offset:0 sc1 lds\n" \
    "v_exp_f32 v205, v205\n v_exp_f32 v206, v206\n v_exp_f32 v207, v207\n v_exp_f32 v208, v208\n" \
    "v_exp_f32 v209, v209\n s_waitcnt lgkmcnt(2)\n v_smfmac_f32_32x32x32_bf16 v[168:183], v[240:243], v[200:207], v251\n" \
    "ds_read_b128 v[240:243], v249 offset:1024\n" \
    "v_exp_f32 v210, v210\n v_exp_f32 v211, v211\n v_exp_f32 v212, v212\n v_exp_f32 v213, v213\n" \
    "v_exp_f32 v214, v214\n v_exp_f32 v215, v215\n v_exp_f32 v216, v216\n v_exp_f32 v217, v217\n" \
    "s_waitcnt lgkmcnt(1)\n v_smfmac_f32_32x32x32_bf16 v[168:183], v[244:247], v[208:215], v251\n" \
    "v_exp_f32 v218, v218\n v_exp_f32 v219, v219\n v_exp_f32 v220, v220\n v_mfma_f32_32x32x16_bf16 v[200:215], v[236:239], v[232:235], v[184:199]\n" \
    "ds_read_b128 v[236:239], v248 offset:3072\n" \
    "ds_read_b128 v[244:247], v249 offset:1536\n" \
    "buffer_load_dwordx4 %[vo], %[rd], %[ko] offen offset:1024 sc1 lds\n" \
    "v_exp_f32 v221, v221\n v_exp_f32 v222, v222\n v_exp_f32 v223, v223\n v_exp_f32 v224, v224\n" \
    "v_exp_f32 v225, v225\n s_waitcnt lgkmcnt(2)\n v_smfmac_f32_32x32x32_bf16 v[168:183], v[240:243], v[216:223], v251\n" \
    "ds_read_b128 v[240:243], v249 offset:2048\n" \
    "v_exp_f32 v226, v226\n v_exp_f32 v227, v227\n v_exp_f32 v228, v228\n v_exp_f32 v229, v229\n" \
    "v_exp_f32 v230, v230\n v_exp_f32 v231, v231\n v_exp_f32 v200, v200\n v_exp_f32 v201, v201\n" \
    "s_waitcnt lgkmcnt(1)\n v_smfmac_f32_32x32x32_bf16 v[168:183], v[244:247], v[224:231], v251\n" \
    "v_exp_f32 v202, v202\n v_exp_f32 v203, v203\n v_exp_f32 v204, v204\n v_mfma_f32_32x32x16_bf16 v[216:231], v[236:239], v[232:235], v[184:199]\n" \
    "ds_read_b128 v[236:239], v248 offset:4096\n" \
    "ds_read_b128 v[244:247], v249 offset:2560\n" \
    "buffer_load_dwordx4 %[vo], %[rd], %[ko] offen offset:2048 sc1 lds\n" \
    "v_exp_f32 v205, v205\n v_exp_f32 v206, v206\n v_exp_f32 v207, v207\n v_exp_f32 v208, v208\n" \
    "v_exp_f32 v209, v209\n s_waitcnt lgkmcnt(2)\n v_smfmac_f32_32x32x32_bf16 v[168:183], v[240:243], v[200:207], v251\n" \
    "ds_read_b128 v[240:243], v249 offset:3072\n" \
    "v_exp_f32 v210, v210\n v_exp_f32 v211, v211\n v_exp_f32 v212, v212\n v_exp_f32 v213, v213\n" \
    "v_exp_f32 v214, v214\n v_exp_f32 v215, v215\n v_exp_f32 v216, v216\n v_exp_f32 v217, v217\n" \
    "s_waitcnt lgkmcnt(1)\n v_smfmac_f32_32x32x32_bf16 v[168:183], v[244:247], v[208:215], v251\n" \
    "v_exp_f32 v218, v218\n v_exp_f32 v219, v219\n v_exp_f32 v220, v220\n v_mfma_f32_32x32x16_bf16 v[200:215], v[236:239], v[232:235], v[184:199]\n" \
    "ds_read_b128 v[236:239], v248 offset:5120\n" \
    "ds_read_b128 v[244:247], v249 offset:3584\n" \
    "buffer_load_dwordx4 %[vo], %[rd], %[ko] offen offset:3072 sc1 lds\n" \
    "s_add_u32 %[ko], %[ko], 4096\n v_exp_f32 v221, v221\n v_exp_f32 v222, v222\n v_exp_f32 v223, v223\n" \
    "v_exp_f32 v224, v224\n v_exp_f32 v225, v225\n s_waitcnt lgkmcnt(2)\n v_smfmac_f32_32x32x32_bf16 v[168:183], v[240:243], v[216:223], v251\n" \
    "v_add_u32 v249, v249, v250\n v_add_u32 v248, 4096, v248\n ds_read_b128 v[240:243], v249\n" \
    "v_exp_f32 v226, v226\n v_exp_f32 v227, v227\n v_exp_f32 v228, v228\n v_exp_f32 v229, v229\n" \
    "v_exp_f32 v230, v230\n v_exp_f32 v231, v231\n" \
    ""
#define ATT_WLOAD \
    "v_exp_f32 v200, v200\n v_exp_f32 v201, v201\n s_waitcnt lgkmcnt(1)\n v_smfmac_f32_32x32x32_bf16 v[168:183], v[244:247], v[224:231], v251\n" \
    "v_exp_f32 v202, v202\n v_exp_f32 v203, v203\n v_exp_f32 v204, v204\n v_mfma_f32_32x32x16_bf16 v[216:231], v[236:239], v[232:235], v[184:199]\n" \
    "ds_read_b128 v[236:239], v248 offset:2048\n" \
    "ds_read_b128 v[244:247], v249 offset:512\n" \
    "buffer_load_dwordx4 %[w0], %[vo], %[rd], %[so0] offen offset:0 sc1\n" \
    "v_exp_f32 v205, v205\n v_exp_f32 v206, v206\n v_exp_f32 v207, v207\n v_exp_f32 v208, v208\n" \
    "v_exp_f32 v209, v209\n s_waitcnt lgkmcnt(2)\n v_smfmac_f32_32x32x32_bf16 v[168:183], v[240:243], v[200:207], v251\n" \
    "ds_read_b128 v[240:243], v249 offset:1024\n" \
    "v_exp_f32 v210, v210\n v_exp_f32 v211, v211\n v_exp_f32 v212, v212\n v_exp_f32 v213, v213\n" \
    "v_exp_f32 v214, v214\n v_exp_f32 v215, v215\n v_exp_f32 v216, v216\n v_exp_f32 v217, v217\n" \
    "s_waitcnt lgkmcnt(1)\n v_smfmac_f32_32x32x32_bf16 v[168:183], v[244:247], v[208:215], v251\n" \
    "v_exp_f32 v218, v218\n v_exp_f32 v219, v219\n v_exp_f32 v220, v220\n v_mfma_f32_32x32x16_bf16 v[200:215], v[236:239], v[232:235], v[184:199]\n" \
    "ds_read_b128 v[236:239], v248 offset:3072\n" \
    "ds_read_b128 v[244:247], v249 offset:1536\n" \
    "buffer_load_dwordx4 %[w1], %[vo], %[rd], %[so0] offen offset:1024 sc1\n" \
    "v_exp_f32 v221, v221\n v_exp_f32 v222, v222\n v_exp_f32 v223, v223\n v_exp_f32 v224, v224\n" \
    "v_exp_f32 v225, v225\n s_waitcnt lgkmcnt(2)\n v_smfmac_f32_32x32x32_bf16 v[168:183], v[240:243], v[216:223], v251\n" \
    "ds_read_b128 v[240:243], v249 offset:2048\n" \
    "v_exp_f32 v226, v226\n v_exp_f32 v227, v227\n v_exp_f32 v228, v228\n v_exp_f32 v229, v229\n" \
    "v_exp_f32 v230, v230\n v_exp_f32 v231, v231\n v_exp_f32 v200, v200\n v_exp_f32 v201, v201\n" \
    "s_waitcnt lgkmcnt(1)\n v_smfmac_f32_32x32x32_bf16 v[168:183], v[244:247], v[224:231], v251\n" \
    "v_exp_f32 v202, v202\n v_exp_f32 v203, v203\n v_exp_f32 v204, v204\n v_mfma_f32_32x32x16_bf16 v[216:231], v[236:239], v[232:235], v[184:199]\n" \
    "ds_read_b128 v[236:239], v248 offset:4096\n" \
    "ds_read_b128 v[244:247], v249 offset:2560\n" \
    "buffer_load_dwordx4 %[w2], %[vo], %[rd], %[so0] offen offset:2048 sc1\n" \
    "v_exp_f32 v205, v205\n v_exp_f32 v206, v206\n v_exp_f32 v207, v207\n v_exp_f32 v208, v208\n" \
    "v_exp_f32 v209, v209\n s_waitcnt lgkmcnt(2)\n v_smfmac_f32_32x32x32_bf16 v[168:183], v[240:243], v[200:207], v251\n" \
    "ds_read_b128 v[240:243], v249 offset:3072\n" \
    "v_exp_f32 v210, v210\n v_exp_f32 v211, v211\n v_exp_f32 v212, v212\n v_exp_f32 v213, v213\n" \
    "v_exp_f32 v214, v214\n v_exp_f32 v215, v215\n v_exp_f32 v216, v216\n v_exp_f32 v217, v217\n" \
    "s_waitcnt lgkmcnt(1)\n v_smfmac_f32_32x32x32_bf16 v[168:183], v[244:247], v[208:215], v251\n" \
    "v_exp_f32 v218, v218\n v_exp_f32 v219, v219\n v_exp_f32 v220, v220\n v_mfma_f32_32x32x16_bf16 v[200:215], v[236:239], v[232:235], v[184:199]\n" \
    "ds_read_b128 v[236:239], v248 offset:5120\n" \
    "ds_read_b128 v[244:247], v249 offset:3584\n" \
    "buffer_load_dwordx4 %[w3], %[vo], %[rd], %[so0] offen offset:3072 sc1\n" \
    "v_exp_f32 v221, v221\n v_exp_f32 v222, v222\n v_exp_f32 v223, v223\n v_exp_f32 v224, v224\n" \
    "v_exp_f32 v225, v225\n s_waitcnt lgkmcnt(2)\n v_smfmac_f32_32x32x32_bf16 v[168:183], v[240:243], v[216:223], v251\n" \
    "v_add_u32 v249, v249, v250\n v_add_u32 v248, 4096, v248\n ds_read_b128 v[240:243], v249\n" \
    "v_exp_f32 v226, v226\n v_exp_f32 v227, v227\n v_exp_f32 v228, v228\n v_exp_f32 v229, v229\n" \
    "v_exp_f32 v230, v230\n v_exp_f32 v231, v231\n v_exp_f32 v200, v200\n v_exp_f32 v201, v201\n" \
    "s_waitcnt lgkmcnt(1)\n v_smfmac_f32_32x32x32_bf16 v[168:183], v[244:247], v[224:231], v251\n" \
    "v_exp_f32 v202, v202\n v_exp_f32 v203, v203\n v_exp_f32 v204, v204\n v_mfma_f32_32x32x16_bf16 v[216:231], v[236:239], v[232:235], v[184:199]\n" \
    "ds_read_b128 v[236:239], v248 offset:2048\n" \
    "ds_read_b128 v[244:247], v249 offset:512\n" \
    "buffer_load_dwordx4 %[w4], %[vo], %[rd], %[so1] offen offset:0 sc1\n" \
    "v_exp_f32 v205, v205\n v_exp_f32 v206, v206\n v_exp_f32 v207, v207\n v_exp_f32 v208, v208\n" \
    "v_exp_f32 v209, v209\n s_waitcnt lgkmcnt(2)\n v_smfmac_f32_32x32x32_bf16 v[168:183], v[240:243], v[200:207], v251\n" \
    "ds_read_b128 v[240:243], v249 offset:1024\n" \
    "v_exp_f32 v210, v210\n v_exp_f32 v211, v211\n v_exp_f32 v212, v212\n v_exp_f32 v213, v213\n" \
    "v_exp_f32 v214, v214\n v_exp_f32 v215, v215\n v_exp_f32 v216, v216\n v_exp_f32 v217, v217\n" \
    "s_waitcnt lgkmcnt(1)\n v_smfmac_f32_32x32x32_bf16 v[168:183], v[244:247], v[208:215], v251\n" \
    "v_exp_f32 v218, v218\n v_exp_f32 v219, v219\n v_exp_f32 v220, v220\n v_mfma_f32_32x32x16_bf16 v[200:215], v[236:239], v[232:235], v[184:199]\n" \
    "ds_read_b128 v[236:239], v248 offset:3072\n" \
    "ds_read_b128 v[244:247], v249 offset:1536\n" \
    "buffer_load_dwordx4 %[w5], %[vo], %[rd], %[so1] offen offset:1024 sc1\n" \
    "v_exp_f32 v221, v221\n v_exp_f32 v222, v222\n v_exp_f32 v223, v223\n v_exp_f32 v224, v224\n" \
    "v_exp_f32 v225, v225\n s_waitcnt lgkmcnt(2)\n v_smfmac_f32_32x32x32_bf16 v[168:183], v[240:243], v[216:223], v251\n" \
    "ds_read_b128 v[240:243], v249 offset:2048\n" \
    "v_exp_f32 v226, v226\n v_exp_f32 v227, v227\n v_exp_f32 v228, v228\n v_exp_f32 v229, v229\n" \
    "v_exp_f32 v230, v230\n v_exp_f32 v231, v231\n v_exp_f32 v200, v200\n v_exp_f32 v201, v201\n" \
    "s_waitcnt lgkmcnt(1)\n v_smfmac_f32_32x32x32_bf16 v[168:183], v[244:247], v[224:231], v251\n" \
    "v_exp_f32 v202, v202\n v_exp_f32 v203, v203\n v_exp_f32 v204, v204\n v_mfma_f32_32x32x16_bf16 v[216:231], v[236:239], v[232:235], v[184:199]\n" \
    "ds_read_b128 v[236:239], v248 offset:4096\n" \
    "ds_read_b128 v[244:247], v249 offset:2560\n" \
    "buffer_load_dwordx4 %[w6], %[vo], %[rd], %[so1] offen offset:2048 sc1\n" \
    "v_exp_f32 v205, v205\n v_exp_f32 v206, v206\n v_exp_f32 v207, v207\n v_exp_f32 v208, v208\n" \
    "v_exp_f32 v209, v209\n s_waitcnt lgkmcnt(2)\n v_smfmac_f32_32x32x32_bf16 v[168:183], v[240:243], v[200:207], v251\n" \
    "ds_read_b128 v[240:243], v249 offset:3072\n" \
    "v_exp_f32 v210, v210\n v_exp_f32 v211, v211\n v_exp_f32 v212, v212\n v_exp_f32 v213, v213\n" \
    "v_exp_f32 v214, v214\n v_exp_f32 v215, v215\n v_exp_f32 v216, v216\n v_exp_f32 v217, v217\n" \
    "s_waitcnt lgkmcnt(1)\n v_smfmac_f32_32x32x32_bf16 v[168:183], v[244:247], v[208:215], v251\n" \
    "v_exp_f32 v218, v218\n v_exp_f32 v219, v219\n v_exp_f32 v220, v220\n v_mfma_f32_32x32x16_bf16 v[200:215], v[236:239], v[232:235], v[184:199]\n" \
    "ds_read_b128 v[236:239], v248 offset:5120\n" \
    "ds_read_b128 v[244:247], v249 offset:3584\n" \
    "buffer_load_dwordx4 %[w7], %[vo], %[rd], %[so1] offen offset:3072 sc1\n" \
    "v_exp_f32 v221, v221\n v_exp_f32 v222, v222\n v_exp_f32 v223, v223\n v_exp_f32 v224, v224\n" \
    "v_exp_f32 v225, v225\n s_waitcnt lgkmcnt(2)\n v_smfmac_f32_32x32x32_bf16 v[168:183], v[240:243], v[216:223], v251\n" \
    "v_add_u32 v249, v249, v250\n v_add_u32 v248, 4096, v248\n ds_read_b128 v[240:243], v249\n" \
    "v_exp_f32 v226, v226\n v_exp_f32 v227, v227\n v_exp_f32 v228, v228\n v_exp_f32 v229, v229\n" \
    "v_exp_f32 v230, v230\n v_exp_f32 v231, v231\n" \
    ""
#define ATT_WIN \
    "ds_read_b128 v[236:239], v248\n" \
    "s_waitcnt lgkmcnt(0)\n v_mfma_f32_32x32x16_bf16 v[200:215], v[236:239], v[232:235], v[216:231]\n" \
    "ds_read_b128 v[236:239], v248 offset:8192\n" \
    "ds_read_b128 v[240:243], v249\n" \
    "s_nop 7\n s_nop 3\n v_exp_f32 v200, v200\n v_exp_f32 v201, v201\n" \
    "s_cmp_lg_u32 %[j0], 1\n s_cbranch_scc1 1f\n v_mov_b32 v184, %[nm]\n v_mov_b32 v185, %[nm]\n" \
    "v_mov_b32 v186, %[nm]\n v_mov_b32 v187, %[nm]\n v_mov_b32 v188, %[nm]\n v_mov_b32 v189, %[nm]\n" \
    "v_mov_b32 v190, %[nm]\n v_mov_b32 v191, %[nm]\n v_mov_b32 v192, %[nm]\n v_mov_b32 v193, %[nm]\n" \
    "v_mov_b32 v194, %[nm]\n v_mov_b32 v195, %[nm]\n v_mov_b32 v196, %[nm]\n v_mov_b32 v197, %[nm]\n" \
    "v_mov_b32 v198, %[nm]\n v_mov_b32 v199, %[nm]\n 1:\n v_exp_f32 v202, v202\n" \
    "v_exp_f32 v203, v203\n v_exp_f32 v204, v204\n s_waitcnt lgkmcnt(1)\n v_mfma_f32_32x32x16_bf16 v[216:231], v[236:239], v[232:235], v[184:199]\n" \
    "ds_read_b128 v[236:239], v248 offset:16384\n" \
    "ds_read_b128 v[244:247], v249 offset:512\n" \
    "v_exp_f32 v205, v205\n v_exp_f32 v206, v206\n v_exp_f32 v207, v207\n v_exp_f32 v208, v208\n" \
    "v_exp_f32 v209, v209\n s_waitcnt lgkmcnt(2)\n v_smfmac_f32_32x32x32_bf16 v[168:183], v[240:243], v[200:207], v251\n" \
    "v_add_u32 v249, v249, v250\n ds_read_b128 v[240:243], v249\n" \
    "v_exp_f32 v210, v210\n v_exp_f32 v211, v211\n v_exp_f32 v212, v212\n v_exp_f32 v213, v213\n" \
    "v_exp_f32 v214, v214\n v_exp_f32 v215, v215\n v_exp_f32 v216, v216\n v_exp_f32 v217, v217\n" \
    "s_waitcnt lgkmcnt(1)\n v_smfmac_f32_32x32x32_bf16 v[168:183], v[244:247], v[208:215], v251\n" \
    "s_cmp_lg_u32 %[j0], 2\n s_cbranch_scc1 2f\n v_mov_b32 v184, %[nm]\n v_mov_b32 v185, %[nm]\n" \
    "v_mov_b32 v186, %[nm]\n v_mov_b32 v187, %[nm]\n v_mov_b32 v188, %[nm]\n v_mov_b32 v189, %[nm]\n" \
    "v_mov_b32 v190, %[nm]\n v_mov_b32 v191, %[nm]\n v_mov_b32 v192, %[nm]\n v_mov_b32 v193, %[nm]\n" \
    "v_mov_b32 v194, %[nm]\n v_mov_b32 v195, %[nm]\n v_mov_b32 v196, %[nm]\n v_mov_b32 v197, %[nm]\n" \
    "v_mov_b32 v198, %[nm]\n v_mov_b32 v199, %[nm]\n 2:\n v_exp_f32 v218, v218\n" \
    "v_exp_f32 v219, v219\n v_exp_f32 v220, v220\n v_mfma_f32_32x32x16_bf16 v[200:215], v[236:239], v[232:235], v[184:199]\n" \
    "ds_read_b128 v[236:239], v248 offset:24576\n" \
    "ds_read_b128 v[244:247], v249 offset:512\n" \
    "v_exp_f32 v221, v221\n v_exp_f32 v222, v222\n v_exp_f32 v223, v223\n v_exp_f32 v224, v224\n" \
    "v_exp_f32 v225, v225\n s_waitcnt lgkmcnt(2)\n v_smfmac_f32_32x32x32_bf16 v[168:183], v[240:243], v[216:223], v251\n" \
    "v_add_u32 v249, v249, v250\n ds_read_b128 v[240:243], v249\n" \
    "v_exp_f32 v226, v226\n v_exp_f32 v227, v227\n v_exp_f32 v228, v228\n v_exp_f32 v229, v229\n" \
    "v_exp_f32 v230, v230\n v_exp_f32 v231, v231\n v_exp_f32 v200, v200\n v_exp_f32 v201, v201\n" \
    "s_waitcnt lgkmcnt(1)\n v_smfmac_f32_32x32x32_bf16 v[168:183], v[244:247], v[224:231], v251\n" \
    "s_cmp_lg_u32 %[j0], 3\n s_cbranch_scc1 3f\n v_mov_b32 v184, %[nm]\n v_mov_b32 v185, %[nm]\n" \
    "v_mov_b32 v186, %[nm]\n v_mov_b32 v187, %[nm]\n v_mov_b32 v188, %[nm]\n v_mov_b32 v189, %[nm]\n" \
    "v_mov_b32 v190, %[nm]\n v_mov_b32 v191, %[nm]\n v_mov_b32 v192, %[nm]\n v_mov_b32 v193, %[nm]\n" \
    "v_mov_b32 v194, %[nm]\n v_mov_b32 v195, %[nm]\n v_mov_b32 v196, %[nm]\n v_mov_b32 v197, %[nm]\n" \
    "v_mov_b32 v198, %[nm]\n v_mov_b32 v199, %[nm]\n 3:\n v_exp_f32 v202, v202\n" \
    "v_exp_f32 v203, v203\n v_exp_f32 v204, v204\n v_mfma_f32_32x32x16_bf16 v[216:231], v[236:239], v[232:235], v[184:199]\n" \
    "ds_read_b128 v[236:239], v248 offset:32768\n" \
    "ds_read_b128 v[244:247], v249 offset:512\n" \
    "v_exp_f32 v205, v205\n v_exp_f32 v206, v206\n v_exp_f32 v207, v207\n v_exp_f32 v208, v208\n" \
    "v_exp_f32 v209, v209\n s_waitcnt lgkmcnt(2)\n v_smfmac_f32_32x32x32_bf16 v[168:183], v[240:243], v[200:207], v251\n" \
    "v_add_u32 v249, v249, v250\n ds_read_b128 v[240:243], v249\n" \
    "v_exp_f32 v210, v210\n v_exp_f32 v211, v211\n v_exp_f32 v212, v212\n v_exp_f32 v213, v213\n" \
    "v_exp_f32 v214, v214\n v_exp_f32 v215, v215\n v_exp_f32 v216, v216\n v_exp_f32 v217, v217\n" \
    "s_waitcnt lgkmcnt(1)\n v_smfmac_f32_32x32x32_bf16 v[168:183], v[244:247], v[208:215], v251\n" \
    "s_cmp_lg_u32 %[j0], 4\n s_cbranch_scc1 4f\n v_mov_b32 v184, %[nm]\n v_mov_b32 v185, %[nm]\n" \
    "v_mov_b32 v186, %[nm]\n v_mov_b32 v187, %[nm]\n v_mov_b32 v188, %[nm]\n v_mov_b32 v189, %[nm]\n" \
    "v_mov_b32 v190, %[nm]\n v_mov_b32 v191, %[nm]\n v_mov_b32 v192, %[nm]\n v_mov_b32 v193, %[nm]\n" \
    "v_mov_b32 v194, %[nm]\n v_mov_b32 v195, %[nm]\n v_mov_b32 v196, %[nm]\n v_mov_b32 v197, %[nm]\n" \
    "v_mov_b32 v198, %[nm]\n v_mov_b32 v199, %[nm]\n 4:\n v_exp_f32 v218, v218\n" \
    "v_exp_f32 v219, v219\n v_exp_f32 v220, v220\n v_mfma_f32_32x32x16_bf16 v[200:215], v[236:239], v[232:235], v[184:199]\n" \
    "ds_read_b128 v[236:239], v248 offset:40960\n" \
    "ds_read_b128 v[244:247], v249 offset:512\n" \
    "v_exp_f32 v221, v221\n v_exp_f32 v222, v222\n v_exp_f32 v223, v223\n v_exp_f32 v224, v224\n" \
    "v_exp_f32 v225, v225\n s_waitcnt lgkmcnt(2)\n v_smfmac_f32_32x32x32_bf16 v[168:183], v[240:243], v[216:223], v251\n" \
    "v_add_u32 v249, v249, v250\n ds_read_b128 v[240:243], v249\n" \
    "v_exp_f32 v226, v226\n v_exp_f32 v227, v227\n v_exp_f32 v228, v228\n v_exp_f32 v229, v229\n" \
    "v_exp_f32 v230, v230\n v_exp_f32 v231, v231\n v_exp_f32 v200, v200\n v_exp_f32 v201, v201\n" \
    "s_waitcnt lgkmcnt(1)\n v_smfmac_f32_32x32x32_bf16 v[168:183], v[244:247], v[224:231], v251\n" \
    "s_cmp_lg_u32 %[j0], 5\n s_cbranch_scc1 5f\n v_mov_b32 v184, %[nm]\n v_mov_b32 v185, %[nm]\n" \
    "v_mov_b32 v186, %[nm]\n v_mov_b32 v187, %[nm]\n v_mov_b32 v188, %[nm]\n v_mov_b32 v189, %[nm]\n" \
    "v_mov_b32 v190, %[nm]\n v_mov_b32 v191, %[nm]\n v_mov_b32 v192, %[nm]\n v_mov_b32 v193, %[nm]\n" \
    "v_mov_b32 v194, %[nm]\n v_mov_b32 v195, %[nm]\n v_mov_b32 v196, %[nm]\n v_mov_b32 v197, %[nm]\n" \
    "v_mov_b32 v198, %[nm]\n v_mov_b32 v199, %[nm]\n 5:\n v_exp_f32 v202, v202\n" \
    "v_exp_f32 v203, v203\n v_exp_f32 v204, v204\n v_mfma_f32_32x32x16_bf16 v[216:231], v[236:239], v[232:235], v[184:199]\n" \
    "ds_read_b128 v[236:239], v248 offset:49152\n" \
    "ds_read_b128 v[244:247], v249 offset:512\n" \
    "v_exp_f32 v205, v205\n v_exp_f32 v206, v206\n v_exp_f32 v207, v207\n v_exp_f32 v208, v208\n" \
    "v_exp_f32 v209, v209\n s_waitcnt lgkmcnt(2)\n v_smfmac_f32_32x32x32_bf16 v[168:183], v[240:243], v[200:207], v251\n" \
    "v_add_u32 v249, v249, v250\n ds_read_b128 v[240:243], v249\n" \
    "v_exp_f32 v210, v210\n v_exp_f32 v211, v211\n v_exp_f32 v212, v212\n v_exp_f32 v213, v213\n" \
    "v_exp_f32 v214, v214\n v_exp_f32 v215, v215\n v_exp_f32 v216, v216\n v_exp_f32 v217, v217\n" \
    "s_waitcnt lgkmcnt(1)\n v_smfmac_f32_32x32x32_bf16 v[168:183], v[244:247], v[208:215], v251\n" \
    "s_cmp_lg_u32 %[j0], 6\n s_cbranch_scc1 6f\n v_mov_b32 v184, %[nm]\n v_mov_b32 v185, %[nm]\n" \
    "v_mov_b32 v186, %[nm]\n v_mov_b32 v187, %[nm]\n v_mov_b32 v188, %[nm]\n v_mov_b32 v189, %[nm]\n" \
    "v_mov_b32 v190, %[nm]\n v_mov_b32 v191, %[nm]\n v_mov_b32 v192, %[nm]\n v_mov_b32 v193, %[nm]\n" \
    "v_mov_b32 v194, %[nm]\n v_mov_b32 v195, %[nm]\n v_mov_b32 v196, %[nm]\n v_mov_b32 v197, %[nm]\n" \
    "v_mov_b32 v198, %[nm]\n v_mov_b32 v199, %[nm]\n 6:\n v_exp_f32 v218, v218\n" \
    "v_exp_f32 v219, v219\n v_exp_f32 v220, v220\n v_mfma_f32_32x32x16_bf16 v[200:215], v[236:239], v[232:235], v[184:199]\n" \
    "ds_read_b128 v[236:239], v248 offset:57344\n" \
    "ds_read_b128 v[244:247], v249 offset:512\n" \
    "v_exp_f32 v221, v221\n v_exp_f32 v222, v222\n v_exp_f32 v223, v223\n v_exp_f32 v224, v224\n" \
    "v_exp_f32 v225, v225\n s_waitcnt lgkmcnt(2)\n v_smfmac_f32_32x32x32_bf16 v[168:183], v[240:243], v[216:223], v251\n" \
    "v_add_u32 v249, v249, v250\n ds_read_b128 v[240:243], v249\n" \
    "v_exp_f32 v226, v226\n v_exp_f32 v227, v227\n v_exp_f32 v228, v228\n v_exp_f32 v229, v229\n" \
    "v_exp_f32 v230, v230\n v_exp_f32 v231, v231\n v_exp_f32 v200, v200\n v_exp_f32 v201, v201\n" \
    "s_waitcnt lgkmcnt(1)\n v_smfmac_f32_32x32x32_bf16 v[168:183], v[244:247], v[224:231], v251\n" \
    "s_cmp_lg_u32 %[j0], 7\n s_cbranch_scc1 7f\n v_mov_b32 v184, %[nm]\n v_mov_b32 v185, %[nm]\n" \
    "v_mov_b32 v186, %[nm]\n v_mov_b32 v187, %[nm]\n v_mov_b32 v188, %[nm]\n v_mov_b32 v189, %[nm]\n" \
    "v_mov_b32 v190, %[nm]\n v_mov_b32 v191, %[nm]\n v_mov_b32 v192, %[nm]\n v_mov_b32 v193, %[nm]\n" \
    "v_mov_b32 v194, %[nm]\n v_mov_b32 v195, %[nm]\n v_mov_b32 v196, %[nm]\n v_mov_b32 v197, %[nm]\n" \
    "v_mov_b32 v198, %[nm]\n v_mov_b32 v199, %[nm]\n 7:\n v_exp_f32 v202, v202\n" \
    "v_exp_f32 v203, v203\n v_exp_f32 v204, v204\n v_mfma_f32_32x32x16_bf16 v[216:231], v[236:239], v[232:235], v[184:199]\n" \
    "ds_read_b128 v[236:239], %[k8]\n" \
    "ds_read_b128 v[244:247], v249 offset:512\n" \
    "v_exp_f32 v205, v205\n v_exp_f32 v206, v206\n v_exp_f32 v207, v207\n v_exp_f32 v208, v208\n" \
    "v_exp_f32 v209, v209\n s_waitcnt lgkmcnt(2)\n v_smfmac_f32_32x32x32_bf16 v[168:183], v[240:243], v[200:207], v251\n" \
    "v_add_u32 v249, v249, v250\n ds_read_b128 v[240:243], v249\n" \
    "s_mov_b32 vcc_lo, 0xffffffff\n s_mov_b32 vcc_hi, 0xffffff00\n v_cndmask_b32 v184, %[big], %[nm], vcc\n s_mov_b32 vcc_lo, 0xfffffffe\n" \
    "s_mov_b32 vcc_hi, 0xfffffe00\n v_cndmask_b32 v185, %[big], %[nm], vcc\n s_mov_b32 vcc_lo, 0xfffffffc\n s_mov_b32 vcc_hi, 0xfffffc00\n" \
    "v_cndmask_b32 v186, %[big], %[nm], vcc\n s_mov_b32 vcc_lo, 0xfffffff8\n s_mov_b32 vcc_hi, 0xfffff800\n v_cndmask_b32 v187, %[big], %[nm], vcc\n" \
    "s_mov_b32 vcc_lo, 0xfffffff0\n s_mov_b32 vcc_hi, 0xfffff000\n v_cndmask_b32 v188, %[big], %[nm], vcc\n s_mov_b32 vcc_lo, 0xffffffe0\n" \
    "s_mov_b32 vcc_hi, 0xffffe000\n v_cndmask_b32 v189, %[big], %[nm], vcc\n s_mov_b32 vcc_lo, 0xffffffc0\n s_mov_b32 vcc_hi, 0xffffc000\n" \
    "v_cndmask_b32 v190, %[big], %[nm], vcc\n s_mov_b32 vcc_lo, 0xffffff80\n s_mov_b32 vcc_hi, 0xffff8000\n v_cndmask_b32 v191, %[big], %[nm], vcc\n" \
    "s_mov_b32 vcc_lo, 0xffff0000\n s_mov_b32 vcc_hi, 0xff000000\n v_cndmask_b32 v192, %[big], %[nm], vcc\n s_mov_b32 vcc_lo, 0xfffe0000\n" \
    "s_mov_b32 vcc_hi, 0xfe000000\n v_cndmask_b32 v193, %[big], %[nm], vcc\n s_mov_b32 vcc_lo, 0xfffc0000\n s_mov_b32 vcc_hi, 0xfc000000\n" \
    "v_cndmask_b32 v194, %[big], %[nm], vcc\n s_mov_b32 vcc_lo, 0xfff80000\n s_mov_b32 vcc_hi, 0xf8000000\n v_cndmask_b32 v195, %[big], %[nm], vcc\n" \
    "s_mov_b32 vcc_lo, 0xfff00000\n s_mov_b32 vcc_hi, 0xf0000000\n v_cndmask_b32 v196, %[big], %[nm], vcc\n s_mov_b32 vcc_lo, 0xffe00000\n" \
    "s_mov_b32 vcc_hi, 0xe0000000\n v_cndmask_b32 v197, %[big], %[nm], vcc\n s_mov_b32 vcc_lo, 0xffc00000\n s_mov_b32 vcc_hi, 0xc0000000\n" \
    "v_cndmask_b32 v198, %[big], %[nm], vcc\n s_mov_b32 vcc_lo, 0xff800000\n s_mov_b32 vcc_hi, 0x80000000\n v_cndmask_b32 v199, %[big], %[nm], vcc\n" \
    "v_exp_f32 v210, v210\n v_exp_f32 v211, v211\n v_exp_f32 v212, v212\n v_exp_f32 v213, v213\n" \
    "v_exp_f32 v214, v214\n v_exp_f32 v215, v215\n v_exp_f32 v216, v216\n v_exp_f32 v217, v217\n" \
    "s_waitcnt lgkmcnt(1)\n v_smfmac_f32_32x32x32_bf16 v[168:183], v[244:247], v[208:215], v251\n" \
    "v_exp_f32 v218, v218\n v_exp_f32 v219, v219\n v_exp_f32 v220, v220\n v_mfma_f32_32x32x16_bf16 v[200:215], v[236:239], v[232:235], v[184:199]\n" \
    "ds_read_b128 v[244:247], v249 offset:512\n" \
    "v_exp_f32 v221, v221\n v_exp_f32 v222, v222\n v_exp_f32 v223, v223\n v_exp_f32 v224, v224\n" \
    "v_exp_f32 v225, v225\n s_waitcnt lgkmcnt(1)\n v_smfmac_f32_32x32x32_bf16 v[168:183], v[240:243], v[216:223], v251\n" \
    "v_add_u32 v249, v249, v250\n ds_read_b128 v[240:243], v249\n" \
    "v_exp_f32 v226, v226\n v_exp_f32 v227, v227\n v_exp_f32 v228, v228\n v_exp_f32 v229, v229\n" \
    "v_exp_f32 v230, v230\n v_exp_f32 v231, v231\n v_exp_f32 v200, v200\n v_exp_f32 v201, v201\n" \
    "s_waitcnt lgkmcnt(1)\n v_smfmac_f32_32x32x32_bf16 v[168:183], v[244:247], v[224:231], v251\n" \
    "v_exp_f32 v202, v202\n v_exp_f32 v203, v203\n v_exp_f32 v204, v204\n ds_read_b128 v[244:247], v249 offset:512\n" \
    "v_exp_f32 v205, v205\n v_exp_f32 v206, v206\n v_exp_f32 v207, v207\n v_exp_f32 v208, v208\n" \
    "v_exp_f32 v209, v209\n s_waitcnt lgkmcnt(1)\n v_smfmac_f32_32x32x32_bf16 v[168:183], v[240:243], v[200:207], v251\n" \
    "v_exp_f32 v210, v210\n v_exp_f32 v211, v211\n v_exp_f32 v212, v212\n v_exp_f32 v213, v213\n" \
    "v_exp_f32 v214, v214\n v_exp_f32 v215, v215\n s_waitcnt lgkmcnt(0)\n s_nop 0\n" \
    "v_smfmac_f32_32x32x32_bf16 v[168:183], v[244:247], v[208:215], v251\n" \
    "s_nop 7\n s_nop 7\n" \
    ""
struct AttState { f32x16 o, c, a, b; u32x4 q, k, v0, v1; unsigned ka, va, vs, idx; };
#define ATT_STATE_OPS(s) "+{v[168:183]}"(s.o), "+{v[200:215]}"(s.a), "+{v[216:231]}"(s.b), "+{v[236:239]}"(s.k), "+{v[240:243]}"(s.v0), "+{v[244:247]}"(s.v1), "+{v248}"(s.ka), "+{v249}"(s.va)
#define ATT_CONST_OPS(s) "{v[184:199]}"(s.c), "{v[232:235]}"(s.q), "{v250}"(s.vs), "{v251}"(s.idx)
DEV void att_begin(AttState& s) {
    asm volatile("ds_read_b128 v[236:239], v248\n\ts_waitcnt lgkmcnt(0)\n\t"
                 "v_mfma_f32_32x32x16_bf16 v[200:215], v[236:239], v[232:235], v[184:199]\n\t"
                 "ds_read_b128 v[236:239], v248 offset:1024\n\tds_read_b128 v[244:247], v249 offset:512\n\tds_read_b128 v[240:243], v249\n\t"
                 "s_waitcnt lgkmcnt(0)\n\ts_nop 7\n\ts_nop 7"
                 : ATT_STATE_OPS(s) : ATT_CONST_OPS(s) : "memory");
}
DEV void att_run(AttState& s, int groups) {
    int n = groups;
    asm volatile("s_nop 1\n1:\n" ATT_BODY "s_sub_u32 %8, %8, 1\n\ts_cmp_lg_u32 %8, 0\n\ts_cbranch_scc1 1b\n\ts_waitcnt lgkmcnt(0)\n\ts_nop 7\n\ts_nop 7"
                 : ATT_STATE_OPS(s), "+s"(n) : ATT_CONST_OPS(s) : "scc", "memory");
}
DEV void att_first(AttState& s, unsigned vo, u32x4 rd, int ko, unsigned lo, u32x4 fill, int r0, int r1, int r2, int r3, int r4, int r5) {
    unsigned keep; ko = UNI(ko); lo = UNI(lo);
    asm volatile("s_mov_b32 %[keep], m0\n\ts_nop 3\n" ATT_FIRST "s_mov_b32 m0, %[keep]\n\ts_waitcnt lgkmcnt(0)\n\ts_nop 7\n\ts_nop 7"
                 : ATT_STATE_OPS(s), [keep] "=&s"(keep), [ko] "+s"(ko), [lo] "+s"(lo)
                 : ATT_CONST_OPS(s), [vo] "v"(vo), [rd] "s"(rd), [fill] "v"(fill), [r0] "s"(UNI(r0)), [r1] "s"(UNI(r1)), [r2] "s"(UNI(r2)), [r3] "s"(UNI(r3)), [r4] "s"(UNI(r4)), [r5] "s"(UNI(r5))
                 : "scc", "memory");
}
DEV void att_wload(AttState& s, u32x4* W, unsigned vo, u32x4 rd, int so) {
    asm volatile("s_nop 4\n" ATT_WLOAD "s_waitcnt lgkmcnt(0)\n\ts_nop 7\n\ts_nop 7"
                 : ATT_STATE_OPS(s), [w0] "=&v"(W[0]), [w1] "=&v"(W[1]), [w2] "=&v"(W[2]), [w3] "=&v"(W[3]), [w4] "=&v"(W[4]), [w5] "=&v"(W[5]), [w6] "=&v"(W[6]), [w7] "=&v"(W[7])
                 : ATT_CONST_OPS(s), [vo] "v"(vo), [rd] "s"(rd), [so0] "s"(UNI(so)), [so1] "s"(UNI(so + 4096)) : "memory");
}
DEV void att_window(AttState& s, float big, float nm, unsigned k8, int j0) {
    asm volatile("s_nop 1\n" ATT_WIN : ATT_STATE_OPS(s), "+{v[184:199]}"(s.c)
                 : "{v[232:235]}"(s.q), "{v250}"(s.vs), "{v251}"(s.idx), [big] "v"(big), [nm] "v"(nm), [k8] "v"(k8), [j0] "s"(UNI(j0)) : "vcc", "scc", "memory");
}
DEV void att_end(AttState& s) {
    asm volatile("s_nop 1\n\tv_smfmac_f32_32x32x32_bf16 v[168:183], v[244:247], v[224:231], v251\n\ts_nop 7\n\ts_nop 7" : ATT_STATE_OPS(s) : ATT_CONST_OPS(s) : "memory");
}
DEV f32x16 splat16(float v) { f32x16 r; for (int i = 0; i < 16; ++i) r[i] = v; return r; }
DEV float rowmax16(f32x16 s, float m) { for (int i = 0; i < 16; ++i) m = fmaxf(m, s[i]); return m; }
DEV u32x4 norm_pack(f32x16 o) { const float r = __builtin_amdgcn_rcpf(o[8]); return (u32x4){pks(o[0] * r, o[1] * r), pks(o[2] * r, o[3] * r), pks(o[4] * r, o[5] * r), pks(o[6] * r, o[7] * r)}; }
constexpr unsigned long long diag_mask(int r) {
    unsigned long long m = 0;
    for (int l = 0; l < 64; ++l) { const int k = (r & 3) + 8 * (r >> 2) + 4 * (l >> 5), key = (k & 0x13) | ((k & 4) << 1) | ((k & 8) >> 1); if (key <= (l & 31)) m |= 1ull << l; }
    return m;
}
template <int R> DEV void mask_fill(f32x16& c, float on, float off) { c[R] = __builtin_amdgcn_inverse_ballot_w64(diag_mask(R)) ? on : off; if constexpr (R < 15) mask_fill<R + 1>(c, on, off); }

DEV void fold_t(const f32x16& acc, const f32x16& cs, float mean, float scale, u32x4& f0, u32x4& f1) {
    float y[16];
#pragma unroll
    for (int r = 0; r < 16; ++r) y[r] = (acc[r] - mean * cs[r]) * scale;
    f0 = (u32x4){pks(y[0], y[1]), pks(y[2], y[3]), pks(y[4], y[5]), pks(y[6], y[7])};
    f1 = (u32x4){pks(y[8], y[9]), pks(y[10], y[11]), pks(y[12], y[13]), pks(y[14], y[15])};
}
struct RowStats { f32x4 m[4], r[4]; };
DEV void row_stats(RowStats& st, lptr lds, int tb, int h) {
#pragma unroll
    for (int g = 0; g < 4; ++g) { st.m[g] = ldsrf(lds + L_MEAN + (tb * 32 + 4 * h) * 4 + g * 32); st.r[g] = ldsrf(lds + L_RSTD + (tb * 32 + 4 * h) * 4 + g * 32); }
}
DEV void fold_n(const f32x16& acc, float cs, const RowStats& st, float (&y)[16]) {
#pragma unroll
    for (int g = 0; g < 4; ++g) {
#pragma unroll
        for (int e = 0; e < 4; ++e) y[4 * g + e] = (acc[4 * g + e] - st.m[g][e] * cs) * st.r[g][e];
    }
}

DEV void kv_partial(const f32x16& acc, const f32x16& cs, bool isV, int tb, lptr lds, lptr red, int lane) {
    const int n = lane & 31, h = lane >> 5;
    if (!isV) {
        const float mean = *(LAS float*)(lds + L_MEAN + (tb * 32 + n) * 4), rstd = *(LAS float*)(lds + L_RSTD + (tb * 32 + n) * 4);
        f32x4 a, b;
#pragma unroll
        for (int e = 0; e < 4; ++e) { a[e] = (acc[e] - mean * cs[e]) * rstd; b[e] = (acc[4 + e] - mean * cs[4 + e]) * rstd; }
        lptr p = red + (h * 32 + slot_of(n)) * 32;
        *(LAS f32x4*)p = a; *(LAS f32x4*)(p + 16) = b;
    } else {
        RowStats st; row_stats(st, lds, tb, h);
        float y[16]; fold_n(acc, cs[0], st, y);
        {
#pragma unroll
            for (int s = 0; s < 2; ++s) { lptr p = red + ((s * 2 + h) * 16 + (n & 15)) * 32;
                *(LAS f32x4*)p = (f32x4){y[8 * s], y[8 * s + 1], y[8 * s + 2], y[8 * s + 3]}; *(LAS f32x4*)(p + 16) = (f32x4){y[8 * s + 4], y[8 * s + 5], y[8 * s + 6], y[8 * s + 7]}; }
        }
    }
}
DEV float kv_final(lptr red0, rsrc_t rw, int dst, int lane) {
    f32x4 a = ldsrf(red0 + lane * 32), b = ldsrf(red0 + lane * 32 + 16);
#pragma unroll
    for (int i = 1; i < 4; ++i) { a += ldsrf(red0 + i * 4096 + lane * 32); b += ldsrf(red0 + i * 4096 + lane * 32 + 16); }
    const u32x4 f = {pks(a[0], a[1]), pks(a[2], a[3]), pks(b[0], b[1]), pks(b[2], b[3])};
    st16(rw, dst + lane * 16, f);
    return max32(addx32(sq4(f, 0.f)));
}

template <bool g0>
DEV void program(const char* __restrict__ x0, const char* __restrict__ wq, const char* __restrict__ wk, const char* __restrict__ wv, const char* __restrict__ woa,
                 const char* __restrict__ wqkv, const char* __restrict__ wob, char* __restrict__ out, char* ws, lptr lds_, const int w_) {
#define FRESH() lptr lds = lds_; asm volatile("" : "+s"(lds)); const unsigned lds0 = (unsigned)(size_t)lds; (void)lds0; int w = w_, b = blockIdx.x, lane; \
    asm volatile("v_mbcnt_lo_u32_b32 %2, -1, 0\n\tv_mbcnt_hi_u32_b32 %2, -1, %2" : "+s"(w), "+s"(b), "=v"(lane)); \
    const int wi = w & 3, tid = w * 64 + lane, rb = 4 * (b & 7) + (b >> 6), cg = (b >> 3) & 7, n = lane & 31, h = lane >> 5, tbo = cg & 1, fg = cg >> 1; \
    unsigned one_ = 0x3F803F80u, tag = tag_; float zero_ = 0.f; asm volatile("" : "+v"(one_), "+v"(zero_), "+s"(tag)); const u32x4 ONES = {one_, one_, one_, one_}; (void)ONES; \
    const bool prod = cg < 4, isV = (cg >> 1) & 1; (void)wi; (void)tid; (void)rb; (void)n; (void)h; (void)tbo; (void)fg; (void)prod; (void)isV
    const rsrc_t rw = __builtin_amdgcn_make_buffer_rsrc(ws, 0, WS_END, 0x00020000);
    const u32x4 rd = {(unsigned)(size_t)ws, (unsigned)((size_t)ws >> 32) & 0xFFFFu, (unsigned)WS_END, 0x00020000u};
    unsigned ev, tag_ = 0;
    asm volatile("global_load_dword %0, %1, %2 sc1" : "=&v"(ev) : "v"(0), "s"(ws + WS_E) : "memory");
    const f32x16 ZERO = splat16(0.f);
    u32x4 W[32];
    unsigned nre = 0; (void)nre;
    u32x4 xr0, xr1;

    {
        FRESH();
        STAMP(0); STAMPC(84);
        if (g0 && prod) {
            const char* src = (isV ? wv : wk) + wi * 2048;
#pragma unroll
            for (int j = 0; j < 4; ++j) dma1(lane * 16, src + (j >> 1) * 8192 + (j & 1) * 1024, lds0 + L_WK + wi * 4096 + j * 1024);
        }
        u32x4 xv[8];
        const int row = 8 * w + (lane >> 3);
        {
            const char* src = x0 + (size_t)(rb * 64 + 8 * w) * 1024; const unsigned vo = (lane >> 3) * 1024 + (lane & 7) * 16;
            asm volatile("s_nop 4\n\tglobal_load_dwordx4 %0, %8, %9\n\tglobal_load_dwordx4 %1, %8, %9 offset:128\n\tglobal_load_dwordx4 %2, %8, %9 offset:256\n\tglobal_load_dwordx4 %3, %8, %9 offset:384\n\t"
                         "global_load_dwordx4 %4, %8, %9 offset:512\n\tglobal_load_dwordx4 %5, %8, %9 offset:640\n\tglobal_load_dwordx4 %6, %8, %9 offset:768\n\tglobal_load_dwordx4 %7, %8, %9 offset:896"
                         : "=&v"(xv[0]), "=&v"(xv[1]), "=&v"(xv[2]), "=&v"(xv[3]), "=&v"(xv[4]), "=&v"(xv[5]), "=&v"(xv[6]), "=&v"(xv[7]) : "v"(vo), "s"(uptr(src)) : "memory");
        }
        {
            const unsigned vo = (lane >> 3) * 1024 + (((lane & 7) ^ (((lane >> 4) & 1) << 2)) * 16);
            const char* src = wq + cg * 128 + (size_t)w * 65536;
#pragma unroll
            for (int m = 0; m < 8; ++m) dma1(vo, src + m * 8192, lds0 + L_WQ + w * 8192 + m * 1024);
        }
        if constexpr (g0) { const unsigned v = ((tid & 11) == 0) ? 0x3F803F80u : 0u; ldsw(lds + L_CONST + tid * 16, (u32x4){v, v, v, v}); }
        else *(LAS unsigned*)(lds + L_CNT) = 0u;
        asm volatile("s_waitcnt vmcnt(8)" : "+v"(xv[0]), "+v"(xv[1]), "+v"(xv[2]), "+v"(xv[3]), "+v"(xv[4]), "+v"(xv[5]), "+v"(xv[6]), "+v"(xv[7]), "+v"(ev) :: "memory");
        tag = tag_ = __builtin_amdgcn_readfirstlane(ev) + 1u;
        STAMP(1);
        const int gw = (rb * 4 + (cg & 3)) * 4 + wi;
        u32x4 pt[8]; unsigned pe[8]; int pid[2] = {0, 0}; bool pkv = false;
        if (g0 && !prod) {
            const int ks = gw & 31, gi = gw >> 5, rowo = 16 * ks + (lane >> 4);
            const char* src = gi < 4 ? woa + gi * 256 : wqkv + (gi - 4) * 256; const int ld = gi < 4 ? 1024 : 3072;
            pid[0] = ks + (gi < 4 ? WT_WOA + gi * 128 : gi < 8 ? WT_QB + (gi - 4) * 128 : WT_KVB + (gi & 3) * 256 + ((gi - 8) >> 2) * 64); pkv = gi >= 8;
#pragma unroll
            for (int m = 0; m < 4; ++m) pt[m] = *(const u32x4*)(src + (size_t)(rowo + 4 * m) * ld + (lane & 15) * 16);
            if (gw < 256) {
                const char* s2 = (gi < 4 ? wob : wq) + (gi & 3) * 256; pid[1] = ks + (gi < 4 ? WT_WOB : WT_QA) + (gi & 3) * 128;
#pragma unroll
                for (int m = 0; m < 4; ++m) pt[4 + m] = *(const u32x4*)(s2 + (size_t)(rowo + 4 * m) * 1024 + (lane & 15) * 16);
            } else if (gw < 320) {
                const unsigned short* s2 = (const unsigned short*)((gi & 1) ? wv : wk) + (16 * ks + 4 * h) * 16 + (lane & 15);
#pragma unroll
                for (int j = 0; j < 8; ++j) pe[j] = s2[((j >> 2) * 8 + (j & 3)) * 16];
            }
        }
        float s = 0.f, q = 0.f;
        lptr dst = lds + L_AT + (row >> 5) * HTS + ((lane & 7) >> 1) * HFS + (row & 31) * 16 + (lane & 1) * 8;
#pragma unroll
        for (int m = 0; m < 8; ++m) {
            *(LAS u32x2*)(dst + m * 4 * HFS) = (u32x2){xv[m][0], xv[m][1]};
            *(LAS u32x2*)(dst + m * 4 * HFS + HHS) = (u32x2){xv[m][2], xv[m][3]};
            s = sm4(xv[m], s); q = sq4(xv[m], q);
        }
        s = sum8(s); q = sum8(q);
        const float mean = s * (1.f / 512.f), var = fmaxf(q * (1.f / 512.f) - mean * mean, 0.f);
        *(LAS float*)(lds + L_MEAN + row * 4) = mean; *(LAS float*)(lds + L_RSTD + row * 4) = __builtin_amdgcn_rsqf(var + LN_EPS);
        BAR();
        STAMP(2);
#define ARRIVE() asm volatile("ds_add_u32 %0, %1" :: "v"(lds0 + L_CNT), "v"(lane == 0 ? 1u : 0u) : "memory")
        if constexpr (g0) {
            if (prod) {
                lptr wp = lds + L_WK + wi * 4096 + (4 * h + ((lane & 15) >> 2)) * 32 + (lane & 3) * 8;
                f32x16 acc = ZERO, cs = ZERO;
                lptr ap = lds + L_AT + tbo * HTS + h * HHS + n * 16 + wi * 4 * HFS;
                u32x4 xf[8], Wj[8];
#pragma unroll
                for (int j = 0; j < 8; ++j) { Wj[j] = trfrag(wp + j * 512, 256); xf[j] = ldsr(ap + ((j >> 2) * 16 + (j & 3)) * HFS); }
                if (isV) {
#pragma unroll
                    for (int j = 0; j < 8; ++j) { acc = mfma(xf[j], Wj[j], acc); cs = mfma(ONES, Wj[j], cs); }
                } else {
#pragma unroll
                    for (int j = 0; j < 8; ++j) { acc = mfma(Wj[j], xf[j], acc); cs = mfma(Wj[j], ONES, cs); }
                }
                kv_partial(acc, cs, isV, tbo, lds, lds + L_WK + wi * 4096, lane);
            }
            BAR();
            STAMP(73);
            if (prod) {
                float km = 0.f;
                if (w == 0) km = kv_final(lds + L_WK, rw, WS_KVA + (isV ? 65536 : 0) + (rb * 2 + tbo) * 1024, lane);
                VM0();
                if (w == 0) st8(rw, only(lane == 0, WS_KAF + (rb * 4 + cg) * 128), (u32x2){isV ? 0u : __float_as_uint(km), tag});
                ARRIVE();
            } else {
                VM0();
                ARRIVE();
                lptr sc = lds + L_WK + wi * 4096;
                lptr tp = sc + (4 * h + ((lane & 15) >> 2)) * 256 + ((lane >> 4) & 1) * 32 + (lane & 3) * 8;
#pragma unroll
                for (int m = 0; m < 4; ++m) ldsw(sc + m * 1024 + lane * 16, pt[m]);
#pragma unroll
                for (int j = 0; j < 4; ++j) st16(rw, WS_WT + (pid[0] + (pkv ? (j >> 1) * 128 + (j & 1) * 32 : j * 32)) * 1024 + lane * 16, trfrag(tp + j * 64, 2048));
                if (gw < 256) {
#pragma unroll
                    for (int m = 0; m < 4; ++m) ldsw(sc + m * 1024 + lane * 16, pt[4 + m]);
#pragma unroll
                    for (int j = 0; j < 4; ++j) st16(rw, WS_WT + (pid[1] + j * 32) * 1024 + lane * 16, trfrag(tp + j * 64, 2048));
                } else if (gw < 320) {
                    st16(rw, WS_WT + (WT_KA + gw - 256) * 1024 + lane * 16, (u32x4){pe[0] | (pe[1] << 16), pe[2] | (pe[3] << 16), pe[4] | (pe[5] << 16), pe[6] | (pe[7] << 16)});
                }
            }
        } else {
            BAR();
            VM0();
            ARRIVE();
            while (UNI(__hip_atomic_load((LAS unsigned*)(lds + L_CNT), __ATOMIC_RELAXED, __HIP_MEMORY_SCOPE_WORKGROUP)) < 8) __builtin_amdgcn_s_sleep(1);
            asm volatile("" ::: "memory");
            STAMP(73);
            const int f = wi & 1, tb = wi >> 1, qq = (lane & 15) >> 2;
            lptr wp = lds + L_WQ + (4 * h + qq) * 128 + ((f * 64 + ((lane >> 4) & 1) * 32 + (lane & 3) * 8) ^ ((qq >> 1) << 6));
            const unsigned ap = lds0 + L_AT + tb * HTS + h * HHS + n * 16;
            f32x16 acc, cs; Tmp t;
#pragma unroll
            for (int ks = 0; ks < 32; ++ks) W[ks] = trfrag(wp + ks * 2048, 1024);
            const float mean = *(LAS float*)(lds + L_MEAN + (tb * 32 + n) * 4), rstd = *(LAS float*)(lds + L_RSTD + (tb * 32 + n) * 4);
            lptr rp = lds + L_AT + tbo * HTS + (8 * fg + 2 * wi) * HFS + h * HHS + n * 16;
            xr0 = ldsr(rp); xr1 = ldsr(rp + HFS);
            colsum32<true>(cs, W, ONES);
            gs<2, 0, HFS>(acc, t, W, ap); gs<2, 1, HFS>(acc, t, W + 8, ap); gs<2, 2, HFS>(acc, t, W + 16, ap); gs<2, 3, HFS>(acc, t, W + 24, ap);
            settle(acc, cs);
            u32x4 q0, q1; fold_t(acc, cs, mean, rstd * QSCALE, q0, q1);
            ldsw(lds + L_QX + ((2 * f) * 2 + tb) * 1024 + lane * 16, q0); ldsw(lds + L_QX + ((2 * f + 1) * 2 + tb) * 1024 + lane * 16, q1);
        }
    }

    _Pragma("clang loop unroll(disable)") for (int p = 0; p < 2; ++p) {
        u32x4 Wj[8];
        {
            FRESH();
            STAMP(3 + 20 * p);
            if (w == 0) {
                const char* fp = ws + WS_KAF + p * 16384 + lane * 128;
                u32x2 a, c;
                _Pragma("clang loop unroll(disable)") for (unsigned sp = 0; sp < SPIN_CAP; ++sp) { a = poll8(fp); c = poll8(fp + 8192); if (__all(a[1] == tag && c[1] == tag)) break; __builtin_amdgcn_s_sleep(2); }
                const float km = maxx32(max32(fmaxf(__uint_as_float(a[0]), __uint_as_float(c[0]))));
                *(LAS float*)(lds + L_KMA) = km;
            }
            BAR();
            STAMP(4 + 20 * p);
            const int kv = WS_KVA + p * 131072;
            const unsigned vo = lane * 16;
            u32x4 c0, c1, c2, c3; const int off = (w >> 2) * 65536 + wi * 4096;
            gld4(c0, c1, c2, c3, vo, rd, kv + off);
            AttState s;
            s.q = ldsr(lds + L_QX + w * 1024 + lane * 16);
            const float m = __builtin_amdgcn_sqrtf(addx32(sq4(s.q, 0.f)) * *(LAS float*)(lds + L_KMA)) * 1.002f;
            const bool exact = !__all(m <= MBOUND);
            s.c = splat16(-m); s.o = splat16(zero_); s.a = s.o; s.b = s.o; s.k = ONES; s.v0 = ONES; s.v1 = ONES;
            s.ka = lds0 + L_KA + lane * 16; s.va = lds0 + (n < 16 ? L_VA + h * 256 + n * 16 : L_CONST + h * 256 + (n - 16) * 16);
            s.vs = n < 16 ? 4096u : 0u; s.idx = 0xDDDDu; asm volatile("" : "+v"(s.idx));
            unsigned fw = SENT; asm volatile("" : "+v"(fw)); const u32x4 F = {fw, fw, fw, fw};
            const int ro = (w >> 2) * 65536 + 16384 + wi * 12288;
            const int r0 = WS_O + 2 * p * 2097152 + (rb * 64 + (w & 1) * 32 + 4 * cg + (w >> 1)) * 1024, r1 = r0 + 2097152;
            const int r2 = WS_KVB + p * 4194304 + (rb * 8 + cg) * 16384 + w * 2048;
            const int r3 = WS_X + 2 * p * 2097152 + (rb * 64 + tbo * 32 + 8 * fg + w) * 1024, r4 = p == 0 ? r3 + 2097152 : r3;
            const int r5 = w < 2 - p ? WS_ST + (2 * p + w) * 262144 + rb * 8192 + tbo * 4096 + fg * 1024 : r3;
            wait4<0>(c0, c1, c2, c3);
            { lptr dst = lds + off + lane * 16; ldsw(dst, c0); ldsw(dst + 1024, c1); ldsw(dst + 2048, c2); ldsw(dst + 3072, c3); }
            BAR();
            STAMP(5 + 20 * p);
            if (!exact) { att_begin(s); att_first(s, vo, rd, kv + ro, lds0 + ro, F, r0, r1, r2, r3, r4, r5); }
            else {
#pragma unroll
                for (int j = 0; j < 3; ++j) dma4s(vo, rd, kv + ro + j * 4096, lds0 + ro + j * 4096);
                st16(rw, r0 + vo, F); st16(rw, r1 + vo, F); st16(rw, r2 + vo, F); st16(rw, r2 + 1024 + vo, F); st16(rw, r3 + vo, F); st16(rw, r4 + vo, F); st16(rw, r5 + vo, F);
            }
            STAMP(7 + 20 * p);
            VM0();
            BAR();
            STAMP(8 + 20 * p);
            if (w == 0) st8(rw, only(lane == 0, WS_RDY + p * 32768 + b * 128), (u32x2){tag, tag});
            if (exact) {
                float mx = NEG_BIG;
                for (int t = 0; t < 64; ++t) mx = rowmax16(mfma(ldsr(lds + L_KA + t * 1024 + lane * 16), s.q, ZERO), mx);
                s.c = splat16(-maxx32(mx));
                att_begin(s); att_run(s, 3);
            }
            att_run(s, 2);
            unsigned f0, f1, f2, f3; const int fo = UNI(WS_RDY + p * 32768); const unsigned fv = lane * 128;
#define RDY_LOAD() asm volatile("s_nop 4\n\tbuffer_load_dword %0, %4, %8, %9 offen sc1\n\tbuffer_load_dword %1, %5, %8, %9 offen sc1\n\tbuffer_load_dword %2, %6, %8, %9 offen sc1\n\tbuffer_load_dword %3, %7, %8, %9 offen sc1" \
                                : "=&v"(f0), "=&v"(f1), "=&v"(f2), "=&v"(f3) : "v"(fv), "v"(fv + 8192), "v"(fv + 16384), "v"(fv + 24576), "s"(rd), "s"(fo) : "memory")
#define RDY_WAIT() asm volatile("s_waitcnt vmcnt(0)" : "+v"(f0), "+v"(f1), "+v"(f2), "+v"(f3) :: "memory")
            RDY_LOAD();
            att_run(s, 2);
            STAMP(9 + 20 * p);
            RDY_WAIT();
            _Pragma("clang loop unroll(disable)") for (unsigned sp = 0; !__all(f0 == tag && f1 == tag && f2 == tag && f3 == tag) && sp < SPIN_CAP; ++sp) { __builtin_amdgcn_s_sleep(4); RDY_LOAD(); RDY_WAIT(); }
            if (p == 0 && b == 0 && w == 0) __builtin_amdgcn_raw_buffer_store_b32(tag, rw, only(lane == 0, WS_E), 0, 16);
            STAMP(10 + 20 * p);
            const int wo = WS_WT + (g0 ? WT_KVB + (cg * 4 + wi) * 32 : WT_WOA + (4 * fg + wi) * 32) * 1024;
            att_wload(s, W, vo, rd, wo); att_wload(s, W + 8, vo, rd, wo + 8192); att_wload(s, W + 16, vo, rd, wo + 16384); att_wload(s, W + 24, vo, rd, wo + 24576);
            att_run(s, 1);
            att_end(s);
            STAMP(11 + 20 * p);
            wwait32(W);
            st16(rw, r0 + vo, norm_pack(s.o));
            BAR();
        }
#define OUT_ISSUE(OBUF, SB) \
            VM0(); STAMP0(SB); nre = 0; \
            const int osrc = WS_O + (OBUF) * 2097152 + (rb * 64 + tbo * 32 + 2 * wi) * 1024; lptr odst = lds + L_AT + 2 * wi * 1024 + lane * 16; \
            u32x4 o_[8]; \
            _Pragma("unroll") for (int c = 0; c < 4; ++c) gld2(o_[2 * c], o_[2 * c + 1], lane * 16, rd, osrc + c * 8192)
#define OUT_STAGE(C, N, SB) do { stage2<N>(o_[2 * C], o_[2 * C + 1], lane * 16, rd, osrc + C * 8192, odst + C * 8192 NRETRY); STAMP0(SB + 1 + C); if (C == 3 && p == 0) STAMPV(SB + 5, nre); BAR(); } while (0)
#define OUT_MMA(PK) do { \
            f32x16 acc; Tmp t; \
            _Pragma("unroll") for (int j = 0; j < 4; ++j) { acc[2 * j] = bflo(xr0[j]); acc[2 * j + 1] = bfhi(xr0[j]); acc[8 + 2 * j] = bflo(xr1[j]); acc[9 + 2 * j] = bfhi(xr1[j]); } \
            const unsigned ap_ = lds0 + L_AT + lane * 16; \
            BAR(); gs<0, 0, 1024>(acc, t, W, ap_); gs<0, 1, 1024>(acc, t, W + 8, ap_); gs<0, 2, 1024>(acc, t, W + 16, ap_); gs<0, 3, 1024>(acc, t, W + 24, ap_); \
            settle(acc); \
            _Pragma("unroll") for (int j = 0; j < 8; ++j) PK[j] = pks(acc[2 * j], acc[2 * j + 1]); } while (0)
#define OUT_PUBLISH(PK, XBUF) do { \
            const u32x4 f0 = {PK[0], PK[1], PK[2], PK[3]}, f1 = {PK[4], PK[5], PK[6], PK[7]}; \
            const int xo = WS_X + (XBUF) * 2097152 + (rb * 64 + tbo * 32 + 8 * fg + 2 * wi) * 1024 + lane * 16; \
            st16(rw, xo, f0); st16(rw, xo + 1024, f1); \
            const float s1 = addx32(sm4(f1, sm4(f0, 0.f))), s2 = addx32(sq4(f1, sq4(f0, 0.f))); \
            st8(rw, only(lane < 32, WS_ST + (XBUF) * 262144 + rb * 8192 + tbo * 4096 + (4 * fg + wi) * 256 + lane * 8), (u32x2){min(__float_as_uint(s1), SENT - 1u), min(__float_as_uint(s2), SENT - 1u)}); } while (0)

        if constexpr (g0) {
            FRESH();
            STAMP(12 + 20 * p);
            { OUT_ISSUE(2 * p, 44); OUT_STAGE(0, 6, 44); OUT_STAGE(1, 4, 44); OUT_STAGE(2, 2, 44); OUT_STAGE(3, 0, 44); }
            STAMP(13 + 20 * p);
            f32x16 cs, a0, a1; Tmp t;
            BAR();
            STAMP(14 + 20 * p);
            lptr ap = lds + L_AT + lane * 16;
            const unsigned aa = lds0 + L_AT + lane * 16, ab = aa + 32768;
            const int kvo = WS_KVB + p * 4194304 + (rb * 8 + cg) * 16384;
            const int isK = UNI(wi < 2 ? 1 : 0);
            colsum8d<true>(isK, cs, W, ONES); colsum8d<false>(isK, cs, W + 8, ONES); colsum8d<false>(isK, cs, W + 16, ONES); colsum8d<false>(isK, cs, W + 24, ONES);
            BAR(); gd<0>(isK, a0, a1, t, W, aa, ab); gd<1>(isK, a0, a1, t, W + 8, aa, ab); gd<2>(isK, a0, a1, t, W + 16, aa, ab);
            RowStats st0, st1; row_stats(st0, lds, 0, h); row_stats(st1, lds, 1, h);
            const float m0 = *(LAS float*)(lds + L_MEAN + n * 4), m1 = *(LAS float*)(lds + L_MEAN + 128 + n * 4), s0 = *(LAS float*)(lds + L_RSTD + n * 4), s1 = *(LAS float*)(lds + L_RSTD + 128 + n * 4);
            gd<3>(isK, a0, a1, t, W + 24, aa, ab);
            settle(cs, a0, a1); STAMP0(56);
            if (isK) {
                u32x4 f0, f1, f2, f3; fold_t(a0, cs, m0, s0, f0, f1);
                const int o = 2 * wi * 2048 + h * 512 + slot_of(n) * 16;
                st16(rw, kvo + o, f0); st16(rw, kvo + o + 2048, f1);
                fold_t(a1, cs, m1, s1, f2, f3);
                st16(rw, kvo + o + 8192, f2); st16(rw, kvo + o + 10240, f3);
                asm volatile("" ::: "memory");
                lptr lo = lds + L_KVB + 65536 + o; ldsw(lo, f0); ldsw(lo + 2048, f1); ldsw(lo + 8192, f2); ldsw(lo + 10240, f3);
                const float k0 = max32(fmaxf(addx32(sq4(f0, 0.f)), addx32(sq4(f2, 0.f)))), k1 = max32(fmaxf(addx32(sq4(f1, 0.f)), addx32(sq4(f3, 0.f))));
                const float km = (lane & 1) ? k1 : k0;
                st8(rw, only(lane < 2, WS_KBF + p * 32768 + (rb * 8 + cg) * 128 + (2 * wi + lane) * 8), (u32x2){__float_as_uint(km), tag});
                *(LAS float*)(lds + L_KMOWN + (2 * wi + (lane & 1)) * 4) = km;
            } else {
                const int o = (2 * (wi - 2) + (n >> 4)) * 2048 + 1024 + h * 256 + (n & 15) * 16;
                float y[16]; fold_n(a0, cs[0], st0, y);
                const u32x4 f0 = {pks(y[0], y[1]), pks(y[2], y[3]), pks(y[4], y[5]), pks(y[6], y[7])}, f1 = {pks(y[8], y[9]), pks(y[10], y[11]), pks(y[12], y[13]), pks(y[14], y[15])};
                st16(rw, kvo + o, f0); st16(rw, kvo + o + 512, f1);
                fold_n(a1, cs[0], st1, y);
                const u32x4 f2 = {pks(y[0], y[1]), pks(y[2], y[3]), pks(y[4], y[5]), pks(y[6], y[7])}, f3 = {pks(y[8], y[9]), pks(y[10], y[11]), pks(y[12], y[13]), pks(y[14], y[15])};
                st16(rw, kvo + o + 8192, f2); st16(rw, kvo + o + 8704, f3);
                asm volatile("" ::: "memory");
                lptr lo = lds + L_KVB + 65536 + o; ldsw(lo, f0); ldsw(lo + 512, f1); ldsw(lo + 8192, f2); ldsw(lo + 8704, f3);
            }
            xr0 = ldsr(ap + (tbo * 32 + 8 * fg + 2 * wi) * 1024); xr1 = ldsr(ap + (tbo * 32 + 8 * fg + 2 * wi + 1) * 1024);
            STAMP(15 + 20 * p);
            VM0(); STAMP0(57); nre = 0;
            {
                const int sb = WS_KVB + p * 4194304 + cg * 16384 + wi * 4096; lptr dst = lds + L_KVB + wi * 4096 + lane * 16;
                const int n0 = sb + (rb >= 4 ? rb - 4 : rb) * 131072, n1 = sb + (rb >= 3 ? rb - 3 : rb) * 131072, n2 = sb + (rb >= 2 ? rb - 2 : rb) * 131072, n3 = sb + (rb >= 1 ? rb - 1 : rb) * 131072;
                const int wo = WS_WT + (WT_WOB + (4 * fg + wi) * 32) * 1024;
                const int nbk = rb - 4 + ((lane >> 2) & 3);
                const unsigned fv = ((nbk >= 0 ? nbk : rb) * 8 + cg) * 128 + (lane & 3) * 8; const int fo = UNI(WS_KBF + p * 32768);
                u32x2 kw;
#define KM_LOAD() asm volatile("s_nop 4\n\tbuffer_load_dwordx2 %0, %1, %2, %3 offen sc1" : "=&v"(kw) : "v"(fv), "s"(rd), "s"(fo) : "memory")
                u32x4 r[16];
                gld4(r[0], r[1], r[2], r[3], lane * 16, rd, n0); gld4(r[4], r[5], r[6], r[7], lane * 16, rd, n1);
                gld4(r[8], r[9], r[10], r[11], lane * 16, rd, n2); gld4(r[12], r[13], r[14], r[15], lane * 16, rd, n3);
                KM_LOAD();
                stage4<13>(r[0], r[1], r[2], r[3], lane * 16, rd, n0, dst NRETRY);
                stage4<9>(r[4], r[5], r[6], r[7], lane * 16, rd, n1, dst + 16384 NRETRY);
                wld8(W, lane * 16, rd, wo);
                stage4<13>(r[8], r[9], r[10], r[11], lane * 16, rd, n2, dst + 32768 NRETRY);
                stage4<9>(r[12], r[13], r[14], r[15], lane * 16, rd, n3, dst + 49152 NRETRY);
                STAMP0(58); if (p == 0) STAMPV(59, nre);
                asm volatile("s_waitcnt vmcnt(8)" : "+v"(kw) :: "memory");
                _Pragma("clang loop unroll(disable)") for (unsigned sp = 0; !__all(kw[1] == tag) && sp < SPIN_CAP; ++sp) { KM_LOAD(); asm volatile("s_waitcnt vmcnt(0)" : "+v"(kw) :: "memory"); }
                float km = __uint_as_float(kw[0]);
                km = fmaxf(km, dpp<0x124>(km)); km = fmaxf(km, dpp<0x128>(km));
                *(LAS float*)(lds + L_KMNB + (lane & 3) * 4) = km;
                STAMP(16 + 20 * p);
                BAR();
                STAMP(17 + 20 * p);
                wld8(W + 8, lane * 16, rd, wo + 8192); wld8(W + 16, lane * 16, rd, wo + 16384); wld8(W + 24, lane * 16, rd, wo + 24576);
                STAMP0(60);
            }
        } else {
            FRESH();
            unsigned pk[8];
            STAMP(12 + 20 * p);
            OUT_MMA(pk);
            STAMP(13 + 20 * p);
            OUT_PUBLISH(pk, 2 * p);
            BAR();
            STAMP(14 + 20 * p);
            VM0(); STAMP0(56); nre = 0;
            const int f = wi & 1, tb = wi >> 1;
            const int wo = WS_WT + (WT_QB + (2 * cg + f) * 32) * 1024;
            {
                const int src = WS_X + 2 * p * 2097152 + (rb * 64 + tb * 32 + f * 4) * 1024; lptr dst = lds + L_AT + (tb * 32 + f * 4) * 1024 + lane * 16;
                const int ssrc = WS_ST + 2 * p * 262144 + rb * 8192; const int u = tid - 256; const unsigned svo = (u & 15) * 256 + (u >> 4) * 16;
                u32x4 r[16], sa, sb;
                gld1(sa, svo, rd, ssrc); gld1(sb, svo, rd, ssrc + 4096);
#pragma unroll
                for (int c = 0; c < 4; ++c) gld4(r[4 * c], r[4 * c + 1], r[4 * c + 2], r[4 * c + 3], lane * 16, rd, src + c * 8192);
                stage4<12>(r[0], r[1], r[2], r[3], lane * 16, rd, src, dst NRETRY); STAMP0(57); BAR();
                stage4<8>(r[4], r[5], r[6], r[7], lane * 16, rd, src + 8192, dst + 8192 NRETRY);
                stage_stats<8>(sa, sb, svo, rd, ssrc, lds, u); STAMP0(58); BAR();
                wld8(W, lane * 16, rd, wo);
                stage4<12>(r[8], r[9], r[10], r[11], lane * 16, rd, src + 16384, dst + 16384 NRETRY); STAMP0(59); BAR();
                wld8(W + 8, lane * 16, rd, wo + 8192);
                stage4<16>(r[12], r[13], r[14], r[15], lane * 16, rd, src + 24576, dst + 24576 NRETRY); STAMP0(60); if (p == 0) STAMPV(61, nre); BAR();
                wld8(W + 16, lane * 16, rd, wo + 16384); wld8(W + 24, lane * 16, rd, wo + 24576);
            }
            STAMP(15 + 20 * p);
            f32x16 acc, cs; Tmp t;
            const unsigned ap = lds0 + L_AT + tb * 32768 + lane * 16;
            const float mean = *(LAS float*)(lds + L_MEAN + (tb * 32 + n) * 4), rstd = *(LAS float*)(lds + L_RSTD + (tb * 32 + n) * 4);
            wwait8<24>(W); colsum8<true, true>(cs, W, ONES); wwait8<16>(W + 8); colsum8<true, false>(cs, W + 8, ONES);
            gs<2, 0, 1024>(acc, t, W, ap); gs<2, 1, 1024>(acc, t, W + 8, ap);
            wwait8<8>(W + 16); gs<2, 2, 1024>(acc, t, W + 16, ap); wwait8<0>(W + 24); gs<2, 3, 1024>(acc, t, W + 24, ap);
            colsum8<true, false>(cs, W + 16, ONES); colsum8<true, false>(cs, W + 24, ONES);
            settle(acc, cs); STAMP0(64);
            u32x4 q0, q1; fold_t(acc, cs, mean, rstd * QSCALE, q0, q1);
            ldsw(lds + L_QX + ((2 * f) * 2 + tb) * 1024 + lane * 16, q0); ldsw(lds + L_QX + ((2 * f + 1) * 2 + tb) * 1024 + lane * 16, q1);
            STAMP(16 + 20 * p);
            BAR();
            STAMP(17 + 20 * p);
        }
        {
            FRESH();
            const int tb = w & 1, hh = w >> 1, kt = 2 * rb + tb, nmid = kt < 7 ? kt : 7;
            const u32x4 q = ldsr(lds + L_QX + w * 1024 + lane * 16);
            const float km = fmaxf(*(LAS float*)(lds + L_KMOWN + hh * 4), *(LAS float*)(lds + L_KMNB + hh * 4));
            float m = __builtin_amdgcn_sqrtf(addx32(sq4(q, 0.f)) * km) * 1.002f;
            lptr kp = lds + L_KVB + (8 + tb) * 8192 + hh * 2048 + lane * 16;
            if (!__all(m <= MBOUND)) {
                f32x16 c; mask_fill<0>(c, 0.f, NEG_BIG);
                float mx = rowmax16(mfma(ldsr(kp), q, c), NEG_BIG);
                for (int t = 1; t <= nmid; ++t) mx = rowmax16(mfma(ldsr(kp - t * 8192), q, ZERO), mx);
                if (kt >= 8) { mask_fill<0>(c, NEG_BIG, 0.f); mx = rowmax16(mfma(ldsr(kp - 65536), q, c), mx); }
                m = maxx32(mx);
            }
            AttState s; float big = NEG_BIG; asm volatile("" : "+v"(big));
            mask_fill<0>(s.b, big, kt >= 8 ? -m : big);
            s.q = q; s.c = splat16(kt >= 8 ? -m : big); s.o = splat16(zero_); s.vs = n < 16 ? 8192 : 0; s.idx = 0xDDDDu; asm volatile("" : "+v"(s.idx));
            asm volatile("" : "={v[200:215]}"(s.a), "={v[236:239]}"(s.k), "={v[240:243]}"(s.v0), "={v[244:247]}"(s.v1));
            s.ka = (unsigned)(size_t)kp - 65536; s.va = lds0 + (n < 16 ? L_KVB + tb * 8192 + hh * 2048 + 1024 + h * 256 + n * 16 : L_CONST + h * 256 + (n - 16) * 16);
            att_window(s, big, -m, (unsigned)(size_t)kp, 8 - kt);
            const f32x16 o = s.o;
            if constexpr (g0) wwait32(W);
            st16(rw, WS_O + (2 * p + 1) * 2097152 + (rb * 64 + tb * 32 + 4 * cg + hh) * 1024 + lane * 16, norm_pack(o));
            STAMP(18 + 20 * p);
            if (p == 0 && g0 && prod) {
                const int wo = WS_WT + ((isV ? WT_VA : WT_KA) + 4 * wi) * 1024 + lane * 16;
#pragma unroll
                for (int j = 0; j < 8; ++j) Wj[j] = ld16(rw, wo + ((j >> 2) * 16 + (j & 3)) * 1024);
            } else if (g0) {
#pragma unroll
                for (int j = 0; j < 8; ++j) asm volatile("" : "=v"(Wj[j]));
            }
            BAR();
        }
        if constexpr (g0) {
            FRESH();
            unsigned pk[8];
            STAMP(19 + 20 * p);
            OUT_MMA(pk);
            STAMP(20 + 20 * p);
            if (p == 1) {
                char* dst = out + (size_t)(rb * 64 + tbo * 32 + n) * 1024 + (128 * fg + 32 * wi) * 2 + h * 16;
#pragma unroll
                for (int g = 0; g < 2; ++g) {
                    auto r0 = __builtin_amdgcn_permlane32_swap(pk[4 * g], pk[4 * g + 2], false, false), r1 = __builtin_amdgcn_permlane32_swap(pk[4 * g + 1], pk[4 * g + 3], false, false);
                    *(u32x4*)(dst + g * 32) = (u32x4){r0[0], r1[0], r0[1], r1[1]};
                }
            } else {
                OUT_PUBLISH(pk, 1);
                BAR();
                f32x16 acc = ZERO, cs = ZERO;
#define JOB(J0, J1, DST, XF) do { if (isV) { _Pragma("unroll") for (int j = J0; j < J1; ++j) DST = mfma(XF, Wj[j], DST); } \
                                  else { _Pragma("unroll") for (int j = J0; j < J1; ++j) DST = mfma(Wj[j], XF, DST); } asm volatile("" : "+v"(DST)); } while (0)
                if (prod) JOB(0, 8, cs, ONES);
                VM0(); STAMP0(66); nre = 0;
                const int s0 = WS_X + 2097152 + (rb * 64 + tbo * 32 + wi * 4) * 1024, s1 = WS_X + 2097152 + (rb * 64 + (tbo ^ 1) * 32 + wi * 4) * 1024;
                lptr d0 = lds + L_AT + (tbo * 32 + wi * 4) * 1024 + lane * 16, d1 = lds + L_AT + ((tbo ^ 1) * 32 + wi * 4) * 1024 + lane * 16;
                const int ssrc = WS_ST + 262144 + rb * 8192; const unsigned svo = (tid & 15) * 256 + (tid >> 4) * 16;
                u32x4 r[16], sa, sb;
                gld1(sa, svo, rd, ssrc); gld1(sb, svo, rd, ssrc + 4096);
                gld4(r[0], r[1], r[2], r[3], lane * 16, rd, s0); gld4(r[4], r[5], r[6], r[7], lane * 16, rd, s0 + 16384);
                gld4(r[8], r[9], r[10], r[11], lane * 16, rd, s1); gld4(r[12], r[13], r[14], r[15], lane * 16, rd, s1 + 16384);
                stage_stats<16>(sa, sb, svo, rd, ssrc, lds, tid); STAMP0(69);
                stage4<12>(r[0], r[1], r[2], r[3], lane * 16, rd, s0, d0 NRETRY); STAMP0(67); BAR();
                if (prod) JOB(0, 4, acc, r[j]);
                stage4<8>(r[4], r[5], r[6], r[7], lane * 16, rd, s0 + 16384, d0 + 16384 NRETRY); STAMP0(68);
                if (prod) { JOB(4, 8, acc, r[j]); kv_partial(acc, cs, isV, tbo, lds, lds + L_KVB + wi * 4096, lane); }
                STAMP(21);
                BAR();
                float km = 0.f;
                if (prod && w == 0) km = kv_final(lds + L_KVB, rw, WS_KVA + 131072 + (isV ? 65536 : 0) + (rb * 2 + tbo) * 1024, lane);
                stage4<4>(r[8], r[9], r[10], r[11], lane * 16, rd, s1, d1 NRETRY); BAR();
                stage4<0>(r[12], r[13], r[14], r[15], lane * 16, rd, s1 + 16384, d1 + 16384 NRETRY);
                if (prod && w == 0) st8(rw, only(lane == 0, WS_KAF + 16384 + (rb * 4 + cg) * 128), (u32x2){isV ? 0u : __float_as_uint(km), tag});
                STAMP0(70); STAMPV(71, nre);
                BAR();
                STAMP0(72);
            }
        } else {
            FRESH();
            STAMP(19 + 20 * p);
            if (p == 1) { OUT_ISSUE(3, 50); OUT_STAGE(0, 6, 50); OUT_STAGE(1, 4, 50); OUT_STAGE(2, 2, 50); OUT_STAGE(3, 0, 50); }
            else {
                const int f = wi & 1, tb = wi >> 1;
                const int wo = WS_WT + (WT_QA + (2 * cg + f) * 32) * 1024;
                {
                    OUT_ISSUE(1, 50);
                    wld8(W, lane * 16, rd, wo); wld8(W + 8, lane * 16, rd, wo + 8192);
                    OUT_STAGE(0, 22, 50); OUT_STAGE(1, 20, 50); OUT_STAGE(2, 18, 50); OUT_STAGE(3, 16, 50);
                    wld8(W + 16, lane * 16, rd, wo + 16384); wld8(W + 24, lane * 16, rd, wo + 24576);
                }
                STAMP(20);
                f32x16 acc, cs; Tmp t;
                const unsigned ap = lds0 + L_AT + tb * 32768 + lane * 16;
                BAR();
                wwait32(W);
                colsum32<true>(cs, W, ONES);
                if (tb != tbo) { BAR(); BAR(); }
                BAR();
                const float mean = *(LAS float*)(lds + L_MEAN + (tb * 32 + n) * 4), rstd = *(LAS float*)(lds + L_RSTD + (tb * 32 + n) * 4);
                gs<1, 0, 1024>(acc, t, W, ap); gs<1, 1, 1024>(acc, t, W + 8, ap); gs<1, 2, 1024>(acc, t, W + 16, ap); gs<1, 3, 1024>(acc, t, W + 24, ap);
                if (tb == tbo) { BAR(); BAR(); }
                settle(acc, cs);
                u32x4 q0, q1; fold_t(acc, cs, mean, rstd * QSCALE, q0, q1);
                ldsw(lds + L_QX + ((2 * f) * 2 + tb) * 1024 + lane * 16, q0); ldsw(lds + L_QX + ((2 * f + 1) * 2 + tb) * 1024 + lane * 16, q1);
                xr0 = ldsr(lds + L_AT + (tbo * 32 + 8 * fg + 2 * wi) * 1024 + lane * 16); xr1 = ldsr(lds + L_AT + (tbo * 32 + 8 * fg + 2 * wi + 1) * 1024 + lane * 16);
                STAMP(21);
            }
        }
    }
}

__global__ void __launch_bounds__(512, 2) il_fwd(const char* __restrict__ x0, const char* __restrict__ wq, const char* __restrict__ wk, const char* __restrict__ wv,
                                                 const char* __restrict__ woa, const char* __restrict__ wqkv, const char* __restrict__ wob, char* __restrict__ out, char* ws) {
    extern __shared__ __attribute__((aligned(16))) char smem[];
    const int w = __builtin_amdgcn_readfirstlane(threadIdx.x >> 6);
    if (w < 4) program<true>(x0, wq, wk, wv, woa, wqkv, wob, out, ws, (lptr)smem, w);
    else program<false>(x0, wq, wk, wv, woa, wqkv, wob, out, ws, (lptr)smem, w);
}

extern "C" void kernel_launch(void* const* d_in, const int* in_sizes, int n_in, void* d_out, int out_size, void* d_ws, size_t ws_size, hipStream_t stream) {
    (void)in_sizes; (void)n_in; (void)out_size; (void)ws_size;
    (void)hipFuncSetAttribute((const void*)il_fwd, hipFuncAttributeMaxDynamicSharedMemorySize, LDS_BYTES);
    hipLaunchKernelGGL(il_fwd, dim3(256), dim3(512), LDS_BYTES, stream, (const char*)d_in[0], (const char*)d_in[1], (const char*)d_in[2], (const char*)d_in[3], (const char*)d_in[4],
                       (const char*)d_in[6], (const char*)d_in[7], (char*)d_out, (char*)d_ws);
}
```

```cpp
#include <hip/hip_runtime.h>
#include <hip/hip_bf16.h>
#include <stdint.h>

typedef float f32x16 __attribute__((ext_vector_type(16)));
typedef float f32x8 __attribute__((ext_vector_type(8)));
typedef float f32x4 __attribute__((ext_vector_type(4)));
typedef float f32x2 __attribute__((ext_vector_type(2)));
typedef unsigned u32x4 __attribute__((ext_vector_type(4)));
typedef unsigned u32x2 __attribute__((ext_vector_type(2)));
typedef short s16x4 __attribute__((ext_vector_type(4)));
typedef __bf16 bf16x2 __attribute__((ext_vector_type(2)));
typedef __bf16 bf16x8 __attribute__((ext_vector_type(8)));
typedef __bf16 bf16x16 __attribute__((ext_vector_type(16)));
#define LAS __attribute__((address_space(3)))
#define GAS __attribute__((address_space(1)))
#if defined(__HIP_DEVICE_COMPILE__)
#define NOPK __attribute__((target("no-packed-fp32-ops")))
#else
#define NOPK
#endif
#define DEV NOPK __device__ __forceinline__
typedef LAS char* lptr;
DEV float fmax_(float a, float b) { return __builtin_fmaxf(a, b); }
DEV unsigned umax_(unsigned a, unsigned b) { return a > b ? a : b; }
DEV unsigned umin_(unsigned a, unsigned b) { return a < b ? a : b; }
DEV unsigned f2u(float a) { return __builtin_bit_cast(unsigned, a); }
DEV float u2f(unsigned a) { return __builtin_bit_cast(float, a); }
DEV bool all_(bool c) { return __builtin_amdgcn_ballot_w64(!c) == 0; }

constexpr int WS_E = 0, WS_RDY = 4096, WS_KAF = WS_RDY + 65536, WS_KBF = WS_KAF + 32768, WS_WT = 262144;
constexpr int WT_WOA = 0, WT_KVB = 512, WT_QB = 1536, WT_WOB = 2048, WT_QA = 2560, WT_KA = 3072, WT_VA = 3104, WT_N = 3136;
constexpr int WS_KVA = WS_WT + WT_N * 1024, WS_O = WS_KVA + 262144, WS_X = WS_O + 4 * 2097152, WS_ST = WS_X + 3 * 2097152;
constexpr int WS_KVB = WS_ST + 3 * 262144, WS_END = WS_KVB + 2 * 4194304;
constexpr int L_AT = 0, L_RING = 0, L_KVB = 65536, L_WQ = 67584, L_WK = 133120;
constexpr int L_CONST = 149504, L_QX = 153600, L_MEAN = 161792, L_RSTD = 162304, L_KMA = 162560, L_KMOWN = 162592, L_KMNB = 162624, L_LATE = 162656;
constexpr int L_RSTD4 = 147456;
constexpr int L_PARK = 65536, L_EX = 131072, L_XR = 49152;
constexpr int LDS_BYTES = 163840;
constexpr int HFS = 1056, HHS = 528, HTS = 32 * HFS;
constexpr unsigned SENT = 0xFFFFFFFFu, SPIN_CAP = 1u << 14;
constexpr float QSCALE = 0.25f * 1.4426950408889634f, LN_EPS = 1e-5f, NEG_BIG = -1e30f;
#define KA_TICKS0 80
#define KA_NP0 45
#define KA_NP 20
#define IL_KSLACK 1.5f
constexpr float KSLACK = IL_KSLACK;
#define HS_OA 15
#define HS_OB 15
#define HS_X 4
#define HS_M 8
#define HQ_D 10
#define KA_TICKS 60
#define OUT_AUX 0
#define KV_TICKS 70
#define IL_MBOUND 50.f
constexpr float MBOUND = IL_MBOUND;

#define STAMP(i) do {} while (0)
#define STAMPC(i) do {} while (0)
#define STAMPV(i, v) do {} while (0)
#define STAMP0(i) do {} while (0)
#define NRETRY
#define NRETRYP
#define NINC
#define BAR() asm volatile("s_waitcnt lgkmcnt(0)\n\ts_barrier" ::: "memory")
#define VM0() asm volatile("s_waitcnt vmcnt(0)" ::: "memory")
#define HEADSTART() __builtin_amdgcn_s_sleep(10)
DEV u32x4 ldsr(lptr p) { return *(const LAS u32x4*)p; }
DEV void ldsw(lptr p, u32x4 v) { *(LAS u32x4*)p = v; }
DEV f32x4 ldsrf(lptr p) { return *(const LAS f32x4*)p; }
DEV f32x16 mfma(u32x4 a, u32x4 b, f32x16 c) { return __builtin_amdgcn_mfma_f32_32x32x16_bf16(__builtin_bit_cast(bf16x8, a), __builtin_bit_cast(bf16x8, b), c, 0, 0, 0); }
DEV f32x16 smf(u32x4 a, f32x8 b, f32x16 c) { return __builtin_amdgcn_smfmac_f32_32x32x32_bf16(__builtin_bit_cast(bf16x8, a), __builtin_bit_cast(bf16x16, b), c, 0xDDDD, 0, 0); }
DEV f32x8 lo8(f32x16 v) { return __builtin_shufflevector(v, v, 0, 1, 2, 3, 4, 5, 6, 7); }
DEV f32x8 hi8(f32x16 v) { return __builtin_shufflevector(v, v, 8, 9, 10, 11, 12, 13, 14, 15); }
DEV unsigned pk2(float a, float b) { f32x2 f = {a, b}; return __builtin_bit_cast(unsigned, __builtin_convertvector(f, bf16x2)); }
DEV float bflo(unsigned u) { return __builtin_bit_cast(float, u << 16); }
DEV float bfhi(unsigned u) { return __builtin_bit_cast(float, u & 0xFFFF0000u); }
DEV float dot2(unsigned a, unsigned b, float c) { return __builtin_amdgcn_fdot2_f32_bf16(__builtin_bit_cast(bf16x2, a), __builtin_bit_cast(bf16x2, b), c, false); }
DEV float sq4(u32x4 a, float c) { return dot2(a[3], a[3], dot2(a[2], a[2], dot2(a[1], a[1], dot2(a[0], a[0], c)))); }
DEV float sm4(u32x4 a, unsigned o, float c) { return dot2(a[3], o, dot2(a[2], o, dot2(a[1], o, dot2(a[0], o, c)))); }
template <int CTRL> DEV float dpp(float x) { return __builtin_bit_cast(float, __builtin_amdgcn_update_dpp(0, __builtin_bit_cast(int, x), CTRL, 0xF, 0xF, true)); }
DEV float sum8(float x) { x += dpp<0xB1>(x); x += dpp<0x4E>(x); x += dpp<0x141>(x); return x; }
DEV float sum16(float x) { x = sum8(x); x += dpp<0x140>(x); return x; }
DEV float max16(float x) { x = fmax_(x, dpp<0xB1>(x)); x = fmax_(x, dpp<0x4E>(x)); x = fmax_(x, dpp<0x141>(x)); x = fmax_(x, dpp<0x140>(x)); return x; }
DEV float max32(float x) { x = max16(x); auto r = __builtin_amdgcn_permlane16_swap(f2u(x), f2u(x), false, false); return fmax_(u2f(r[0]), u2f(r[1])); }
DEV float addx32(float x) { auto r = __builtin_amdgcn_permlane32_swap(f2u(x), f2u(x), false, false); return u2f(r[0]) + u2f(r[1]); }
DEV float maxx32(float x) { auto r = __builtin_amdgcn_permlane32_swap(f2u(x), f2u(x), false, false); return fmax_(u2f(r[0]), u2f(r[1])); }
DEV unsigned mx4(u32x4 a) { return umax_(umax_(a[0], a[1]), umax_(a[2], a[3])); }
DEV int slot_of(int t) { return (t & 0x13) | ((t & 4) << 1) | ((t & 8) >> 1); }
DEV u32x4 trfrag(lptr p, int second) {
    s16x4 a = __builtin_amdgcn_ds_read_tr16_b64_v4i16((LAS s16x4*)p), b = __builtin_amdgcn_ds_read_tr16_b64_v4i16((LAS s16x4*)(p + second));
    u32x2 x = __builtin_bit_cast(u32x2, a), y = __builtin_bit_cast(u32x2, b);
    return (u32x4){x[0], x[1], y[0], y[1]};
}
DEV int only(bool c, int off) { return c ? off : 0x7C000000; }
typedef __amdgpu_buffer_rsrc_t rsrc_t;
DEV void st16(rsrc_t r, int off, u32x4 v) { __builtin_amdgcn_raw_buffer_store_b128(v, r, off, 0, 16); }
DEV void st16(rsrc_t r, unsigned vo, int so, u32x4 v) { __builtin_amdgcn_raw_buffer_store_b128(v, r, vo, so, 16); }
DEV void st8(rsrc_t r, unsigned vo, int so, u32x2 v) { __builtin_amdgcn_raw_buffer_store_b64(v, r, vo, so, 16); }
DEV void st8(rsrc_t r, int off, u32x2 v) { __builtin_amdgcn_raw_buffer_store_b64(v, r, off, 0, 16); }
DEV u32x4 ld16(rsrc_t r, int off) { return __builtin_amdgcn_raw_buffer_load_b128(r, off, 0, 16); }
DEV u32x4 ld16(rsrc_t r, unsigned vo, int so) { return __builtin_amdgcn_raw_buffer_load_b128(r, vo, so, 16); }
#define UNI(x) __builtin_amdgcn_readfirstlane(x)
DEV void gld1(u32x4& a, unsigned vo, u32x4 rd, int so) { asm volatile("s_nop 4\n\tbuffer_load_dwordx4 %0, %1, %2, %3 offen sc1" : "=&v"(a) : "v"(vo), "s"(rd), "s"(UNI(so)) : "memory"); }
DEV void gld2(u32x4& a, u32x4& b, unsigned vo, u32x4 rd, int so) {
    asm volatile("s_nop 4\n\tbuffer_load_dwordx4 %0, %2, %3, %4 offen sc1\n\tbuffer_load_dwordx4 %1, %2, %3, %4 offen offset:1024 sc1" : "=&v"(a), "=&v"(b) : "v"(vo), "s"(rd), "s"(UNI(so)) : "memory"); }
DEV void gld4(u32x4& a, u32x4& b, u32x4& c, u32x4& d, unsigned vo, u32x4 rd, int so) {
    asm volatile("s_nop 4\n\tbuffer_load_dwordx4 %0, %4, %5, %6 offen sc1\n\tbuffer_load_dwordx4 %1, %4, %5, %6 offen offset:1024 sc1\n\t"
                 "buffer_load_dwordx4 %2, %4, %5, %6 offen offset:2048 sc1\n\tbuffer_load_dwordx4 %3, %4, %5, %6 offen offset:3072 sc1"
                 : "=&v"(a), "=&v"(b), "=&v"(c), "=&v"(d) : "v"(vo), "s"(rd), "s"(UNI(so)) : "memory"); }
DEV const char* uptr(const char* p) { const size_t a = (size_t)p; return (const char*)(((size_t)(unsigned)UNI((int)(a >> 32)) << 32) | (unsigned)UNI((int)a)); }
template <int N> DEV void wait2(u32x4& a, u32x4& b) { asm volatile("s_waitcnt vmcnt(%2)" : "+v"(a), "+v"(b) : "i"(N) : "memory"); }
template <int N> DEV void wait4(u32x4& a, u32x4& b, u32x4& c, u32x4& d) { asm volatile("s_waitcnt vmcnt(%4)" : "+v"(a), "+v"(b), "+v"(c), "+v"(d) : "i"(N) : "memory"); }
DEV void dma1(unsigned vo, const char* sb, unsigned ldsb) {
    unsigned keep;
    asm volatile("s_mov_b32 %0, m0\n\ts_mov_b32 m0, %3\n\ts_nop 3\n\tglobal_load_lds_dwordx4 %1, %2\n\ts_mov_b32 m0, %0" : "=&s"(keep) : "v"(vo), "s"(uptr(sb)), "s"(UNI(ldsb)) : "memory"); }

DEV bool clean4(u32x4 a, u32x4 b, u32x4 c, u32x4 d) { return all_(umax_(umax_(mx4(a), mx4(b)), umax_(mx4(c), mx4(d))) != SENT); }
template <int N> DEV void stage4(u32x4& a, u32x4& b, u32x4& c, u32x4& d, unsigned vo, u32x4 rd, int sb, lptr dst NRETRYP) {
    wait4<N>(a, b, c, d);
    ldsw(dst, a); ldsw(dst + 1024, b); ldsw(dst + 2048, c); ldsw(dst + 3072, d);
    asm volatile("" ::: "memory");
    if (__builtin_expect(!clean4(a, b, c, d), 0)) {
        _Pragma("clang loop unroll(disable)") for (unsigned sp = 0; sp < SPIN_CAP; ++sp) { NINC gld4(a, b, c, d, vo, rd, sb); wait4<0>(a, b, c, d); if (clean4(a, b, c, d)) break; }
        ldsw(dst, a); ldsw(dst + 1024, b); ldsw(dst + 2048, c); ldsw(dst + 3072, d);
    }
}
DEV void gate(unsigned vo, u32x4 rd, int so, unsigned tag) {
    so = UNI(so);
    _Pragma("clang loop unroll(disable)") for (unsigned sp = 0; sp < SPIN_CAP; ++sp) {
        unsigned x;
        asm volatile("s_nop 4\n\tbuffer_load_dword %0, %1, %2, %3 offen sc1\n\ts_waitcnt vmcnt(0)" : "=&v"(x) : "v"(vo), "s"(rd), "s"(so) : "memory");
        if (all_(x == tag)) break;
        __builtin_amdgcn_s_sleep(4);
    }
}
template <bool G0> DEV unsigned filled_word(int b, int lane) {
    const int rb = 4 * (b & 7) + (b >> 6), n_ = rb - 1 - ((lane >> 3) & 3), nb = n_ >= 0 ? n_ : rb;
    return (G0 ? (b & ~56) | ((lane >> 3) << 3) : (nb >> 2) | (b & 56) | ((nb & 3) << 6)) * 128 + (lane & 7) * 4;
}
template <int N> DEV void valid4(u32x4& a, u32x4& b, u32x4& c, u32x4& d, unsigned vo, u32x4 rd, int sb NRETRYP) {
    wait4<N>(a, b, c, d);
    if (__builtin_expect(!clean4(a, b, c, d), 0)) {
        _Pragma("clang loop unroll(disable)") for (unsigned sp = 0; sp < SPIN_CAP; ++sp) { NINC gld4(a, b, c, d, vo, rd, sb); wait4<0>(a, b, c, d); if (clean4(a, b, c, d)) break; }
    }
}
template <bool FOUR> DEV void put_stats(lptr lds, int tok, float s, float q) {
    const float mean = s * (1.f / 512.f), var = fmax_(q * (1.f / 512.f) - mean * mean, 0.f);
    const unsigned hi = pk2(-mean, -mean);
    *(LAS u32x2*)(lds + L_MEAN + tok * 8) = (u32x2){hi, pk2(-mean - bfhi(hi), 0.f)};
    const float r = __builtin_amdgcn_rsqf(var + LN_EPS);
    *(LAS float*)(lds + L_RSTD + tok * 4) = r;
    if constexpr (FOUR) *(LAS f32x4*)(lds + L_RSTD4 + tok * 16) = (f32x4){r, r, r, r};
}
DEV u32x4 mean_frag(lptr lds, int tok) { const u32x2 m = *(LAS u32x2*)(lds + L_MEAN + tok * 8); return (u32x4){m[0], m[1], m[0], m[1]}; }
DEV u32x4 cs_frag(float cs, int h, float z) { cs = h ? z : cs; const unsigned hi = pk2(cs, z); return (u32x4){pk2(cs, cs - bflo(hi)), hi, f2u(z), f2u(z)}; }
struct StatLd { u32x2 a, b, c, d; };
DEV void stats_load(StatLd& v, unsigned vo, u32x4 rd, int so) {
    asm volatile("s_nop 4\n\tbuffer_load_dwordx2 %0, %4, %5, %6 offen sc1\n\tbuffer_load_dwordx2 %1, %4, %5, %6 offen offset:256 sc1\n\t"
                 "buffer_load_dwordx2 %2, %4, %5, %6 offen offset:512 sc1\n\tbuffer_load_dwordx2 %3, %4, %5, %6 offen offset:768 sc1"
                 : "=&v"(v.a), "=&v"(v.b), "=&v"(v.c), "=&v"(v.d) : "v"(vo), "s"(rd), "s"(UNI(so)) : "memory"); }
template <int N> DEV void stats_wait(StatLd& v) { asm volatile("s_waitcnt vmcnt(%4)" : "+v"(v.a), "+v"(v.b), "+v"(v.c), "+v"(v.d) : "i"(N) : "memory"); }
DEV float addx16(float x) { auto r = __builtin_amdgcn_permlane16_swap(f2u(x), f2u(x), false, false); return u2f(r[0]) + u2f(r[1]); }
template <int N> DEV void stage_stats(StatLd& v, unsigned vo, u32x4 rd, int so, lptr lds, int tok) {
    stats_wait<N>(v);
    _Pragma("clang loop unroll(disable)")
    for (unsigned sp = 0; !all_(umax_(umax_(umax_(v.a[0], v.a[1]), umax_(v.b[0], v.b[1])), umax_(umax_(v.c[0], v.c[1]), umax_(v.d[0], v.d[1]))) != SENT) && sp < SPIN_CAP; ++sp) { stats_load(v, vo, rd, so); stats_wait<0>(v); }
    const float s = (u2f(v.a[0]) + u2f(v.b[0])) + (u2f(v.c[0]) + u2f(v.d[0])), q = (u2f(v.a[1]) + u2f(v.b[1])) + (u2f(v.c[1]) + u2f(v.d[1]));
    put_stats<true>(lds, tok, addx32(addx16(s)), addx32(addx16(q)));
}

#define GS_LL \
    "ds_read_b128 v[200:203], %[w] offset:0\n ds_read_b128 v[204:207], %[a] offset:0\n ds_read_b128 v[208:211], %[w] offset:1024\n" \
    "ds_read_b128 v[212:215], %[a] offset:1024\n ds_read_b128 v[216:219], %[w] offset:2048\n ds_read_b128 v[220:223], %[a] offset:2048\n" \
    "ds_read_b128 v[224:227], %[w] offset:3072\n ds_read_b128 v[228:231], %[a] offset:3072\n s_waitcnt lgkmcnt(7)\n" \
    "v_mfma_f32_32x32x16_bf16 %[d], %[o], v[200:203], 0\n s_waitcnt lgkmcnt(6)\n v_mfma_f32_32x32x16_bf16 %[c], v[200:203], v[204:207], 0\n" \
    "ds_read_b128 v[200:203], %[w] offset:4096\n ds_read_b128 v[204:207], %[a] offset:4096\n s_waitcnt lgkmcnt(7)\n" \
    "v_mfma_f32_32x32x16_bf16 %[d], %[o], v[208:211], %[d]\n s_waitcnt lgkmcnt(6)\n v_mfma_f32_32x32x16_bf16 %[c], v[208:211], v[212:215], %[c]\n" \
    "ds_read_b128 v[208:211], %[w] offset:5120\n ds_read_b128 v[212:215], %[a] offset:5120\n s_waitcnt lgkmcnt(7)\n" \
    "v_mfma_f32_32x32x16_bf16 %[d], %[o], v[216:219], %[d]\n s_waitcnt lgkmcnt(6)\n v_mfma_f32_32x32x16_bf16 %[c], v[216:219], v[220:223], %[c]\n" \
    "ds_read_b128 v[216:219], %[w] offset:6144\n ds_read_b128 v[220:223], %[a] offset:6144\n s_waitcnt lgkmcnt(7)\n" \
    "v_mfma_f32_32x32x16_bf16 %[d], %[o], v[224:227], %[d]\n s_waitcnt lgkmcnt(6)\n v_mfma_f32_32x32x16_bf16 %[c], v[224:227], v[228:231], %[c]\n" \
    "ds_read_b128 v[224:227], %[w] offset:7168\n ds_read_b128 v[228:231], %[a] offset:7168\n s_waitcnt lgkmcnt(7)\n" \
    "v_mfma_f32_32x32x16_bf16 %[d], %[o], v[200:203], %[d]\n s_waitcnt lgkmcnt(6)\n v_mfma_f32_32x32x16_bf16 %[c], v[200:203], v[204:207], %[c]\n" \
    "ds_read_b128 v[200:203], %[w] offset:8192\n ds_read_b128 v[204:207], %[a] offset:8192\n s_waitcnt lgkmcnt(7)\n" \
    "v_mfma_f32_32x32x16_bf16 %[d], %[o], v[208:211], %[d]\n s_waitcnt lgkmcnt(6)\n v_mfma_f32_32x32x16_bf16 %[c], v[208:211], v[212:215], %[c]\n" \
    "ds_read_b128 v[208:211], %[w] offset:9216\n ds_read_b128 v[212:215], %[a] offset:9216\n s_waitcnt lgkmcnt(7)\n" \
    "v_mfma_f32_32x32x16_bf16 %[d], %[o], v[216:219], %[d]\n s_waitcnt lgkmcnt(6)\n v_mfma_f32_32x32x16_bf16 %[c], v[216:219], v[220:223], %[c]\n" \
    "ds_read_b128 v[216:219], %[w] offset:10240\n ds_read_b128 v[220:223], %[a] offset:10240\n s_waitcnt lgkmcnt(7)\n" \
    "v_mfma_f32_32x32x16_bf16 %[d], %[o], v[224:227], %[d]\n s_waitcnt lgkmcnt(6)\n v_mfma_f32_32x32x16_bf16 %[c], v[224:227], v[228:231], %[c]\n" \
    "ds_read_b128 v[224:227], %[w] offset:11264\n ds_read_b128 v[228:231], %[a] offset:11264\n s_waitcnt lgkmcnt(7)\n" \
    "v_mfma_f32_32x32x16_bf16 %[d], %[o], v[200:203], %[d]\n s_waitcnt lgkmcnt(6)\n v_mfma_f32_32x32x16_bf16 %[c], v[200:203], v[204:207], %[c]\n" \
    "ds_read_b128 v[200:203], %[w] offset:12288\n ds_read_b128 v[204:207], %[a] offset:12288\n s_waitcnt lgkmcnt(7)\n" \
    "v_mfma_f32_32x32x16_bf16 %[d], %[o], v[208:211], %[d]\n s_waitcnt lgkmcnt(6)\n v_mfma_f32_32x32x16_bf16 %[c], v[208:211], v[212:215], %[c]\n" \
    "ds_read_b128 v[208:211], %[w] offset:13312\n ds_read_b128 v[212:215], %[a] offset:13312\n s_waitcnt lgkmcnt(7)\n" \
    "v_mfma_f32_32x32x16_bf16 %[d], %[o], v[216:219], %[d]\n s_waitcnt lgkmcnt(6)\n v_mfma_f32_32x32x16_bf16 %[c], v[216:219], v[220:223], %[c]\n" \
    "ds_read_b128 v[216:219], %[w] offset:14336\n ds_read_b128 v[220:223], %[a] offset:14336\n s_waitcnt lgkmcnt(7)\n" \
    "v_mfma_f32_32x32x16_bf16 %[d], %[o], v[224:227], %[d]\n s_waitcnt lgkmcnt(6)\n v_mfma_f32_32x32x16_bf16 %[c], v[224:227], v[228:231], %[c]\n" \
    "ds_read_b128 v[224:227], %[w] offset:15360\n ds_read_b128 v[228:231], %[a] offset:15360\n s_waitcnt lgkmcnt(7)\n" \
    "v_mfma_f32_32x32x16_bf16 %[d], %[o], v[200:203], %[d]\n s_waitcnt lgkmcnt(6)\n v_mfma_f32_32x32x16_bf16 %[c], v[200:203], v[204:207], %[c]\n" \
    "ds_read_b128 v[200:203], %[w] offset:16384\n ds_read_b128 v[204:207], %[a] offset:16384\n s_waitcnt lgkmcnt(7)\n" \
    "v_mfma_f32_32x32x16_bf16 %[d], %[o], v[208:211], %[d]\n s_waitcnt lgkmcnt(6)\n v_mfma_f32_32x32x16_bf16 %[c], v[208:211], v[212:215], %[c]\n" \
    "ds_read_b128 v[208:211], %[w] offset:17408\n ds_read_b128 v[212:215], %[a] offset:17408\n s_waitcnt lgkmcnt(7)\n" \
    "v_mfma_f32_32x32x16_bf16 %[d], %[o], v[216:219], %[d]\n s_waitcnt lgkmcnt(6)\n v_mfma_f32_32x32x16_bf16 %[c], v[216:219], v[220:223], %[c]\n" \
    "ds_read_b128 v[216:219], %[w] offset:18432\n ds_read_b128 v[220:223], %[a] offset:18432\n s_waitcnt lgkmcnt(7)\n" \
    "v_mfma_f32_32x32x16_bf16 %[d], %[o], v[224:227], %[d]\n s_waitcnt lgkmcnt(6)\n v_mfma_f32_32x32x16_bf16 %[c], v[224:227], v[228:231], %[c]\n" \
    "ds_read_b128 v[224:227], %[w] offset:19456\n ds_read_b128 v[228:231], %[a] offset:19456\n s_waitcnt lgkmcnt(7)\n" \
    "v_mfma_f32_32x32x16_bf16 %[d], %[o], v[200:203], %[d]\n s_waitcnt lgkmcnt(6)\n v_mfma_f32_32x32x16_bf16 %[c], v[200:203], v[204:207], %[c]\n" \
    "ds_read_b128 v[200:203], %[w] offset:20480\n ds_read_b128 v[204:207], %[a] offset:20480\n s_waitcnt lgkmcnt(7)\n" \
    "v_mfma_f32_32x32x16_bf16 %[d], %[o], v[208:211], %[d]\n s_waitcnt lgkmcnt(6)\n v_mfma_f32_32x32x16_bf16 %[c], v[208:211], v[212:215], %[c]\n" \
    "ds_read_b128 v[208:211], %[w] offset:21504\n ds_read_b128 v[212:215], %[a] offset:21504\n s_waitcnt lgkmcnt(7)\n" \
    "v_mfma_f32_32x32x16_bf16 %[d], %[o], v[216:219], %[d]\n s_waitcnt lgkmcnt(6)\n v_mfma_f32_32x32x16_bf16 %[c], v[216:219], v[220:223], %[c]\n" \
    "ds_read_b128 v[216:219], %[w] offset:22528\n ds_read_b128 v[220:223], %[a] offset:22528\n s_waitcnt lgkmcnt(7)\n" \
    "v_mfma_f32_32x32x16_bf16 %[d], %[o], v[224:227], %[d]\n s_waitcnt lgkmcnt(6)\n v_mfma_f32_32x32x16_bf16 %[c], v[224:227], v[228:231], %[c]\n" \
    "ds_read_b128 v[224:227], %[w] offset:23552\n ds_read_b128 v[228:231], %[a] offset:23552\n s_waitcnt lgkmcnt(7)\n" \
    "v_mfma_f32_32x32x16_bf16 %[d], %[o], v[200:203], %[d]\n s_waitcnt lgkmcnt(6)\n v_mfma_f32_32x32x16_bf16 %[c], v[200:203], v[204:207], %[c]\n" \
    "ds_read_b128 v[200:203], %[w] offset:24576\n ds_read_b128 v[204:207], %[a] offset:24576\n s_waitcnt lgkmcnt(7)\n" \
    "v_mfma_f32_32x32x16_bf16 %[d], %[o], v[208:211], %[d]\n s_waitcnt lgkmcnt(6)\n v_mfma_f32_32x32x16_bf16 %[c], v[208:211], v[212:215], %[c]\n" \
    "ds_read_b128 v[208:211], %[w] offset:25600\n ds_read_b128 v[212:215], %[a] offset:25600\n s_waitcnt lgkmcnt(7)\n" \
    "v_mfma_f32_32x32x16_bf16 %[d], %[o], v[216:219], %[d]\n s_waitcnt lgkmcnt(6)\n v_mfma_f32_32x32x16_bf16 %[c], v[216:219], v[220:223], %[c]\n" \
    "ds_read_b128 v[216:219], %[w] offset:26624\n ds_read_b128 v[220:223], %[a] offset:26624\n s_waitcnt lgkmcnt(7)\n" \
    "v_mfma_f32_32x32x16_bf16 %[d], %[o], v[224:227], %[d]\n s_waitcnt lgkmcnt(6)\n v_mfma_f32_32x32x16_bf16 %[c], v[224:227], v[228:231], %[c]\n" \
    "ds_read_b128 v[224:227], %[w] offset:27648\n ds_read_b128 v[228:231], %[a] offset:27648\n s_waitcnt lgkmcnt(7)\n" \
    "v_mfma_f32_32x32x16_bf16 %[d], %[o], v[200:203], %[d]\n s_waitcnt lgkmcnt(6)\n v_mfma_f32_32x32x16_bf16 %[c], v[200:203], v[204:207], %[c]\n" \
    "ds_read_b128 v[200:203], %[w] offset:28672\n ds_read_b128 v[204:207], %[a] offset:28672\n s_waitcnt lgkmcnt(7)\n" \
    "v_mfma_f32_32x32x16_bf16 %[d], %[o], v[208:211], %[d]\n s_waitcnt lgkmcnt(6)\n v_mfma_f32_32x32x16_bf16 %[c], v[208:211], v[212:215], %[c]\n" \
    "ds_read_b128 v[208:211], %[w] offset:29696\n ds_read_b128 v[212:215], %[a] offset:29696\n s_waitcnt lgkmcnt(7)\n" \
    "v_mfma_f32_32x32x16_bf16 %[d], %[o], v[216:219], %[d]\n s_waitcnt lgkmcnt(6)\n v_mfma_f32_32x32x16_bf16 %[c], v[216:219], v[220:223], %[c]\n" \
    "ds_read_b128 v[216:219], %[w] offset:30720\n ds_read_b128 v[220:223], %[a] offset:30720\n s_waitcnt lgkmcnt(7)\n" \
    "v_mfma_f32_32x32x16_bf16 %[d], %[o], v[224:227], %[d]\n s_waitcnt lgkmcnt(6)\n v_mfma_f32_32x32x16_bf16 %[c], v[224:227], v[228:231], %[c]\n" \
    "ds_read_b128 v[224:227], %[w] offset:31744\n ds_read_b128 v[228:231], %[a] offset:31744\n s_waitcnt lgkmcnt(7)\n" \
    "v_mfma_f32_32x32x16_bf16 %[d], %[o], v[200:203], %[d]\n s_waitcnt lgkmcnt(6)\n v_mfma_f32_32x32x16_bf16 %[c], v[200:203], v[204:207], %[c]\n" \
    "s_waitcnt lgkmcnt(5)\n v_mfma_f32_32x32x16_bf16 %[d], %[o], v[208:211], %[d]\n s_waitcnt lgkmcnt(4)\n" \
    "v_mfma_f32_32x32x16_bf16 %[c], v[208:211], v[212:215], %[c]\n s_waitcnt lgkmcnt(3)\n v_mfma_f32_32x32x16_bf16 %[d], %[o], v[216:219], %[d]\n" \
    "s_waitcnt lgkmcnt(2)\n v_mfma_f32_32x32x16_bf16 %[c], v[216:219], v[220:223], %[c]\n s_waitcnt lgkmcnt(1)\n" \
    "v_mfma_f32_32x32x16_bf16 %[d], %[o], v[224:227], %[d]\n s_waitcnt lgkmcnt(0)\n v_mfma_f32_32x32x16_bf16 %[c], v[224:227], v[228:231], %[c]\n" \
    ""
#define GS_B8_0 \
    "ds_read_b128 %[t0], %[a] offset:%c[s]*0\n ds_read_b128 %[t1], %[a] offset:%c[s]*1\n ds_read_b128 %[t2], %[a] offset:%c[s]*2\n" \
    "ds_read_b128 %[t3], %[a] offset:%c[s]*3\n ds_read_b128 %[t4], %[a] offset:%c[s]*4\n ds_read_b128 %[t5], %[a] offset:%c[s]*5\n" \
    "s_waitcnt lgkmcnt(5)\n v_mfma_f32_32x32x16_bf16 %[c], %[w0], %[t0], %[c]\n ds_read_b128 %[t0], %[a] offset:%c[s]*6\n" \
    "s_waitcnt lgkmcnt(5)\n v_mfma_f32_32x32x16_bf16 %[c], %[w1], %[t1], %[c]\n ds_read_b128 %[t1], %[a] offset:%c[s]*7\n" \
    "s_waitcnt lgkmcnt(5)\n v_mfma_f32_32x32x16_bf16 %[c], %[w2], %[t2], %[c]\n ds_read_b128 %[t2], %[a] offset:%c[s]*8\n" \
    "s_waitcnt lgkmcnt(5)\n v_mfma_f32_32x32x16_bf16 %[c], %[w3], %[t3], %[c]\n ds_read_b128 %[t3], %[a] offset:%c[s]*9\n" \
    "s_waitcnt lgkmcnt(5)\n v_mfma_f32_32x32x16_bf16 %[c], %[w4], %[t4], %[c]\n ds_read_b128 %[t4], %[a] offset:%c[s]*10\n" \
    "s_waitcnt lgkmcnt(5)\n v_mfma_f32_32x32x16_bf16 %[c], %[w5], %[t5], %[c]\n ds_read_b128 %[t5], %[a] offset:%c[s]*11\n" \
    "s_waitcnt lgkmcnt(5)\n v_mfma_f32_32x32x16_bf16 %[c], %[w6], %[t0], %[c]\n ds_read_b128 %[t0], %[a] offset:%c[s]*12\n" \
    "s_waitcnt lgkmcnt(5)\n v_mfma_f32_32x32x16_bf16 %[c], %[w7], %[t1], %[c]\n ds_read_b128 %[t1], %[a] offset:%c[s]*13\n" \
    ""
#define GS_B8_1 \
    "s_waitcnt lgkmcnt(5)\n v_mfma_f32_32x32x16_bf16 %[c], %[w0], %[t2], %[c]\n ds_read_b128 %[t2], %[a] offset:%c[s]*14\n" \
    "s_waitcnt lgkmcnt(5)\n v_mfma_f32_32x32x16_bf16 %[c], %[w1], %[t3], %[c]\n ds_read_b128 %[t3], %[a] offset:%c[s]*15\n" \
    "s_waitcnt lgkmcnt(5)\n v_mfma_f32_32x32x16_bf16 %[c], %[w2], %[t4], %[c]\n ds_read_b128 %[t4], %[a] offset:%c[s]*16\n" \
    "s_waitcnt lgkmcnt(5)\n v_mfma_f32_32x32x16_bf16 %[c], %[w3], %[t5], %[c]\n ds_read_b128 %[t5], %[a] offset:%c[s]*17\n" \
    "s_waitcnt lgkmcnt(5)\n v_mfma_f32_32x32x16_bf16 %[c], %[w4], %[t0], %[c]\n ds_read_b128 %[t0], %[a] offset:%c[s]*18\n" \
    "s_waitcnt lgkmcnt(5)\n v_mfma_f32_32x32x16_bf16 %[c], %[w5], %[t1], %[c]\n ds_read_b128 %[t1], %[a] offset:%c[s]*19\n" \
    "s_waitcnt lgkmcnt(5)\n v_mfma_f32_32x32x16_bf16 %[c], %[w6], %[t2], %[c]\n ds_read_b128 %[t2], %[a] offset:%c[s]*20\n" \
    "s_waitcnt lgkmcnt(5)\n v_mfma_f32_32x32x16_bf16 %[c], %[w7], %[t3], %[c]\n ds_read_b128 %[t3], %[a] offset:%c[s]*21\n" \
    ""
#define GS_B8_2 \
    "s_waitcnt lgkmcnt(5)\n v_mfma_f32_32x32x16_bf16 %[c], %[w0], %[t4], %[c]\n ds_read_b128 %[t4], %[a] offset:%c[s]*22\n" \
    "s_waitcnt lgkmcnt(5)\n v_mfma_f32_32x32x16_bf16 %[c], %[w1], %[t5], %[c]\n ds_read_b128 %[t5], %[a] offset:%c[s]*23\n" \
    "s_waitcnt lgkmcnt(5)\n v_mfma_f32_32x32x16_bf16 %[c], %[w2], %[t0], %[c]\n ds_read_b128 %[t0], %[a] offset:%c[s]*24\n" \
    "s_waitcnt lgkmcnt(5)\n v_mfma_f32_32x32x16_bf16 %[c], %[w3], %[t1], %[c]\n ds_read_b128 %[t1], %[a] offset:%c[s]*25\n" \
    "s_waitcnt lgkmcnt(5)\n v_mfma_f32_32x32x16_bf16 %[c], %[w4], %[t2], %[c]\n ds_read_b128 %[t2], %[a] offset:%c[s]*26\n" \
    "s_waitcnt lgkmcnt(5)\n v_mfma_f32_32x32x16_bf16 %[c], %[w5], %[t3], %[c]\n ds_read_b128 %[t3], %[a] offset:%c[s]*27\n" \
    "s_waitcnt lgkmcnt(5)\n v_mfma_f32_32x32x16_bf16 %[c], %[w6], %[t4], %[c]\n ds_read_b128 %[t4], %[a] offset:%c[s]*28\n" \
    "s_waitcnt lgkmcnt(5)\n v_mfma_f32_32x32x16_bf16 %[c], %[w7], %[t5], %[c]\n ds_read_b128 %[t5], %[a] offset:%c[s]*29\n" \
    ""
#define GS_B8_3 \
    "s_waitcnt lgkmcnt(5)\n v_mfma_f32_32x32x16_bf16 %[c], %[w0], %[t0], %[c]\n ds_read_b128 %[t0], %[a] offset:%c[s]*30\n" \
    "s_waitcnt lgkmcnt(5)\n v_mfma_f32_32x32x16_bf16 %[c], %[w1], %[t1], %[c]\n ds_read_b128 %[t1], %[a] offset:%c[s]*31\n" \
    "s_waitcnt lgkmcnt(5)\n v_mfma_f32_32x32x16_bf16 %[c], %[w2], %[t2], %[c]\n s_waitcnt lgkmcnt(4)\n" \
    "v_mfma_f32_32x32x16_bf16 %[c], %[w3], %[t3], %[c]\n s_waitcnt lgkmcnt(3)\n v_mfma_f32_32x32x16_bf16 %[c], %[w4], %[t4], %[c]\n" \
    "s_waitcnt lgkmcnt(2)\n v_mfma_f32_32x32x16_bf16 %[c], %[w5], %[t5], %[c]\n s_waitcnt lgkmcnt(1)\n" \
    "v_mfma_f32_32x32x16_bf16 %[c], %[w6], %[t0], %[c]\n s_waitcnt lgkmcnt(0)\n v_mfma_f32_32x32x16_bf16 %[c], %[w7], %[t1], %[c]\n" \
    ""
#define GS_NB_0 \
    "ds_read_b128 %[t0], %[a] offset:%c[s]*0\n ds_read_b128 %[t1], %[a] offset:%c[s]*1\n ds_read_b128 %[t2], %[a] offset:%c[s]*2\n" \
    "ds_read_b128 %[t3], %[a] offset:%c[s]*3\n ds_read_b128 %[t4], %[a] offset:%c[s]*4\n ds_read_b128 %[t5], %[a] offset:%c[s]*5\n" \
    "s_waitcnt lgkmcnt(5)\n v_mfma_f32_32x32x16_bf16 %[c], %[w0], %[t0], 0\n ds_read_b128 %[t0], %[a] offset:%c[s]*6\n" \
    "s_waitcnt lgkmcnt(5)\n v_mfma_f32_32x32x16_bf16 %[c], %[w1], %[t1], %[c]\n ds_read_b128 %[t1], %[a] offset:%c[s]*7\n" \
    "s_waitcnt lgkmcnt(5)\n v_mfma_f32_32x32x16_bf16 %[c], %[w2], %[t2], %[c]\n ds_read_b128 %[t2], %[a] offset:%c[s]*8\n" \
    "s_waitcnt lgkmcnt(5)\n v_mfma_f32_32x32x16_bf16 %[c], %[w3], %[t3], %[c]\n ds_read_b128 %[t3], %[a] offset:%c[s]*9\n" \
    "s_waitcnt lgkmcnt(5)\n v_mfma_f32_32x32x16_bf16 %[c], %[w4], %[t4], %[c]\n ds_read_b128 %[t4], %[a] offset:%c[s]*10\n" \
    "s_waitcnt lgkmcnt(5)\n v_mfma_f32_32x32x16_bf16 %[c], %[w5], %[t5], %[c]\n ds_read_b128 %[t5], %[a] offset:%c[s]*11\n" \
    "s_waitcnt lgkmcnt(5)\n v_mfma_f32_32x32x16_bf16 %[c], %[w6], %[t0], %[c]\n ds_read_b128 %[t0], %[a] offset:%c[s]*12\n" \
    "s_waitcnt lgkmcnt(5)\n v_mfma_f32_32x32x16_bf16 %[c], %[w7], %[t1], %[c]\n ds_read_b128 %[t1], %[a] offset:%c[s]*13\n" \
    ""
#define GS_NB_1 \
    "s_waitcnt lgkmcnt(5)\n v_mfma_f32_32x32x16_bf16 %[c], %[w0], %[t2], %[c]\n ds_read_b128 %[t2], %[a] offset:%c[s]*14\n" \
    "s_waitcnt lgkmcnt(5)\n v_mfma_f32_32x32x16_bf16 %[c], %[w1], %[t3], %[c]\n ds_read_b128 %[t3], %[a] offset:%c[s]*15\n" \
    "s_waitcnt lgkmcnt(5)\n v_mfma_f32_32x32x16_bf16 %[c], %[w2], %[t4], %[c]\n ds_read_b128 %[t4], %[a] offset:%c[s]*16\n" \
    "s_waitcnt lgkmcnt(5)\n v_mfma_f32_32x32x16_bf16 %[c], %[w3], %[t5], %[c]\n ds_read_b128 %[t5], %[a] offset:%c[s]*17\n" \
    "s_waitcnt lgkmcnt(5)\n v_mfma_f32_32x32x16_bf16 %[c], %[w4], %[t0], %[c]\n ds_read_b128 %[t0], %[a] offset:%c[s]*18\n" \
    "s_waitcnt lgkmcnt(5)\n v_mfma_f32_32x32x16_bf16 %[c], %[w5], %[t1], %[c]\n ds_read_b128 %[t1], %[a] offset:%c[s]*19\n" \
    "s_waitcnt lgkmcnt(5)\n v_mfma_f32_32x32x16_bf16 %[c], %[w6], %[t2], %[c]\n ds_read_b128 %[t2], %[a] offset:%c[s]*20\n" \
    "s_waitcnt lgkmcnt(5)\n v_mfma_f32_32x32x16_bf16 %[c], %[w7], %[t3], %[c]\n ds_read_b128 %[t3], %[a] offset:%c[s]*21\n" \
    ""
#define GS_NB_2 \
    "s_waitcnt lgkmcnt(5)\n v_mfma_f32_32x32x16_bf16 %[c], %[w0], %[t4], %[c]\n ds_read_b128 %[t4], %[a] offset:%c[s]*22\n" \
    "s_waitcnt lgkmcnt(5)\n v_mfma_f32_32x32x16_bf16 %[c], %[w1], %[t5], %[c]\n ds_read_b128 %[t5], %[a] offset:%c[s]*23\n" \
    "s_waitcnt lgkmcnt(5)\n v_mfma_f32_32x32x16_bf16 %[c], %[w2], %[t0], %[c]\n ds_read_b128 %[t0], %[a] offset:%c[s]*24\n" \
    "s_waitcnt lgkmcnt(5)\n v_mfma_f32_32x32x16_bf16 %[c], %[w3], %[t1], %[c]\n ds_read_b128 %[t1], %[a] offset:%c[s]*25\n" \
    "s_waitcnt lgkmcnt(5)\n v_mfma_f32_32x32x16_bf16 %[c], %[w4], %[t2], %[c]\n ds_read_b128 %[t2], %[a] offset:%c[s]*26\n" \
    "s_waitcnt lgkmcnt(5)\n v_mfma_f32_32x32x16_bf16 %[c], %[w5], %[t3], %[c]\n ds_read_b128 %[t3], %[a] offset:%c[s]*27\n" \
    "s_waitcnt lgkmcnt(5)\n v_mfma_f32_32x32x16_bf16 %[c], %[w6], %[t4], %[c]\n ds_read_b128 %[t4], %[a] offset:%c[s]*28\n" \
    "s_waitcnt lgkmcnt(5)\n v_mfma_f32_32x32x16_bf16 %[c], %[w7], %[t5], %[c]\n ds_read_b128 %[t5], %[a] offset:%c[s]*29\n" \
    ""
#define GS_NB_3 \
    "s_waitcnt lgkmcnt(5)\n v_mfma_f32_32x32x16_bf16 %[c], %[w0], %[t0], %[c]\n ds_read_b128 %[t0], %[a] offset:%c[s]*30\n" \
    "s_waitcnt lgkmcnt(5)\n v_mfma_f32_32x32x16_bf16 %[c], %[w1], %[t1], %[c]\n ds_read_b128 %[t1], %[a] offset:%c[s]*31\n" \
    "s_waitcnt lgkmcnt(5)\n v_mfma_f32_32x32x16_bf16 %[c], %[w2], %[t2], %[c]\n s_waitcnt lgkmcnt(4)\n" \
    "v_mfma_f32_32x32x16_bf16 %[c], %[w3], %[t3], %[c]\n s_waitcnt lgkmcnt(3)\n v_mfma_f32_32x32x16_bf16 %[c], %[w4], %[t4], %[c]\n" \
    "s_waitcnt lgkmcnt(2)\n v_mfma_f32_32x32x16_bf16 %[c], %[w5], %[t5], %[c]\n s_waitcnt lgkmcnt(1)\n" \
    "v_mfma_f32_32x32x16_bf16 %[c], %[w6], %[t0], %[c]\n s_waitcnt lgkmcnt(0)\n v_mfma_f32_32x32x16_bf16 %[c], %[w7], %[t1], %[c]\n" \
    ""
#define GD_T_0 \
    "ds_read_b128 %[t0], %[a] offset:0\n ds_read_b128 %[t1], %[b] offset:0\n ds_read_b128 %[t2], %[a] offset:1024\n" \
    "ds_read_b128 %[t3], %[b] offset:1024\n ds_read_b128 %[t4], %[a] offset:2048\n ds_read_b128 %[t5], %[b] offset:2048\n" \
    "ds_read_b128 %[t6], %[a] offset:3072\n ds_read_b128 %[t7], %[b] offset:3072\n s_waitcnt lgkmcnt(7)\n" \
    "v_mfma_f32_32x32x16_bf16 %[c], %[w0], %[t0], 0\n ds_read_b128 %[t0], %[a] offset:4096\n s_waitcnt lgkmcnt(7)\n" \
    "v_mfma_f32_32x32x16_bf16 %[d], %[w0], %[t1], 0\n ds_read_b128 %[t1], %[b] offset:4096\n s_waitcnt lgkmcnt(7)\n" \
    "v_mfma_f32_32x32x16_bf16 %[c], %[w1], %[t2], %[c]\n ds_read_b128 %[t2], %[a] offset:5120\n s_waitcnt lgkmcnt(7)\n" \
    "v_mfma_f32_32x32x16_bf16 %[d], %[w1], %[t3], %[d]\n ds_read_b128 %[t3], %[b] offset:5120\n s_waitcnt lgkmcnt(7)\n" \
    "v_mfma_f32_32x32x16_bf16 %[c], %[w2], %[t4], %[c]\n ds_read_b128 %[t4], %[a] offset:6144\n s_waitcnt lgkmcnt(7)\n" \
    "v_mfma_f32_32x32x16_bf16 %[d], %[w2], %[t5], %[d]\n ds_read_b128 %[t5], %[b] offset:6144\n s_waitcnt lgkmcnt(7)\n" \
    "v_mfma_f32_32x32x16_bf16 %[c], %[w3], %[t6], %[c]\n ds_read_b128 %[t6], %[a] offset:7168\n s_waitcnt lgkmcnt(7)\n" \
    "v_mfma_f32_32x32x16_bf16 %[d], %[w3], %[t7], %[d]\n ds_read_b128 %[t7], %[b] offset:7168\n s_waitcnt lgkmcnt(7)\n" \
    "v_mfma_f32_32x32x16_bf16 %[c], %[w4], %[t0], %[c]\n ds_read_b128 %[t0], %[a] offset:8192\n s_waitcnt lgkmcnt(7)\n" \
    "v_mfma_f32_32x32x16_bf16 %[d], %[w4], %[t1], %[d]\n ds_read_b128 %[t1], %[b] offset:8192\n s_waitcnt lgkmcnt(7)\n" \
    "v_mfma_f32_32x32x16_bf16 %[c], %[w5], %[t2], %[c]\n ds_read_b128 %[t2], %[a] offset:9216\n s_waitcnt lgkmcnt(7)\n" \
    "v_mfma_f32_32x32x16_bf16 %[d], %[w5], %[t3], %[d]\n ds_read_b128 %[t3], %[b] offset:9216\n s_waitcnt lgkmcnt(7)\n" \
    "v_mfma_f32_32x32x16_bf16 %[c], %[w6], %[t4], %[c]\n ds_read_b128 %[t4], %[a] offset:10240\n s_waitcnt lgkmcnt(7)\n" \
    "v_mfma_f32_32x32x16_bf16 %[d], %[w6], %[t5], %[d]\n ds_read_b128 %[t5], %[b] offset:10240\n s_waitcnt lgkmcnt(7)\n" \
    "v_mfma_f32_32x32x16_bf16 %[c], %[w7], %[t6], %[c]\n ds_read_b128 %[t6], %[a] offset:11264\n s_waitcnt lgkmcnt(7)\n" \
    "v_mfma_f32_32x32x16_bf16 %[d], %[w7], %[t7], %[d]\n ds_read_b128 %[t7], %[b] offset:11264\n" \
    ""
#define GD_T_1 \
    "s_waitcnt lgkmcnt(7)\n v_mfma_f32_32x32x16_bf16 %[c], %[w0], %[t0], %[c]\n ds_read_b128 %[t0], %[a] offset:12288\n" \
    "s_waitcnt lgkmcnt(7)\n v_mfma_f32_32x32x16_bf16 %[d], %[w0], %[t1], %[d]\n ds_read_b128 %[t1], %[b] offset:12288\n" \
    "s_waitcnt lgkmcnt(7)\n v_mfma_f32_32x32x16_bf16 %[c], %[w1], %[t2], %[c]\n ds_read_b128 %[t2], %[a] offset:13312\n" \
    "s_waitcnt lgkmcnt(7)\n v_mfma_f32_32x32x16_bf16 %[d], %[w1], %[t3], %[d]\n ds_read_b128 %[t3], %[b] offset:13312\n" \
    "s_waitcnt lgkmcnt(7)\n v_mfma_f32_32x32x16_bf16 %[c], %[w2], %[t4], %[c]\n ds_read_b128 %[t4], %[a] offset:14336\n" \
    "s_waitcnt lgkmcnt(7)\n v_mfma_f32_32x32x16_bf16 %[d], %[w2], %[t5], %[d]\n ds_read_b128 %[t5], %[b] offset:14336\n" \
    "s_waitcnt lgkmcnt(7)\n v_mfma_f32_32x32x16_bf16 %[c], %[w3], %[t6], %[c]\n ds_read_b128 %[t6], %[a] offset:15360\n" \
    "s_waitcnt lgkmcnt(7)\n v_mfma_f32_32x32x16_bf16 %[d], %[w3], %[t7], %[d]\n ds_read_b128 %[t7], %[b] offset:15360\n" \
    "s_waitcnt lgkmcnt(7)\n v_mfma_f32_32x32x16_bf16 %[c], %[w4], %[t0], %[c]\n ds_read_b128 %[t0], %[a] offset:16384\n" \
    "s_waitcnt lgkmcnt(7)\n v_mfma_f32_32x32x16_bf16 %[d], %[w4], %[t1], %[d]\n ds_read_b128 %[t1], %[b] offset:16384\n" \
    "s_waitcnt lgkmcnt(7)\n v_mfma_f32_32x32x16_bf16 %[c], %[w5], %[t2], %[c]\n ds_read_b128 %[t2], %[a] offset:17408\n" \
    "s_waitcnt lgkmcnt(7)\n v_mfma_f32_32x32x16_bf16 %[d], %[w5], %[t3], %[d]\n ds_read_b128 %[t3], %[b] offset:17408\n" \
    "s_waitcnt lgkmcnt(7)\n v_mfma_f32_32x32x16_bf16 %[c], %[w6], %[t4], %[c]\n ds_read_b128 %[t4], %[a] offset:18432\n" \
    "s_waitcnt lgkmcnt(7)\n v_mfma_f32_32x32x16_bf16 %[d], %[w6], %[t5], %[d]\n ds_read_b128 %[t5], %[b] offset:18432\n" \
    "s_waitcnt lgkmcnt(7)\n v_mfma_f32_32x32x16_bf16 %[c], %[w7], %[t6], %[c]\n ds_read_b128 %[t6], %[a] offset:19456\n" \
    "s_waitcnt lgkmcnt(7)\n v_mfma_f32_32x32x16_bf16 %[d], %[w7], %[t7], %[d]\n ds_read_b128 %[t7], %[b] offset:19456\n" \
    ""
#define GD_T_2 \
    "s_waitcnt lgkmcnt(7)\n v_mfma_f32_32x32x16_bf16 %[c], %[w0], %[t0], %[c]\n ds_read_b128 %[t0], %[a] offset:20480\n" \
    "s_waitcnt lgkmcnt(7)\n v_mfma_f32_32x32x16_bf16 %[d], %[w0], %[t1], %[d]\n ds_read_b128 %[t1], %[b] offset:20480\n" \
    "s_waitcnt lgkmcnt(7)\n v_mfma_f32_32x32x16_bf16 %[c], %[w1], %[t2], %[c]\n ds_read_b128 %[t2], %[a] offset:21504\n" \
    "s_waitcnt lgkmcnt(7)\n v_mfma_f32_32x32x16_bf16 %[d], %[w1], %[t3], %[d]\n ds_read_b128 %[t3], %[b] offset:21504\n" \
    "s_waitcnt lgkmcnt(7)\n v_mfma_f32_32x32x16_bf16 %[c], %[w2], %[t4], %[c]\n ds_read_b128 %[t4], %[a] offset:22528\n" \
    "s_waitcnt lgkmcnt(7)\n v_mfma_f32_32x32x16_bf16 %[d], %[w2], %[t5], %[d]\n ds_read_b128 %[t5], %[b] offset:22528\n" \
    "s_waitcnt lgkmcnt(7)\n v_mfma_f32_32x32x16_bf16 %[c], %[w3], %[t6], %[c]\n ds_read_b128 %[t6], %[a] offset:23552\n" \
    "s_waitcnt lgkmcnt(7)\n v_mfma_f32_32x32x16_bf16 %[d], %[w3], %[t7], %[d]\n ds_read_b128 %[t7], %[b] offset:23552\n" \
    "s_waitcnt lgkmcnt(7)\n v_mfma_f32_32x32x16_bf16 %[c], %[w4], %[t0], %[c]\n ds_read_b128 %[t0], %[a] offset:24576\n" \
    "s_waitcnt lgkmcnt(7)\n v_mfma_f32_32x32x16_bf16 %[d], %[w4], %[t1], %[d]\n ds_read_b128 %[t1], %[b] offset:24576\n" \
    "s_waitcnt lgkmcnt(7)\n v_mfma_f32_32x32x16_bf16 %[c], %[w5], %[t2], %[c]\n ds_read_b128 %[t2], %[a] offset:25600\n" \
    "s_waitcnt lgkmcnt(7)\n v_mfma_f32_32x32x16_bf16 %[d], %[w5], %[t3], %[d]\n ds_read_b128 %[t3], %[b] offset:25600\n" \
    "s_waitcnt lgkmcnt(7)\n v_mfma_f32_32x32x16_bf16 %[c], %[w6], %[t4], %[c]\n ds_read_b128 %[t4], %[a] offset:26624\n" \
    "s_waitcnt lgkmcnt(7)\n v_mfma_f32_32x32x16_bf16 %[d], %[w6], %[t5], %[d]\n ds_read_b128 %[t5], %[b] offset:26624\n" \
    "s_waitcnt lgkmcnt(7)\n v_mfma_f32_32x32x16_bf16 %[c], %[w7], %[t6], %[c]\n ds_read_b128 %[t6], %[a] offset:27648\n" \
    "s_waitcnt lgkmcnt(7)\n v_mfma_f32_32x32x16_bf16 %[d], %[w7], %[t7], %[d]\n ds_read_b128 %[t7], %[b] offset:27648\n" \
    ""
#define GD_T_3 \
    "s_waitcnt lgkmcnt(7)\n v_mfma_f32_32x32x16_bf16 %[c], %[w0], %[t0], %[c]\n ds_read_b128 %[t0], %[a] offset:28672\n" \
    "s_waitcnt lgkmcnt(7)\n v_mfma_f32_32x32x16_bf16 %[d], %[w0], %[t1], %[d]\n ds_read_b128 %[t1], %[b] offset:28672\n" \
    "s_waitcnt lgkmcnt(7)\n v_mfma_f32_32x32x16_bf16 %[c], %[w1], %[t2], %[c]\n ds_read_b128 %[t2], %[a] offset:29696\n" \
    "s_waitcnt lgkmcnt(7)\n v_mfma_f32_32x32x16_bf16 %[d], %[w1], %[t3], %[d]\n ds_read_b128 %[t3], %[b] offset:29696\n" \
    "s_waitcnt lgkmcnt(7)\n v_mfma_f32_32x32x16_bf16 %[c], %[w2], %[t4], %[c]\n ds_read_b128 %[t4], %[a] offset:30720\n" \
    "s_waitcnt lgkmcnt(7)\n v_mfma_f32_32x32x16_bf16 %[d], %[w2], %[t5], %[d]\n ds_read_b128 %[t5], %[b] offset:30720\n" \
    "s_waitcnt lgkmcnt(7)\n v_mfma_f32_32x32x16_bf16 %[c], %[w3], %[t6], %[c]\n ds_read_b128 %[t6], %[a] offset:31744\n" \
    "s_waitcnt lgkmcnt(7)\n v_mfma_f32_32x32x16_bf16 %[d], %[w3], %[t7], %[d]\n ds_read_b128 %[t7], %[b] offset:31744\n" \
    "s_waitcnt lgkmcnt(7)\n v_mfma_f32_32x32x16_bf16 %[c], %[w4], %[t0], %[c]\n s_waitcnt lgkmcnt(6)\n" \
    "v_mfma_f32_32x32x16_bf16 %[d], %[w4], %[t1], %[d]\n s_waitcnt lgkmcnt(5)\n v_mfma_f32_32x32x16_bf16 %[c], %[w5], %[t2], %[c]\n" \
    "s_waitcnt lgkmcnt(4)\n v_mfma_f32_32x32x16_bf16 %[d], %[w5], %[t3], %[d]\n s_waitcnt lgkmcnt(3)\n" \
    "v_mfma_f32_32x32x16_bf16 %[c], %[w6], %[t4], %[c]\n s_waitcnt lgkmcnt(2)\n v_mfma_f32_32x32x16_bf16 %[d], %[w6], %[t5], %[d]\n" \
    "s_waitcnt lgkmcnt(1)\n v_mfma_f32_32x32x16_bf16 %[c], %[w7], %[t6], %[c]\n s_waitcnt lgkmcnt(0)\n" \
    "v_mfma_f32_32x32x16_bf16 %[d], %[w7], %[t7], %[d]\n" \
    ""
#define GD_N_0 \
    "ds_read_b128 %[t0], %[a] offset:0\n ds_read_b128 %[t1], %[b] offset:0\n ds_read_b128 %[t2], %[a] offset:1024\n" \
    "ds_read_b128 %[t3], %[b] offset:1024\n ds_read_b128 %[t4], %[a] offset:2048\n ds_read_b128 %[t5], %[b] offset:2048\n" \
    "ds_read_b128 %[t6], %[a] offset:3072\n ds_read_b128 %[t7], %[b] offset:3072\n s_waitcnt lgkmcnt(7)\n" \
    "v_mfma_f32_32x32x16_bf16 %[c], %[t0], %[w0], 0\n ds_read_b128 %[t0], %[a] offset:4096\n s_waitcnt lgkmcnt(7)\n" \
    "v_mfma_f32_32x32x16_bf16 %[d], %[t1], %[w0], 0\n ds_read_b128 %[t1], %[b] offset:4096\n s_waitcnt lgkmcnt(7)\n" \
    "v_mfma_f32_32x32x16_bf16 %[c], %[t2], %[w1], %[c]\n ds_read_b128 %[t2], %[a] offset:5120\n s_waitcnt lgkmcnt(7)\n" \
    "v_mfma_f32_32x32x16_bf16 %[d], %[t3], %[w1], %[d]\n ds_read_b128 %[t3], %[b] offset:5120\n s_waitcnt lgkmcnt(7)\n" \
    "v_mfma_f32_32x32x16_bf16 %[c], %[t4], %[w2], %[c]\n ds_read_b128 %[t4], %[a] offset:6144\n s_waitcnt lgkmcnt(7)\n" \
    "v_mfma_f32_32x32x16_bf16 %[d], %[t5], %[w2], %[d]\n ds_read_b128 %[t5], %[b] offset:6144\n s_waitcnt lgkmcnt(7)\n" \
    "v_mfma_f32_32x32x16_bf16 %[c], %[t6], %[w3], %[c]\n ds_read_b128 %[t6], %[a] offset:7168\n s_waitcnt lgkmcnt(7)\n" \
    "v_mfma_f32_32x32x16_bf16 %[d], %[t7], %[w3], %[d]\n ds_read_b128 %[t7], %[b] offset:7168\n s_waitcnt lgkmcnt(7)\n" \
    "v_mfma_f32_32x32x16_bf16 %[c], %[t0], %[w4], %[c]\n ds_read_b128 %[t0], %[a] offset:8192\n s_waitcnt lgkmcnt(7)\n" \
    "v_mfma_f32_32x32x16_bf16 %[d], %[t1], %[w4], %[d]\n ds_read_b128 %[t1], %[b] offset:8192\n s_waitcnt lgkmcnt(7)\n" \
    "v_mfma_f32_32x32x16_bf16 %[c], %[t2], %[w5], %[c]\n ds_read_b128 %[t2], %[a] offset:9216\n s_waitcnt lgkmcnt(7)\n" \
    "v_mfma_f32_32x32x16_bf16 %[d], %[t3], %[w5], %[d]\n ds_read_b128 %[t3], %[b] offset:9216\n s_waitcnt lgkmcnt(7)\n" \
    "v_mfma_f32_32x32x16_bf16 %[c], %[t4], %[w6], %[c]\n ds_read_b128 %[t4], %[a] offset:10240\n s_waitcnt lgkmcnt(7)\n" \
    "v_mfma_f32_32x32x16_bf16 %[d], %[t5], %[w6], %[d]\n ds_read_b128 %[t5], %[b] offset:10240\n s_waitcnt lgkmcnt(7)\n" \
    "v_mfma_f32_32x32x16_bf16 %[c], %[t6], %[w7], %[c]\n ds_read_b128 %[t6], %[a] offset:11264\n s_waitcnt lgkmcnt(7)\n" \
    "v_mfma_f32_32x32x16_bf16 %[d], %[t7], %[w7], %[d]\n ds_read_b128 %[t7], %[b] offset:11264\n" \
    ""
#define GD_N_1 \
    "s_waitcnt lgkmcnt(7)\n v_mfma_f32_32x32x16_bf16 %[c], %[t0], %[w0], %[c]\n ds_read_b128 %[t0], %[a] offset:12288\n" \
    "s_waitcnt lgkmcnt(7)\n v_mfma_f32_32x32x16_bf16 %[d], %[t1], %[w0], %[d]\n ds_read_b128 %[t1], %[b] offset:12288\n" \
    "s_waitcnt lgkmcnt(7)\n v_mfma_f32_32x32x16_bf16 %[c], %[t2], %[w1], %[c]\n ds_read_b128 %[t2], %[a] offset:13312\n" \
    "s_waitcnt lgkmcnt(7)\n v_mfma_f32_32x32x16_bf16 %[d], %[t3], %[w1], %[d]\n ds_read_b128 %[t3], %[b] offset:13312\n" \
    "s_waitcnt lgkmcnt(7)\n v_mfma_f32_32x32x16_bf16 %[c], %[t4], %[w2], %[c]\n ds_read_b128 %[t4], %[a] offset:14336\n" \
    "s_waitcnt lgkmcnt(7)\n v_mfma_f32_32x32x16_bf16 %[d], %[t5], %[w2], %[d]\n ds_read_b128 %[t5], %[b] offset:14336\n" \
    "s_waitcnt lgkmcnt(7)\n v_mfma_f32_32x32x16_bf16 %[c], %[t6], %[w3], %[c]\n ds_read_b128 %[t6], %[a] offset:15360\n" \
    "s_waitcnt lgkmcnt(7)\n v_mfma_f32_32x32x16_bf16 %[d], %[t7], %[w3], %[d]\n ds_read_b128 %[t7], %[b] offset:15360\n" \
    "s_waitcnt lgkmcnt(7)\n v_mfma_f32_32x32x16_bf16 %[c], %[t0], %[w4], %[c]\n ds_read_b128 %[t0], %[a] offset:16384\n" \
    "s_waitcnt lgkmcnt(7)\n v_mfma_f32_32x32x16_bf16 %[d], %[t1], %[w4], %[d]\n ds_read_b128 %[t1], %[b] offset:16384\n" \
    "s_waitcnt lgkmcnt(7)\n v_mfma_f32_32x32x16_bf16 %[c], %[t2], %[w5], %[c]\n ds_read_b128 %[t2], %[a] offset:17408\n" \
    "s_waitcnt lgkmcnt(7)\n v_mfma_f32_32x32x16_bf16 %[d], %[t3], %[w5], %[d]\n ds_read_b128 %[t3], %[b] offset:17408\n" \
    "s_waitcnt lgkmcnt(7)\n v_mfma_f32_32x32x16_bf16 %[c], %[t4], %[w6], %[c]\n ds_read_b128 %[t4], %[a] offset:18432\n" \
    "s_waitcnt lgkmcnt(7)\n v_mfma_f32_32x32x16_bf16 %[d], %[t5], %[w6], %[d]\n ds_read_b128 %[t5], %[b] offset:18432\n" \
    "s_waitcnt lgkmcnt(7)\n v_mfma_f32_32x32x16_bf16 %[c], %[t6], %[w7], %[c]\n ds_read_b128 %[t6], %[a] offset:19456\n" \
    "s_waitcnt lgkmcnt(7)\n v_mfma_f32_32x32x16_bf16 %[d], %[t7], %[w7], %[d]\n ds_read_b128 %[t7], %[b] offset:19456\n" \
    ""
#define GD_N_2 \
    "s_waitcnt lgkmcnt(7)\n v_mfma_f32_32x32x16_bf16 %[c], %[t0], %[w0], %[c]\n ds_read_b128 %[t0], %[a] offset:20480\n" \
    "s_waitcnt lgkmcnt(7)\n v_mfma_f32_32x32x16_bf16 %[d], %[t1], %[w0], %[d]\n ds_read_b128 %[t1], %[b] offset:20480\n" \
    "s_waitcnt lgkmcnt(7)\n v_mfma_f32_32x32x16_bf16 %[c], %[t2], %[w1], %[c]\n ds_read_b128 %[t2], %[a] offset:21504\n" \
    "s_waitcnt lgkmcnt(7)\n v_mfma_f32_32x32x16_bf16 %[d], %[t3], %[w1], %[d]\n ds_read_b128 %[t3], %[b] offset:21504\n" \
    "s_waitcnt lgkmcnt(7)\n v_mfma_f32_32x32x16_bf16 %[c], %[t4], %[w2], %[c]\n ds_read_b128 %[t4], %[a] offset:22528\n" \
    "s_waitcnt lgkmcnt(7)\n v_mfma_f32_32x32x16_bf16 %[d], %[t5], %[w2], %[d]\n ds_read_b128 %[t5], %[b] offset:22528\n" \
    "s_waitcnt lgkmcnt(7)\n v_mfma_f32_32x32x16_bf16 %[c], %[t6], %[w3], %[c]\n ds_read_b128 %[t6], %[a] offset:23552\n" \
    "s_waitcnt lgkmcnt(7)\n v_mfma_f32_32x32x16_bf16 %[d], %[t7], %[w3], %[d]\n ds_read_b128 %[t7], %[b] offset:23552\n" \
    "s_waitcnt lgkmcnt(7)\n v_mfma_f32_32x32x16_bf16 %[c], %[t0], %[w4], %[c]\n ds_read_b128 %[t0], %[a] offset:24576\n" \
    "s_waitcnt lgkmcnt(7)\n v_mfma_f32_32x32x16_bf16 %[d], %[t1], %[w4], %[d]\n ds_read_b128 %[t1], %[b] offset:24576\n" \
    "s_waitcnt lgkmcnt(7)\n v_mfma_f32_32x32x16_bf16 %[c], %[t2], %[w5], %[c]\n ds_read_b128 %[t2], %[a] offset:25600\n" \
    "s_waitcnt lgkmcnt(7)\n v_mfma_f32_32x32x16_bf16 %[d], %[t3], %[w5], %[d]\n ds_read_b128 %[t3], %[b] offset:25600\n" \
    "s_waitcnt lgkmcnt(7)\n v_mfma_f32_32x32x16_bf16 %[c], %[t4], %[w6], %[c]\n ds_read_b128 %[t4], %[a] offset:26624\n" \
    "s_waitcnt lgkmcnt(7)\n v_mfma_f32_32x32x16_bf16 %[d], %[t5], %[w6], %[d]\n ds_read_b128 %[t5], %[b] offset:26624\n" \
    "s_waitcnt lgkmcnt(7)\n v_mfma_f32_32x32x16_bf16 %[c], %[t6], %[w7], %[c]\n ds_read_b128 %[t6], %[a] offset:27648\n" \
    "s_waitcnt lgkmcnt(7)\n v_mfma_f32_32x32x16_bf16 %[d], %[t7], %[w7], %[d]\n ds_read_b128 %[t7], %[b] offset:27648\n" \
    ""
#define GD_N_3 \
    "s_waitcnt lgkmcnt(7)\n v_mfma_f32_32x32x16_bf16 %[c], %[t0], %[w0], %[c]\n ds_read_b128 %[t0], %[a] offset:28672\n" \
    "s_waitcnt lgkmcnt(7)\n v_mfma_f32_32x32x16_bf16 %[d], %[t1], %[w0], %[d]\n ds_read_b128 %[t1], %[b] offset:28672\n" \
    "s_waitcnt lgkmcnt(7)\n v_mfma_f32_32x32x16_bf16 %[c], %[t2], %[w1], %[c]\n ds_read_b128 %[t2], %[a] offset:29696\n" \
    "s_waitcnt lgkmcnt(7)\n v_mfma_f32_32x32x16_bf16 %[d], %[t3], %[w1], %[d]\n ds_read_b128 %[t3], %[b] offset:29696\n" \
    "s_waitcnt lgkmcnt(7)\n v_mfma_f32_32x32x16_bf16 %[c], %[t4], %[w2], %[c]\n ds_read_b128 %[t4], %[a] offset:30720\n" \
    "s_waitcnt lgkmcnt(7)\n v_mfma_f32_32x32x16_bf16 %[d], %[t5], %[w2], %[d]\n ds_read_b128 %[t5], %[b] offset:30720\n" \
    "s_waitcnt lgkmcnt(7)\n v_mfma_f32_32x32x16_bf16 %[c], %[t6], %[w3], %[c]\n ds_read_b128 %[t6], %[a] offset:31744\n" \
    "s_waitcnt lgkmcnt(7)\n v_mfma_f32_32x32x16_bf16 %[d], %[t7], %[w3], %[d]\n ds_read_b128 %[t7], %[b] offset:31744\n" \
    "s_waitcnt lgkmcnt(7)\n v_mfma_f32_32x32x16_bf16 %[c], %[t0], %[w4], %[c]\n s_waitcnt lgkmcnt(6)\n" \
    "v_mfma_f32_32x32x16_bf16 %[d], %[t1], %[w4], %[d]\n s_waitcnt lgkmcnt(5)\n v_mfma_f32_32x32x16_bf16 %[c], %[t2], %[w5], %[c]\n" \
    "s_waitcnt lgkmcnt(4)\n v_mfma_f32_32x32x16_bf16 %[d], %[t3], %[w5], %[d]\n s_waitcnt lgkmcnt(3)\n" \
    "v_mfma_f32_32x32x16_bf16 %[c], %[t4], %[w6], %[c]\n s_waitcnt lgkmcnt(2)\n v_mfma_f32_32x32x16_bf16 %[d], %[t5], %[w6], %[d]\n" \
    "s_waitcnt lgkmcnt(1)\n v_mfma_f32_32x32x16_bf16 %[c], %[t6], %[w7], %[c]\n s_waitcnt lgkmcnt(0)\n" \
    "v_mfma_f32_32x32x16_bf16 %[d], %[t7], %[w7], %[d]\n" \
    ""
#define WIN_Q0 [w0] "{v[40:43]}"(W[0]), [w1] "{v[44:47]}"(W[1]), [w2] "{v[48:51]}"(W[2]), [w3] "{v[52:55]}"(W[3]), [w4] "{v[56:59]}"(W[4]), [w5] "{v[60:63]}"(W[5]), [w6] "{v[64:67]}"(W[6]), [w7] "{v[68:71]}"(W[7])
#define WOUT_Q0 [w0] "=&{v[40:43]}"(W[0]), [w1] "=&{v[44:47]}"(W[1]), [w2] "=&{v[48:51]}"(W[2]), [w3] "=&{v[52:55]}"(W[3]), [w4] "=&{v[56:59]}"(W[4]), [w5] "=&{v[60:63]}"(W[5]), [w6] "=&{v[64:67]}"(W[6]), [w7] "=&{v[68:71]}"(W[7])
#define WIO_Q0 "+{v[40:43]}"(W[0]), "+{v[44:47]}"(W[1]), "+{v[48:51]}"(W[2]), "+{v[52:55]}"(W[3]), "+{v[56:59]}"(W[4]), "+{v[60:63]}"(W[5]), "+{v[64:67]}"(W[6]), "+{v[68:71]}"(W[7])
#define POUT_Q0 [p0] "=&{v[8:11]}"(P[0]), [p1] "=&{v[12:15]}"(P[1])
#define WIN_Q1 [w0] "{v[72:75]}"(W[8]), [w1] "{v[76:79]}"(W[9]), [w2] "{v[80:83]}"(W[10]), [w3] "{v[84:87]}"(W[11]), [w4] "{v[88:91]}"(W[12]), [w5] "{v[92:95]}"(W[13]), [w6] "{v[96:99]}"(W[14]), [w7] "{v[100:103]}"(W[15])
#define WOUT_Q1 [w0] "=&{v[72:75]}"(W[8]), [w1] "=&{v[76:79]}"(W[9]), [w2] "=&{v[80:83]}"(W[10]), [w3] "=&{v[84:87]}"(W[11]), [w4] "=&{v[88:91]}"(W[12]), [w5] "=&{v[92:95]}"(W[13]), [w6] "=&{v[96:99]}"(W[14]), [w7] "=&{v[100:103]}"(W[15])
#define WIO_Q1 "+{v[72:75]}"(W[8]), "+{v[76:79]}"(W[9]), "+{v[80:83]}"(W[10]), "+{v[84:87]}"(W[11]), "+{v[88:91]}"(W[12]), "+{v[92:95]}"(W[13]), "+{v[96:99]}"(W[14]), "+{v[100:103]}"(W[15])
#define POUT_Q1 [p0] "=&{v[16:19]}"(P[2]), [p1] "=&{v[20:23]}"(P[3])
#define WIN_Q2 [w0] "{v[104:107]}"(W[16]), [w1] "{v[108:111]}"(W[17]), [w2] "{v[112:115]}"(W[18]), [w3] "{v[116:119]}"(W[19]), [w4] "{v[120:123]}"(W[20]), [w5] "{v[124:127]}"(W[21]), [w6] "{v[128:131]}"(W[22]), [w7] "{v[132:135]}"(W[23])
#define WOUT_Q2 [w0] "=&{v[104:107]}"(W[16]), [w1] "=&{v[108:111]}"(W[17]), [w2] "=&{v[112:115]}"(W[18]), [w3] "=&{v[116:119]}"(W[19]), [w4] "=&{v[120:123]}"(W[20]), [w5] "=&{v[124:127]}"(W[21]), [w6] "=&{v[128:131]}"(W[22]), [w7] "=&{v[132:135]}"(W[23])
#define WIO_Q2 "+{v[104:107]}"(W[16]), "+{v[108:111]}"(W[17]), "+{v[112:115]}"(W[18]), "+{v[116:119]}"(W[19]), "+{v[120:123]}"(W[20]), "+{v[124:127]}"(W[21]), "+{v[128:131]}"(W[22]), "+{v[132:135]}"(W[23])
#define POUT_Q2 [p0] "=&{v[24:27]}"(P[4]), [p1] "=&{v[28:31]}"(P[5])
#define WIN_Q3 [w0] "{v[136:139]}"(W[24]), [w1] "{v[140:143]}"(W[25]), [w2] "{v[144:147]}"(W[26]), [w3] "{v[148:151]}"(W[27]), [w4] "{v[152:155]}"(W[28]), [w5] "{v[156:159]}"(W[29]), [w6] "{v[160:163]}"(W[30]), [w7] "{v[164:167]}"(W[31])
#define WOUT_Q3 [w0] "=&{v[136:139]}"(W[24]), [w1] "=&{v[140:143]}"(W[25]), [w2] "=&{v[144:147]}"(W[26]), [w3] "=&{v[148:151]}"(W[27]), [w4] "=&{v[152:155]}"(W[28]), [w5] "=&{v[156:159]}"(W[29]), [w6] "=&{v[160:163]}"(W[30]), [w7] "=&{v[164:167]}"(W[31])
#define WIO_Q3 "+{v[136:139]}"(W[24]), "+{v[140:143]}"(W[25]), "+{v[144:147]}"(W[26]), "+{v[148:151]}"(W[27]), "+{v[152:155]}"(W[28]), "+{v[156:159]}"(W[29]), "+{v[160:163]}"(W[30]), "+{v[164:167]}"(W[31])
#define POUT_Q3 [p0] "=&{v[32:35]}"(P[6]), [p1] "=&{v[36:39]}"(P[7])
#define MM_ "v_mfma_f32_32x32x16_bf16 "
struct Tmp { u32x4 t0, t1, t2, t3, t4, t5, t6, t7; };
#define T6_(m) [t0] m "{v[200:203]}"(t.t0), [t1] m "{v[204:207]}"(t.t1), [t2] m "{v[208:211]}"(t.t2), [t3] m "{v[212:215]}"(t.t3), [t4] m "{v[216:219]}"(t.t4), [t5] m "{v[220:223]}"(t.t5)
#define T8_(m) T6_(m), [t6] m "{v[224:227]}"(t.t6), [t7] m "{v[228:231]}"(t.t7)
#define GS_(TXT, CM, TM, WI) asm volatile(TXT : [c] CM "{v[168:183]}"(c), T6_(TM) : WI, [a] "v"(a), [s] "i"(ST) : "memory")
#define GD_(TT, TN, CM, TM, WI) asm volatile("s_cmp_lg_u32 %[f], 0\n\ts_cbranch_scc1 1f\n" TN "s_branch 2f\n1:\n" TT "2:" \
    : [c] CM "{v[168:183]}"(c), [d] CM "{v[184:199]}"(d), T8_(TM) : WI, [a] "v"(a), [b] "v"(b), [f] "s"(T) : "scc", "memory")
DEV void gs32_res(f32x16& c, const u32x4* W, unsigned a) {
    constexpr int ST = 1024; Tmp t;
    GS_(GS_B8_0, "+", "=&", WIN_Q0); GS_(GS_B8_1, "+", "+", WIN_Q1); GS_(GS_B8_2, "+", "+", WIN_Q2); GS_(GS_B8_3, "+", "+", WIN_Q3);
}
template <int ST> DEV void gs32(f32x16& c, const u32x4* W, unsigned a) {
    Tmp t; GS_(GS_NB_0, "=&", "=&", WIN_Q0); GS_(GS_NB_1, "+", "+", WIN_Q1); GS_(GS_NB_2, "+", "+", WIN_Q2); GS_(GS_NB_3, "+", "+", WIN_Q3);
}
#define MN_(w, ci) MM_ "%[c], %[o], %[w" #w "], " ci "\n\t"
#define CS_(CM, C0, WI, END) asm volatile("s_nop 1\n\t" MN_(0, C0) MN_(1, "%[c]") MN_(2, "%[c]") MN_(3, "%[c]") MN_(4, "%[c]") MN_(5, "%[c]") MN_(6, "%[c]") MN_(7, "%[c]") END \
                                          : [c] CM "{v[232:247]}"(c) : WI, [o] "v"(ones))
DEV void colsum_a(f32x16& c, const u32x4* W, u32x4 ones) { CS_("=&", "0", WIN_Q0, ""); CS_("+", "%[c]", WIN_Q1, ""); }
DEV float colsum_b(f32x16& c, const u32x4* W, u32x4 ones) { CS_("+", "%[c]", WIN_Q2, ""); CS_("+", "%[c]", WIN_Q3, "s_nop 7\n\ts_nop 7"); return c[0]; }
DEV float colsum32(const u32x4* W, u32x4 ones) { f32x16 c; colsum_a(c, W, ones); return colsum_b(c, W, ones); }
DEV float colsum8(const u32x4* W, u32x4 ones) { f32x16 c = mfma(ones, W[0], (f32x16)(0.f)); for (int j = 1; j < 8; ++j) c = mfma(ones, W[j], c); return c[0]; }
DEV float gs32_ll(f32x16& c, unsigned w, unsigned a, u32x4 ones) {
    f32x16 d;
    asm volatile(GS_LL "s_nop 7\n\ts_nop 7" : [c] "=&{v[168:183]}"(c), [d] "=&{v[232:247]}"(d) : [w] "v"(w), [a] "v"(a), [o] "v"(ones)
                 : "memory", "v200", "v201", "v202", "v203", "v204", "v205", "v206", "v207", "v208", "v209", "v210", "v211", "v212", "v213", "v214", "v215",
                   "v216", "v217", "v218", "v219", "v220", "v221", "v222", "v223", "v224", "v225", "v226", "v227", "v228", "v229", "v230", "v231");
    return d[0];
}
template <int Q> DEV void gd(int T, f32x16& c, f32x16& d, Tmp& t, const u32x4* W, unsigned a, unsigned b) {
    if constexpr (Q == 0) GD_(GD_T_0, GD_N_0, "=&", "=&", WIN_Q0); else if constexpr (Q == 1) GD_(GD_T_1, GD_N_1, "+", "+", WIN_Q1);
    else if constexpr (Q == 2) GD_(GD_T_2, GD_N_2, "+", "+", WIN_Q2); else GD_(GD_T_3, GD_N_3, "+", "+", WIN_Q3);
}
#define WLD_(WO) asm volatile("s_nop 4\n\tbuffer_load_dwordx4 %[w0], %[vo], %[rd], %[s0] offen sc1\n\tbuffer_load_dwordx4 %[w1], %[vo], %[rd], %[s0] offen offset:1024 sc1\n\t" \
                 "buffer_load_dwordx4 %[w2], %[vo], %[rd], %[s0] offen offset:2048 sc1\n\tbuffer_load_dwordx4 %[w3], %[vo], %[rd], %[s0] offen offset:3072 sc1\n\t" \
                 "buffer_load_dwordx4 %[w4], %[vo], %[rd], %[s1] offen sc1\n\tbuffer_load_dwordx4 %[w5], %[vo], %[rd], %[s1] offen offset:1024 sc1\n\t" \
                 "buffer_load_dwordx4 %[w6], %[vo], %[rd], %[s1] offen offset:2048 sc1\n\tbuffer_load_dwordx4 %[w7], %[vo], %[rd], %[s1] offen offset:3072 sc1" \
                 : WO : [vo] "v"(vo), [rd] "s"(rd), [s0] "s"(UNI(so + Q * 8192)), [s1] "s"(UNI(so + Q * 8192 + 4096)) : "memory")
template <int Q> DEV void wld(u32x4* W, unsigned vo, u32x4 rd, int so) {
    if constexpr (Q == 0) WLD_(WOUT_Q0); else if constexpr (Q == 1) WLD_(WOUT_Q1); else if constexpr (Q == 2) WLD_(WOUT_Q2); else WLD_(WOUT_Q3); }
DEV void mean_step(f32x16& c, u32x4 cf, u32x4 mf) { asm volatile("s_nop 1\n\t" MM_ "%0, %1, %2, %0\n\ts_nop 7\n\ts_nop 7" : "+{v[168:183]}"(c) : "v"(cf), "v"(mf)); }
DEV void mean_step2(int T, f32x16& c, f32x16& d, u32x4 cf, u32x4 m0, u32x4 m1) {
    asm volatile("s_cmp_lg_u32 %5, 0\n\ts_cbranch_scc1 1f\n\t" MM_ "%0, %3, %2, %0\n\t" MM_ "%1, %4, %2, %1\n\ts_branch 2f\n1:\n\t" MM_ "%0, %2, %3, %0\n\t" MM_ "%1, %2, %4, %1\n2:\n\ts_nop 7\n\ts_nop 7"
                 : "+{v[168:183]}"(c), "+{v[184:199]}"(d) : "v"(cf), "v"(m0), "v"(m1), "s"(T) : "scc"); }
DEV void settle(f32x16& a) { asm volatile("s_nop 7\n\ts_nop 7" : "+v"(a)); }

#define MQA0_0 \
    "v_exp_f32 v200, v200\n v_exp_f32 v201, v201\n v_smfmac_f32_32x32x32_bf16 v[8:23], v[244:247], v[224:231], v251\n" \
    "v_exp_f32 v202, v202\n v_exp_f32 v203, v203\n v_exp_f32 v204, v204\n v_mfma_f32_32x32x16_bf16 v[216:231], v[236:239], v[24:27], v[184:199]\n" \
    "ds_read_b128 v[244:247], v249 offset:512\n" \
    "ds_read_b128 v[28:31], v248 offset:1024\n" \
    "buffer_load_dwordx2 v[32:33], v39, %[rd], s49 offen sc1\n" \
    "v_exp_f32 v205, v205\n v_exp_f32 v206, v206\n v_exp_f32 v207, v207\n v_exp_f32 v208, v208\n v_exp_f32 v209, v209\n" \
    "s_waitcnt lgkmcnt(2)\n v_smfmac_f32_32x32x32_bf16 v[168:183], v[240:243], v[200:207], v251\n" \
    "v_exp_f32 v210, v210\n v_exp_f32 v211, v211\n v_exp_f32 v212, v212\n v_exp_f32 v213, v213\n v_exp_f32 v214, v214\n" \
    "v_exp_f32 v215, v215\n v_exp_f32 v216, v216\n v_exp_f32 v217, v217\n s_waitcnt lgkmcnt(0)\n v_smfmac_f32_32x32x32_bf16 v[168:183], v[244:247], v[208:215], v251\n" \
    "v_exp_f32 v218, v218\n v_exp_f32 v219, v219\n v_exp_f32 v220, v220\n v_mfma_f32_32x32x16_bf16 v[200:215], v[28:31], v[232:235], v[184:199]\n" \
    "v_exp_f32 v221, v221\n v_exp_f32 v222, v222\n v_exp_f32 v223, v223\n v_exp_f32 v224, v224\n v_exp_f32 v225, v225\n" \
    "v_smfmac_f32_32x32x32_bf16 v[8:23], v[240:243], v[216:223], v251\n" \
    "ds_read_b128 v[240:243], v249 offset:1024\n" \
    "v_exp_f32 v226, v226\n v_exp_f32 v227, v227\n v_exp_f32 v228, v228\n v_exp_f32 v229, v229\n v_exp_f32 v230, v230\n" \
    "v_exp_f32 v231, v231\n v_exp_f32 v200, v200\n v_exp_f32 v201, v201\n v_smfmac_f32_32x32x32_bf16 v[8:23], v[244:247], v[224:231], v251\n" \
    "v_exp_f32 v202, v202\n v_exp_f32 v203, v203\n v_exp_f32 v204, v204\n v_mfma_f32_32x32x16_bf16 v[216:231], v[28:31], v[24:27], v[184:199]\n" \
    "ds_read_b128 v[244:247], v249 offset:1536\n" \
    "ds_read_b128 v[236:239], v248 offset:2048\n" \
    "buffer_store_dwordx4 v[4:7], v36, %[rd], %[r0] offen sc1\n" \
    "v_exp_f32 v205, v205\n v_exp_f32 v206, v206\n v_exp_f32 v207, v207\n v_exp_f32 v208, v208\n v_exp_f32 v209, v209\n" \
    "s_waitcnt lgkmcnt(2)\n v_smfmac_f32_32x32x32_bf16 v[168:183], v[240:243], v[200:207], v251\n" \
    "v_exp_f32 v210, v210\n v_exp_f32 v211, v211\n v_exp_f32 v212, v212\n v_exp_f32 v213, v213\n v_exp_f32 v214, v214\n" \
    "v_exp_f32 v215, v215\n v_exp_f32 v216, v216\n v_exp_f32 v217, v217\n s_waitcnt lgkmcnt(0)\n v_smfmac_f32_32x32x32_bf16 v[168:183], v[244:247], v[208:215], v251\n" \
    "v_exp_f32 v218, v218\n v_exp_f32 v219, v219\n v_exp_f32 v220, v220\n v_mfma_f32_32x32x16_bf16 v[200:215], v[236:239], v[232:235], v[184:199]\n" \
    "s_waitcnt vmcnt(1)\n s_movk_i32 s51, 0x2000\n 40:\n v_cmp_ne_u32 vcc, s50, v33\n s_cbranch_vccz 50f\n" \
    "s_sub_u32 s51, s51, 1\n s_cbranch_scc1 50f\n s_add_u32 s48, s48, 1\n s_sleep 4\n buffer_load_dwordx2 v[32:33], v39, %[rd], s49 offen sc1\n" \
    "s_waitcnt vmcnt(0)\n s_branch 40b\n 50:\n s_add_u32 m0, s41, 4096\n s_add_u32 s40, s47, s46\n" \
    "buffer_load_dwordx4 v36, %[rd], s40 offen sc1 lds\n" \
    "buffer_load_dwordx4 v36, %[rd], s40 offen offset:1024 sc1 lds\n" \
    "s_add_u32 s46, s46, 8192\n s_and_b32 s46, s46, 0xffff\n ds_max_u32 v35, v32\n v_exp_f32 v221, v221\n v_exp_f32 v222, v222\n" \
    "v_exp_f32 v223, v223\n v_exp_f32 v224, v224\n v_exp_f32 v225, v225\n v_smfmac_f32_32x32x32_bf16 v[8:23], v[240:243], v[216:223], v251\n" \
    "ds_read_b128 v[240:243], v249 offset:2048\n" \
    "v_exp_f32 v226, v226\n v_exp_f32 v227, v227\n v_exp_f32 v228, v228\n v_exp_f32 v229, v229\n v_exp_f32 v230, v230\n" \
    "v_exp_f32 v231, v231\n v_exp_f32 v200, v200\n v_exp_f32 v201, v201\n v_smfmac_f32_32x32x32_bf16 v[8:23], v[244:247], v[224:231], v251\n" \
    "v_exp_f32 v202, v202\n v_exp_f32 v203, v203\n v_exp_f32 v204, v204\n v_mfma_f32_32x32x16_bf16 v[216:231], v[236:239], v[24:27], v[184:199]\n" \
    "ds_read_b128 v[244:247], v249 offset:2560\n" \
    "ds_read_b128 v[28:31], v248 offset:3072\n" \
    "v_exp_f32 v205, v205\n v_exp_f32 v206, v206\n v_exp_f32 v207, v207\n v_exp_f32 v208, v208\n v_exp_f32 v209, v209\n" \
    "s_waitcnt lgkmcnt(2)\n v_smfmac_f32_32x32x32_bf16 v[168:183], v[240:243], v[200:207], v251\n" \
    "v_exp_f32 v210, v210\n v_exp_f32 v211, v211\n v_exp_f32 v212, v212\n v_exp_f32 v213, v213\n v_exp_f32 v214, v214\n" \
    "v_exp_f32 v215, v215\n v_exp_f32 v216, v216\n v_exp_f32 v217, v217\n s_waitcnt lgkmcnt(0)\n v_smfmac_f32_32x32x32_bf16 v[168:183], v[244:247], v[208:215], v251\n" \
    "v_exp_f32 v218, v218\n v_exp_f32 v219, v219\n v_exp_f32 v220, v220\n v_mfma_f32_32x32x16_bf16 v[200:215], v[28:31], v[232:235], v[184:199]\n" \
    "v_exp_f32 v221, v221\n v_exp_f32 v222, v222\n v_exp_f32 v223, v223\n v_exp_f32 v224, v224\n v_exp_f32 v225, v225\n" \
    "v_smfmac_f32_32x32x32_bf16 v[8:23], v[240:243], v[216:223], v251\n" \
    "ds_read_b128 v[240:243], v249 offset:3072\n" \
    "v_exp_f32 v226, v226\n v_exp_f32 v227, v227\n v_exp_f32 v228, v228\n v_exp_f32 v229, v229\n v_exp_f32 v230, v230\n" \
    "v_exp_f32 v231, v231\n v_exp_f32 v200, v200\n v_exp_f32 v201, v201\n v_smfmac_f32_32x32x32_bf16 v[8:23], v[244:247], v[224:231], v251\n" \
    "v_exp_f32 v202, v202\n v_exp_f32 v203, v203\n v_exp_f32 v204, v204\n v_mfma_f32_32x32x16_bf16 v[216:231], v[28:31], v[24:27], v[184:199]\n" \
    "ds_read_b128 v[244:247], v249 offset:3584\n" \
    "s_waitcnt vmcnt(0)\n s_barrier\n ds_read_b128 v[236:239], v248 offset:4096\n" \
    "ds_read_b32 v34, v35\n" \
    "s_add_u32 m0, s41, 8192\n s_add_u32 s40, s47, s46\n buffer_load_dwordx4 v36, %[rd], s40 offen sc1 lds\n" \
    "v_exp_f32 v205, v205\n v_exp_f32 v206, v206\n v_exp_f32 v207, v207\n v_exp_f32 v208, v208\n v_exp_f32 v209, v209\n" \
    "s_waitcnt lgkmcnt(2)\n v_smfmac_f32_32x32x32_bf16 v[168:183], v[240:243], v[200:207], v251\n" \
    "v_exp_f32 v210, v210\n v_exp_f32 v211, v211\n v_exp_f32 v212, v212\n v_exp_f32 v213, v213\n v_exp_f32 v214, v214\n" \
    "v_exp_f32 v215, v215\n v_exp_f32 v216, v216\n v_exp_f32 v217, v217\n s_waitcnt lgkmcnt(0)\n v_smfmac_f32_32x32x32_bf16 v[168:183], v[244:247], v[208:215], v251\n" \
    "v_exp_f32 v218, v218\n v_exp_f32 v219, v219\n v_exp_f32 v220, v220\n v_mfma_f32_32x32x16_bf16 v[200:215], v[236:239], v[232:235], v[184:199]\n" \
    "buffer_load_dwordx4 v36, %[rd], s40 offen offset:1024 sc1 lds\n" \
    "s_add_u32 s46, s46, 8192\n s_and_b32 s46, s46, 0xffff\n v_exp_f32 v221, v221\n v_exp_f32 v222, v222\n v_exp_f32 v223, v223\n" \
    "v_exp_f32 v224, v224\n v_exp_f32 v225, v225\n v_smfmac_f32_32x32x32_bf16 v[8:23], v[240:243], v[216:223], v251\n" \
    "v_add_u32 v249, v249, v250\n ds_read_b128 v[240:243], v249\n" \
    "v_exp_f32 v226, v226\n v_exp_f32 v227, v227\n v_exp_f32 v228, v228\n v_exp_f32 v229, v229\n v_exp_f32 v230, v230\n" \
    "v_exp_f32 v231, v231\n" \
    ""
#define MQA0_1 \
    "v_exp_f32 v200, v200\n v_exp_f32 v201, v201\n v_smfmac_f32_32x32x32_bf16 v[8:23], v[244:247], v[224:231], v251\n" \
    "v_exp_f32 v202, v202\n v_exp_f32 v203, v203\n v_exp_f32 v204, v204\n v_mfma_f32_32x32x16_bf16 v[216:231], v[236:239], v[24:27], v[184:199]\n" \
    "ds_read_b128 v[244:247], v249 offset:512\n" \
    "ds_read_b128 v[28:31], v248 offset:5120\n" \
    "v_exp_f32 v205, v205\n v_exp_f32 v206, v206\n v_exp_f32 v207, v207\n v_exp_f32 v208, v208\n v_exp_f32 v209, v209\n" \
    "s_waitcnt lgkmcnt(2)\n v_smfmac_f32_32x32x32_bf16 v[168:183], v[240:243], v[200:207], v251\n" \
    "v_exp_f32 v210, v210\n v_exp_f32 v211, v211\n v_exp_f32 v212, v212\n v_exp_f32 v213, v213\n v_exp_f32 v214, v214\n" \
    "v_exp_f32 v215, v215\n v_exp_f32 v216, v216\n v_exp_f32 v217, v217\n s_waitcnt lgkmcnt(0)\n v_smfmac_f32_32x32x32_bf16 v[168:183], v[244:247], v[208:215], v251\n" \
    "v_exp_f32 v218, v218\n v_exp_f32 v219, v219\n v_exp_f32 v220, v220\n v_mfma_f32_32x32x16_bf16 v[200:215], v[28:31], v[232:235], v[184:199]\n" \
    "v_exp_f32 v221, v221\n v_exp_f32 v222, v222\n v_exp_f32 v223, v223\n v_exp_f32 v224, v224\n v_exp_f32 v225, v225\n" \
    "v_smfmac_f32_32x32x32_bf16 v[8:23], v[240:243], v[216:223], v251\n" \
    "ds_read_b128 v[240:243], v249 offset:1024\n" \
    "v_exp_f32 v226, v226\n v_exp_f32 v227, v227\n v_exp_f32 v228, v228\n v_exp_f32 v229, v229\n v_exp_f32 v230, v230\n" \
    "v_exp_f32 v231, v231\n v_exp_f32 v200, v200\n v_exp_f32 v201, v201\n v_smfmac_f32_32x32x32_bf16 v[8:23], v[244:247], v[224:231], v251\n" \
    "v_exp_f32 v202, v202\n v_exp_f32 v203, v203\n v_exp_f32 v204, v204\n v_mfma_f32_32x32x16_bf16 v[216:231], v[28:31], v[24:27], v[184:199]\n" \
    "ds_read_b128 v[244:247], v249 offset:1536\n" \
    "ds_read_b128 v[236:239], v248 offset:6144\n" \
    "buffer_store_dwordx4 v[4:7], v36, %[rd], %[r0] offen sc1\n" \
    "v_exp_f32 v205, v205\n v_exp_f32 v206, v206\n v_exp_f32 v207, v207\n v_exp_f32 v208, v208\n v_exp_f32 v209, v209\n" \
    "s_waitcnt lgkmcnt(2)\n v_smfmac_f32_32x32x32_bf16 v[168:183], v[240:243], v[200:207], v251\n" \
    "v_exp_f32 v210, v210\n v_exp_f32 v211, v211\n v_exp_f32 v212, v212\n v_exp_f32 v213, v213\n v_exp_f32 v214, v214\n" \
    "v_exp_f32 v215, v215\n v_exp_f32 v216, v216\n v_exp_f32 v217, v217\n s_waitcnt lgkmcnt(0)\n v_smfmac_f32_32x32x32_bf16 v[168:183], v[244:247], v[208:215], v251\n" \
    "v_exp_f32 v218, v218\n v_exp_f32 v219, v219\n v_exp_f32 v220, v220\n v_mfma_f32_32x32x16_bf16 v[200:215], v[236:239], v[232:235], v[184:199]\n" \
    "v_exp_f32 v221, v221\n v_exp_f32 v222, v222\n v_exp_f32 v223, v223\n v_exp_f32 v224, v224\n v_exp_f32 v225, v225\n" \
    "v_smfmac_f32_32x32x32_bf16 v[8:23], v[240:243], v[216:223], v251\n" \
    "ds_read_b128 v[240:243], v249 offset:2048\n" \
    "v_exp_f32 v226, v226\n v_exp_f32 v227, v227\n v_exp_f32 v228, v228\n v_exp_f32 v229, v229\n v_exp_f32 v230, v230\n" \
    "v_exp_f32 v231, v231\n v_exp_f32 v200, v200\n v_exp_f32 v201, v201\n v_smfmac_f32_32x32x32_bf16 v[8:23], v[244:247], v[224:231], v251\n" \
    "v_exp_f32 v202, v202\n v_exp_f32 v203, v203\n v_exp_f32 v204, v204\n v_mfma_f32_32x32x16_bf16 v[216:231], v[236:239], v[24:27], v[184:199]\n" \
    "ds_read_b128 v[244:247], v249 offset:2560\n" \
    "ds_read_b128 v[28:31], v248 offset:7168\n" \
    "buffer_load_dword %[f], %[fv], %[rd], %[fo] offen sc1\n" \
    "v_exp_f32 v205, v205\n v_exp_f32 v206, v206\n v_exp_f32 v207, v207\n v_exp_f32 v208, v208\n v_exp_f32 v209, v209\n" \
    "s_waitcnt lgkmcnt(2)\n v_smfmac_f32_32x32x32_bf16 v[168:183], v[240:243], v[200:207], v251\n" \
    "v_exp_f32 v210, v210\n v_exp_f32 v211, v211\n v_exp_f32 v212, v212\n v_exp_f32 v213, v213\n v_exp_f32 v214, v214\n" \
    "v_exp_f32 v215, v215\n v_exp_f32 v216, v216\n v_exp_f32 v217, v217\n s_waitcnt lgkmcnt(0)\n v_smfmac_f32_32x32x32_bf16 v[168:183], v[244:247], v[208:215], v251\n" \
    "v_exp_f32 v218, v218\n v_exp_f32 v219, v219\n v_exp_f32 v220, v220\n v_mfma_f32_32x32x16_bf16 v[200:215], v[28:31], v[232:235], v[184:199]\n" \
    "v_exp_f32 v221, v221\n v_exp_f32 v222, v222\n v_exp_f32 v223, v223\n v_exp_f32 v224, v224\n v_exp_f32 v225, v225\n" \
    "v_smfmac_f32_32x32x32_bf16 v[8:23], v[240:243], v[216:223], v251\n" \
    "ds_read_b128 v[240:243], v249 offset:3072\n" \
    "v_exp_f32 v226, v226\n v_exp_f32 v227, v227\n v_exp_f32 v228, v228\n v_exp_f32 v229, v229\n v_exp_f32 v230, v230\n" \
    "v_exp_f32 v231, v231\n v_exp_f32 v200, v200\n v_exp_f32 v201, v201\n v_smfmac_f32_32x32x32_bf16 v[8:23], v[244:247], v[224:231], v251\n" \
    "v_exp_f32 v202, v202\n v_exp_f32 v203, v203\n v_exp_f32 v204, v204\n v_mfma_f32_32x32x16_bf16 v[216:231], v[28:31], v[24:27], v[184:199]\n" \
    "ds_read_b128 v[244:247], v249 offset:3584\n" \
    "s_waitcnt vmcnt(2)\n s_barrier\n ds_read_b128 v[236:239], v248 offset:8192\n" \
    "s_add_u32 m0, s41, 0\n s_add_u32 s40, s47, s46\n buffer_load_dwordx4 v36, %[rd], s40 offen sc1 lds\n" \
    "v_exp_f32 v205, v205\n v_exp_f32 v206, v206\n v_exp_f32 v207, v207\n v_exp_f32 v208, v208\n v_exp_f32 v209, v209\n" \
    "s_waitcnt lgkmcnt(2)\n v_smfmac_f32_32x32x32_bf16 v[168:183], v[240:243], v[200:207], v251\n" \
    "v_exp_f32 v210, v210\n v_exp_f32 v211, v211\n v_exp_f32 v212, v212\n v_exp_f32 v213, v213\n v_exp_f32 v214, v214\n" \
    "v_exp_f32 v215, v215\n v_exp_f32 v216, v216\n v_exp_f32 v217, v217\n s_waitcnt lgkmcnt(0)\n v_smfmac_f32_32x32x32_bf16 v[168:183], v[244:247], v[208:215], v251\n" \
    "v_exp_f32 v218, v218\n v_exp_f32 v219, v219\n v_exp_f32 v220, v220\n v_mfma_f32_32x32x16_bf16 v[200:215], v[236:239], v[232:235], v[184:199]\n" \
    "buffer_load_dwordx4 v36, %[rd], s40 offen offset:1024 sc1 lds\n" \
    "s_add_u32 s46, s46, 8192\n s_and_b32 s46, s46, 0xffff\n v_exp_f32 v221, v221\n v_exp_f32 v222, v222\n v_exp_f32 v223, v223\n" \
    "v_exp_f32 v224, v224\n v_exp_f32 v225, v225\n v_smfmac_f32_32x32x32_bf16 v[8:23], v[240:243], v[216:223], v251\n" \
    "v_add_u32 v249, v249, v250\n ds_read_b128 v[240:243], v249\n" \
    "v_exp_f32 v226, v226\n v_exp_f32 v227, v227\n v_exp_f32 v228, v228\n v_exp_f32 v229, v229\n v_exp_f32 v230, v230\n" \
    "v_exp_f32 v231, v231\n" \
    ""
#define MQA0_2 \
    "v_exp_f32 v200, v200\n v_exp_f32 v201, v201\n v_smfmac_f32_32x32x32_bf16 v[8:23], v[244:247], v[224:231], v251\n" \
    "v_exp_f32 v202, v202\n v_exp_f32 v203, v203\n v_exp_f32 v204, v204\n v_mfma_f32_32x32x16_bf16 v[216:231], v[236:239], v[24:27], v[184:199]\n" \
    "ds_read_b128 v[244:247], v249 offset:512\n" \
    "ds_read_b128 v[28:31], v248 offset:9216\n" \
    "buffer_load_dwordx4 v[40:43], v36, %[rd], s44 offen offset:0 sc1\n" \
    "v_exp_f32 v205, v205\n v_exp_f32 v206, v206\n v_exp_f32 v207, v207\n v_exp_f32 v208, v208\n v_exp_f32 v209, v209\n" \
    "s_waitcnt lgkmcnt(2)\n v_smfmac_f32_32x32x32_bf16 v[168:183], v[240:243], v[200:207], v251\n" \
    "v_exp_f32 v210, v210\n v_exp_f32 v211, v211\n v_exp_f32 v212, v212\n v_exp_f32 v213, v213\n v_exp_f32 v214, v214\n" \
    "v_exp_f32 v215, v215\n v_exp_f32 v216, v216\n v_exp_f32 v217, v217\n s_waitcnt lgkmcnt(0)\n v_smfmac_f32_32x32x32_bf16 v[168:183], v[244:247], v[208:215], v251\n" \
    "v_exp_f32 v218, v218\n v_exp_f32 v219, v219\n v_exp_f32 v220, v220\n v_mfma_f32_32x32x16_bf16 v[200:215], v[28:31], v[232:235], v[184:199]\n" \
    "buffer_load_dwordx4 v[44:47], v36, %[rd], s44 offen offset:1024 sc1\n" \
    "v_exp_f32 v221, v221\n v_exp_f32 v222, v222\n v_exp_f32 v223, v223\n v_exp_f32 v224, v224\n v_exp_f32 v225, v225\n" \
    "v_smfmac_f32_32x32x32_bf16 v[8:23], v[240:243], v[216:223], v251\n" \
    "ds_read_b128 v[240:243], v249 offset:1024\n" \
    "v_exp_f32 v226, v226\n v_exp_f32 v227, v227\n v_exp_f32 v228, v228\n v_exp_f32 v229, v229\n v_exp_f32 v230, v230\n" \
    "v_exp_f32 v231, v231\n v_exp_f32 v200, v200\n v_exp_f32 v201, v201\n v_smfmac_f32_32x32x32_bf16 v[8:23], v[244:247], v[224:231], v251\n" \
    "v_exp_f32 v202, v202\n v_exp_f32 v203, v203\n v_exp_f32 v204, v204\n v_mfma_f32_32x32x16_bf16 v[216:231], v[28:31], v[24:27], v[184:199]\n" \
    "ds_read_b128 v[244:247], v249 offset:1536\n" \
    "ds_read_b128 v[236:239], v248 offset:10240\n" \
    "buffer_load_dwordx4 v[48:51], v36, %[rd], s44 offen offset:2048 sc1\n" \
    "buffer_store_dwordx4 v[4:7], v36, %[rd], %[r0] offen sc1\n" \
    "v_exp_f32 v205, v205\n v_exp_f32 v206, v206\n v_exp_f32 v207, v207\n v_exp_f32 v208, v208\n v_exp_f32 v209, v209\n" \
    "s_waitcnt lgkmcnt(2)\n v_smfmac_f32_32x32x32_bf16 v[168:183], v[240:243], v[200:207], v251\n" \
    "v_exp_f32 v210, v210\n v_exp_f32 v211, v211\n v_exp_f32 v212, v212\n v_exp_f32 v213, v213\n v_exp_f32 v214, v214\n" \
    "v_exp_f32 v215, v215\n v_exp_f32 v216, v216\n v_exp_f32 v217, v217\n s_waitcnt lgkmcnt(0)\n v_smfmac_f32_32x32x32_bf16 v[168:183], v[244:247], v[208:215], v251\n" \
    "v_exp_f32 v218, v218\n v_exp_f32 v219, v219\n v_exp_f32 v220, v220\n v_mfma_f32_32x32x16_bf16 v[200:215], v[236:239], v[232:235], v[184:199]\n" \
    "buffer_load_dwordx4 v[52:55], v36, %[rd], s44 offen offset:3072 sc1\n" \
    "s_mov_b32 m0, s43\n s_add_u32 s43, s43, 2048\n buffer_load_dwordx4 v36, %[rd], s42 offen sc1 lds\n" \
    "v_exp_f32 v221, v221\n v_exp_f32 v222, v222\n v_exp_f32 v223, v223\n v_exp_f32 v224, v224\n v_exp_f32 v225, v225\n" \
    "v_smfmac_f32_32x32x32_bf16 v[8:23], v[240:243], v[216:223], v251\n" \
    "ds_read_b128 v[240:243], v249 offset:2048\n" \
    "v_exp_f32 v226, v226\n v_exp_f32 v227, v227\n v_exp_f32 v228, v228\n v_exp_f32 v229, v229\n v_exp_f32 v230, v230\n" \
    "v_exp_f32 v231, v231\n v_exp_f32 v200, v200\n v_exp_f32 v201, v201\n v_smfmac_f32_32x32x32_bf16 v[8:23], v[244:247], v[224:231], v251\n" \
    "v_exp_f32 v202, v202\n v_exp_f32 v203, v203\n v_exp_f32 v204, v204\n v_mfma_f32_32x32x16_bf16 v[216:231], v[236:239], v[24:27], v[184:199]\n" \
    "ds_read_b128 v[244:247], v249 offset:2560\n" \
    "ds_read_b128 v[28:31], v248 offset:11264\n" \
    "buffer_load_dwordx4 v[56:59], v36, %[rd], s45 offen offset:0 sc1\n" \
    "v_exp_f32 v205, v205\n v_exp_f32 v206, v206\n v_exp_f32 v207, v207\n v_exp_f32 v208, v208\n v_exp_f32 v209, v209\n" \
    "s_waitcnt lgkmcnt(2)\n v_smfmac_f32_32x32x32_bf16 v[168:183], v[240:243], v[200:207], v251\n" \
    "v_exp_f32 v210, v210\n v_exp_f32 v211, v211\n v_exp_f32 v212, v212\n v_exp_f32 v213, v213\n v_exp_f32 v214, v214\n" \
    "v_exp_f32 v215, v215\n v_exp_f32 v216, v216\n v_exp_f32 v217, v217\n s_waitcnt lgkmcnt(0)\n v_smfmac_f32_32x32x32_bf16 v[168:183], v[244:247], v[208:215], v251\n" \
    "v_exp_f32 v218, v218\n v_exp_f32 v219, v219\n v_exp_f32 v220, v220\n v_mfma_f32_32x32x16_bf16 v[200:215], v[28:31], v[232:235], v[184:199]\n" \
    "buffer_load_dwordx4 v[60:63], v36, %[rd], s45 offen offset:1024 sc1\n" \
    "buffer_load_dwordx4 v36, %[rd], s42 offen offset:1024 sc1 lds\n" \
    "s_add_u32 s42, s42, 2048\n v_exp_f32 v221, v221\n v_exp_f32 v222, v222\n v_exp_f32 v223, v223\n v_exp_f32 v224, v224\n" \
    "v_exp_f32 v225, v225\n v_smfmac_f32_32x32x32_bf16 v[8:23], v[240:243], v[216:223], v251\n" \
    "ds_read_b128 v[240:243], v249 offset:3072\n" \
    "v_exp_f32 v226, v226\n v_exp_f32 v227, v227\n v_exp_f32 v228, v228\n v_exp_f32 v229, v229\n v_exp_f32 v230, v230\n" \
    "v_exp_f32 v231, v231\n v_exp_f32 v200, v200\n v_exp_f32 v201, v201\n v_smfmac_f32_32x32x32_bf16 v[8:23], v[244:247], v[224:231], v251\n" \
    "v_exp_f32 v202, v202\n v_exp_f32 v203, v203\n v_exp_f32 v204, v204\n v_mfma_f32_32x32x16_bf16 v[216:231], v[28:31], v[24:27], v[184:199]\n" \
    "ds_read_b128 v[244:247], v249 offset:3584\n" \
    "s_waitcnt vmcnt(9)\n s_barrier\n ds_read_b128 v[236:239], v248 offset:0\n" \
    "buffer_load_dwordx4 v[64:67], v36, %[rd], s45 offen offset:2048 sc1\n" \
    "s_add_u32 m0, s41, 4096\n s_add_u32 s40, s47, s46\n buffer_load_dwordx4 v36, %[rd], s40 offen sc1 lds\n" \
    "v_exp_f32 v205, v205\n v_exp_f32 v206, v206\n v_exp_f32 v207, v207\n v_exp_f32 v208, v208\n v_exp_f32 v209, v209\n" \
    "s_waitcnt lgkmcnt(2)\n v_smfmac_f32_32x32x32_bf16 v[168:183], v[240:243], v[200:207], v251\n" \
    "v_exp_f32 v210, v210\n v_exp_f32 v211, v211\n v_exp_f32 v212, v212\n v_exp_f32 v213, v213\n v_exp_f32 v214, v214\n" \
    "v_exp_f32 v215, v215\n v_exp_f32 v216, v216\n v_exp_f32 v217, v217\n s_waitcnt lgkmcnt(0)\n v_smfmac_f32_32x32x32_bf16 v[168:183], v[244:247], v[208:215], v251\n" \
    "v_exp_f32 v218, v218\n v_exp_f32 v219, v219\n v_exp_f32 v220, v220\n v_mfma_f32_32x32x16_bf16 v[200:215], v[236:239], v[232:235], v[184:199]\n" \
    "buffer_load_dwordx4 v[68:71], v36, %[rd], s45 offen offset:3072 sc1\n" \
    "s_add_u32 s44, s44, 8192\n s_add_u32 s45, s45, 8192\n buffer_load_dwordx4 v36, %[rd], s40 offen offset:1024 sc1 lds\n" \
    "s_add_u32 s46, s46, 8192\n s_and_b32 s46, s46, 0xffff\n v_exp_f32 v221, v221\n v_exp_f32 v222, v222\n v_exp_f32 v223, v223\n" \
    "v_exp_f32 v224, v224\n v_exp_f32 v225, v225\n v_smfmac_f32_32x32x32_bf16 v[8:23], v[240:243], v[216:223], v251\n" \
    "v_add_u32 v249, v249, v252\n ds_read_b128 v[240:243], v249\n" \
    "v_exp_f32 v226, v226\n v_exp_f32 v227, v227\n v_exp_f32 v228, v228\n v_exp_f32 v229, v229\n v_exp_f32 v230, v230\n" \
    "v_exp_f32 v231, v231\n" \
    ""
#define MQA0_3 \
    "v_exp_f32 v200, v200\n v_exp_f32 v201, v201\n v_smfmac_f32_32x32x32_bf16 v[8:23], v[244:247], v[224:231], v251\n" \
    "v_exp_f32 v202, v202\n v_exp_f32 v203, v203\n v_exp_f32 v204, v204\n v_mfma_f32_32x32x16_bf16 v[216:231], v[236:239], v[24:27], v[184:199]\n" \
    "ds_read_b128 v[244:247], v249 offset:512\n" \
    "ds_read_b128 v[28:31], v248 offset:1024\n" \
    "buffer_load_dwordx4 v[72:75], v36, %[rd], s44 offen offset:0 sc1\n" \
    "v_exp_f32 v205, v205\n v_exp_f32 v206, v206\n v_exp_f32 v207, v207\n v_exp_f32 v208, v208\n v_exp_f32 v209, v209\n" \
    "s_waitcnt lgkmcnt(2)\n v_smfmac_f32_32x32x32_bf16 v[168:183], v[240:243], v[200:207], v251\n" \
    "v_exp_f32 v210, v210\n v_exp_f32 v211, v211\n v_exp_f32 v212, v212\n v_exp_f32 v213, v213\n v_exp_f32 v214, v214\n" \
    "v_exp_f32 v215, v215\n v_exp_f32 v216, v216\n v_exp_f32 v217, v217\n s_waitcnt lgkmcnt(0)\n v_smfmac_f32_32x32x32_bf16 v[168:183], v[244:247], v[208:215], v251\n" \
    "v_exp_f32 v218, v218\n v_exp_f32 v219, v219\n v_exp_f32 v220, v220\n v_mfma_f32_32x32x16_bf16 v[200:215], v[28:31], v[232:235], v[184:199]\n" \
    "buffer_load_dwordx4 v[76:79], v36, %[rd], s44 offen offset:1024 sc1\n" \
    "v_exp_f32 v221, v221\n v_exp_f32 v222, v222\n v_exp_f32 v223, v223\n v_exp_f32 v224, v224\n v_exp_f32 v225, v225\n" \
    "v_smfmac_f32_32x32x32_bf16 v[8:23], v[240:243], v[216:223], v251\n" \
    "ds_read_b128 v[240:243], v249 offset:1024\n" \
    "v_exp_f32 v226, v226\n v_exp_f32 v227, v227\n v_exp_f32 v228, v228\n v_exp_f32 v229, v229\n v_exp_f32 v230, v230\n" \
    "v_exp_f32 v231, v231\n v_exp_f32 v200, v200\n v_exp_f32 v201, v201\n v_smfmac_f32_32x32x32_bf16 v[8:23], v[244:247], v[224:231], v251\n" \
    "v_exp_f32 v202, v202\n v_exp_f32 v203, v203\n v_exp_f32 v204, v204\n v_mfma_f32_32x32x16_bf16 v[216:231], v[28:31], v[24:27], v[184:199]\n" \
    "ds_read_b128 v[244:247], v249 offset:1536\n" \
    "ds_read_b128 v[236:239], v248 offset:2048\n" \
    "buffer_load_dwordx4 v[80:83], v36, %[rd], s44 offen offset:2048 sc1\n" \
    "buffer_store_dwordx4 v[4:7], v36, %[rd], %[r0] offen sc1\n" \
    "v_exp_f32 v205, v205\n v_exp_f32 v206, v206\n v_exp_f32 v207, v207\n v_exp_f32 v208, v208\n v_exp_f32 v209, v209\n" \
    "s_waitcnt lgkmcnt(2)\n v_smfmac_f32_32x32x32_bf16 v[168:183], v[240:243], v[200:207], v251\n" \
    "v_exp_f32 v210, v210\n v_exp_f32 v211, v211\n v_exp_f32 v212, v212\n v_exp_f32 v213, v213\n v_exp_f32 v214, v214\n" \
    "v_exp_f32 v215, v215\n v_exp_f32 v216, v216\n v_exp_f32 v217, v217\n s_waitcnt lgkmcnt(0)\n v_smfmac_f32_32x32x32_bf16 v[168:183], v[244:247], v[208:215], v251\n" \
    "v_exp_f32 v218, v218\n v_exp_f32 v219, v219\n v_exp_f32 v220, v220\n v_mfma_f32_32x32x16_bf16 v[200:215], v[236:239], v[232:235], v[184:199]\n" \
    "buffer_load_dwordx4 v[84:87], v36, %[rd], s44 offen offset:3072 sc1\n" \
    "s_mov_b32 m0, s43\n s_add_u32 s43, s43, 2048\n buffer_load_dwordx4 v36, %[rd], s42 offen sc1 lds\n" \
    "v_exp_f32 v221, v221\n v_exp_f32 v222, v222\n v_exp_f32 v223, v223\n v_exp_f32 v224, v224\n v_exp_f32 v225, v225\n" \
    "v_smfmac_f32_32x32x32_bf16 v[8:23], v[240:243], v[216:223], v251\n" \
    "ds_read_b128 v[240:243], v249 offset:2048\n" \
    "v_exp_f32 v226, v226\n v_exp_f32 v227, v227\n v_exp_f32 v228, v228\n v_exp_f32 v229, v229\n v_exp_f32 v230, v230\n" \
    "v_exp_f32 v231, v231\n v_exp_f32 v200, v200\n v_exp_f32 v201, v201\n v_smfmac_f32_32x32x32_bf16 v[8:23], v[244:247], v[224:231], v251\n" \
    "v_exp_f32 v202, v202\n v_exp_f32 v203, v203\n v_exp_f32 v204, v204\n v_mfma_f32_32x32x16_bf16 v[216:231], v[236:239], v[24:27], v[184:199]\n" \
    "ds_read_b128 v[244:247], v249 offset:2560\n" \
    "ds_read_b128 v[28:31], v248 offset:3072\n" \
    "buffer_load_dwordx4 v[88:91], v36, %[rd], s45 offen offset:0 sc1\n" \
    "v_exp_f32 v205, v205\n v_exp_f32 v206, v206\n v_exp_f32 v207, v207\n v_exp_f32 v208, v208\n v_exp_f32 v209, v209\n" \
    "s_waitcnt lgkmcnt(2)\n v_smfmac_f32_32x32x32_bf16 v[168:183], v[240:243], v[200:207], v251\n" \
    "v_exp_f32 v210, v210\n v_exp_f32 v211, v211\n v_exp_f32 v212, v212\n v_exp_f32 v213, v213\n v_exp_f32 v214, v214\n" \
    "v_exp_f32 v215, v215\n v_exp_f32 v216, v216\n v_exp_f32 v217, v217\n s_waitcnt lgkmcnt(0)\n v_smfmac_f32_32x32x32_bf16 v[168:183], v[244:247], v[208:215], v251\n" \
    "v_exp_f32 v218, v218\n v_exp_f32 v219, v219\n v_exp_f32 v220, v220\n v_mfma_f32_32x32x16_bf16 v[200:215], v[28:31], v[232:235], v[184:199]\n" \
    "buffer_load_dwordx4 v[92:95], v36, %[rd], s45 offen offset:1024 sc1\n" \
    "buffer_load_dwordx4 v36, %[rd], s42 offen offset:1024 sc1 lds\n" \
    "s_add_u32 s42, s42, 2048\n v_exp_f32 v221, v221\n v_exp_f32 v222, v222\n v_exp_f32 v223, v223\n v_exp_f32 v224, v224\n" \
    "v_exp_f32 v225, v225\n v_smfmac_f32_32x32x32_bf16 v[8:23], v[240:243], v[216:223], v251\n" \
    "ds_read_b128 v[240:243], v249 offset:3072\n" \
    "v_exp_f32 v226, v226\n v_exp_f32 v227, v227\n v_exp_f32 v228, v228\n v_exp_f32 v229, v229\n v_exp_f32 v230, v230\n" \
    "v_exp_f32 v231, v231\n v_exp_f32 v200, v200\n v_exp_f32 v201, v201\n v_smfmac_f32_32x32x32_bf16 v[8:23], v[244:247], v[224:231], v251\n" \
    "v_exp_f32 v202, v202\n v_exp_f32 v203, v203\n v_exp_f32 v204, v204\n v_mfma_f32_32x32x16_bf16 v[216:231], v[28:31], v[24:27], v[184:199]\n" \
    "ds_read_b128 v[244:247], v249 offset:3584\n" \
    "s_waitcnt vmcnt(9)\n s_barrier\n ds_read_b128 v[236:239], v248 offset:4096\n" \
    "buffer_load_dwordx4 v[96:99], v36, %[rd], s45 offen offset:2048 sc1\n" \
    "s_add_u32 m0, s41, 8192\n s_add_u32 s40, s47, s46\n buffer_load_dwordx4 v36, %[rd], s40 offen sc1 lds\n" \
    "v_exp_f32 v205, v205\n v_exp_f32 v206, v206\n v_exp_f32 v207, v207\n v_exp_f32 v208, v208\n v_exp_f32 v209, v209\n" \
    "s_waitcnt lgkmcnt(2)\n v_smfmac_f32_32x32x32_bf16 v[168:183], v[240:243], v[200:207], v251\n" \
    "v_exp_f32 v210, v210\n v_exp_f32 v211, v211\n v_exp_f32 v212, v212\n v_exp_f32 v213, v213\n v_exp_f32 v214, v214\n" \
    "v_exp_f32 v215, v215\n v_exp_f32 v216, v216\n v_exp_f32 v217, v217\n s_waitcnt lgkmcnt(0)\n v_smfmac_f32_32x32x32_bf16 v[168:183], v[244:247], v[208:215], v251\n" \
    "v_exp_f32 v218, v218\n v_exp_f32 v219, v219\n v_exp_f32 v220, v220\n v_mfma_f32_32x32x16_bf16 v[200:215], v[236:239], v[232:235], v[184:199]\n" \
    "buffer_load_dwordx4 v[100:103], v36, %[rd], s45 offen offset:3072 sc1\n" \
    "s_add_u32 s44, s44, 8192\n s_add_u32 s45, s45, 8192\n buffer_load_dwordx4 v36, %[rd], s40 offen offset:1024 sc1 lds\n" \
    "s_add_u32 s46, s46, 8192\n s_and_b32 s46, s46, 0xffff\n v_exp_f32 v221, v221\n v_exp_f32 v222, v222\n v_exp_f32 v223, v223\n" \
    "v_exp_f32 v224, v224\n v_exp_f32 v225, v225\n v_smfmac_f32_32x32x32_bf16 v[8:23], v[240:243], v[216:223], v251\n" \
    "v_add_u32 v249, v249, v250\n ds_read_b128 v[240:243], v249\n" \
    "v_exp_f32 v226, v226\n v_exp_f32 v227, v227\n v_exp_f32 v228, v228\n v_exp_f32 v229, v229\n v_exp_f32 v230, v230\n" \
    "v_exp_f32 v231, v231\n" \
    ""
#define MQA0_4 \
    "v_exp_f32 v200, v200\n v_exp_f32 v201, v201\n v_smfmac_f32_32x32x32_bf16 v[8:23], v[244:247], v[224:231], v251\n" \
    "v_exp_f32 v202, v202\n v_exp_f32 v203, v203\n v_exp_f32 v204, v204\n v_mfma_f32_32x32x16_bf16 v[216:231], v[236:239], v[24:27], v[184:199]\n" \
    "ds_read_b128 v[244:247], v249 offset:512\n" \
    "ds_read_b128 v[28:31], v248 offset:5120\n" \
    "buffer_load_dwordx4 v[104:107], v36, %[rd], s44 offen offset:0 sc1\n" \
    "v_exp_f32 v205, v205\n v_exp_f32 v206, v206\n v_exp_f32 v207, v207\n v_exp_f32 v208, v208\n v_exp_f32 v209, v209\n" \
    "s_waitcnt lgkmcnt(2)\n v_smfmac_f32_32x32x32_bf16 v[168:183], v[240:243], v[200:207], v251\n" \
    "v_exp_f32 v210, v210\n v_exp_f32 v211, v211\n v_exp_f32 v212, v212\n v_exp_f32 v213, v213\n v_exp_f32 v214, v214\n" \
    "v_exp_f32 v215, v215\n v_exp_f32 v216, v216\n v_exp_f32 v217, v217\n s_waitcnt lgkmcnt(0)\n v_smfmac_f32_32x32x32_bf16 v[168:183], v[244:247], v[208:215], v251\n" \
    "v_exp_f32 v218, v218\n v_exp_f32 v219, v219\n v_exp_f32 v220, v220\n v_mfma_f32_32x32x16_bf16 v[200:215], v[28:31], v[232:235], v[184:199]\n" \
    "buffer_load_dwordx4 v[108:111], v36, %[rd], s44 offen offset:1024 sc1\n" \
    "v_exp_f32 v221, v221\n v_exp_f32 v222, v222\n v_exp_f32 v223, v223\n v_exp_f32 v224, v224\n v_exp_f32 v225, v225\n" \
    "v_smfmac_f32_32x32x32_bf16 v[8:23], v[240:243], v[216:223], v251\n" \
    "ds_read_b128 v[240:243], v249 offset:1024\n" \
    "v_exp_f32 v226, v226\n v_exp_f32 v227, v227\n v_exp_f32 v228, v228\n v_exp_f32 v229, v229\n v_exp_f32 v230, v230\n" \
    "v_exp_f32 v231, v231\n v_exp_f32 v200, v200\n v_exp_f32 v201, v201\n v_smfmac_f32_32x32x32_bf16 v[8:23], v[244:247], v[224:231], v251\n" \
    "v_exp_f32 v202, v202\n v_exp_f32 v203, v203\n v_exp_f32 v204, v204\n v_mfma_f32_32x32x16_bf16 v[216:231], v[28:31], v[24:27], v[184:199]\n" \
    "ds_read_b128 v[244:247], v249 offset:1536\n" \
    "ds_read_b128 v[236:239], v248 offset:6144\n" \
    "buffer_load_dwordx4 v[112:115], v36, %[rd], s44 offen offset:2048 sc1\n" \
    "buffer_store_dwordx4 v[4:7], v36, %[rd], %[r0] offen sc1\n" \
    "v_exp_f32 v205, v205\n v_exp_f32 v206, v206\n v_exp_f32 v207, v207\n v_exp_f32 v208, v208\n v_exp_f32 v209, v209\n" \
    "s_waitcnt lgkmcnt(2)\n v_smfmac_f32_32x32x32_bf16 v[168:183], v[240:243], v[200:207], v251\n" \
    "v_exp_f32 v210, v210\n v_exp_f32 v211, v211\n v_exp_f32 v212, v212\n v_exp_f32 v213, v213\n v_exp_f32 v214, v214\n" \
    "v_exp_f32 v215, v215\n v_exp_f32 v216, v216\n v_exp_f32 v217, v217\n s_waitcnt lgkmcnt(0)\n v_smfmac_f32_32x32x32_bf16 v[168:183], v[244:247], v[208:215], v251\n" \
    "v_exp_f32 v218, v218\n v_exp_f32 v219, v219\n v_exp_f32 v220, v220\n v_mfma_f32_32x32x16_bf16 v[200:215], v[236:239], v[232:235], v[184:199]\n" \
    "buffer_load_dwordx4 v[116:119], v36, %[rd], s44 offen offset:3072 sc1\n" \
    "s_mov_b32 m0, s43\n s_add_u32 s43, s43, 2048\n buffer_load_dwordx4 v36, %[rd], s42 offen sc1 lds\n" \
    "v_exp_f32 v221, v221\n v_exp_f32 v222, v222\n v_exp_f32 v223, v223\n v_exp_f32 v224, v224\n v_exp_f32 v225, v225\n" \
    "v_smfmac_f32_32x32x32_bf16 v[8:23], v[240:243], v[216:223], v251\n" \
    "ds_read_b128 v[240:243], v249 offset:2048\n" \
    "v_exp_f32 v226, v226\n v_exp_f32 v227, v227\n v_exp_f32 v228, v228\n v_exp_f32 v229, v229\n v_exp_f32 v230, v230\n" \
    "v_exp_f32 v231, v231\n v_exp_f32 v200, v200\n v_exp_f32 v201, v201\n v_smfmac_f32_32x32x32_bf16 v[8:23], v[244:247], v[224:231], v251\n" \
    "v_exp_f32 v202, v202\n v_exp_f32 v203, v203\n v_exp_f32 v204, v204\n v_mfma_f32_32x32x16_bf16 v[216:231], v[236:239], v[24:27], v[184:199]\n" \
    "ds_read_b128 v[244:247], v249 offset:2560\n" \
    "ds_read_b128 v[28:31], v248 offset:7168\n" \
    "buffer_load_dwordx4 v[120:123], v36, %[rd], s45 offen offset:0 sc1\n" \
    "v_exp_f32 v205, v205\n v_exp_f32 v206, v206\n v_exp_f32 v207, v207\n v_exp_f32 v208, v208\n v_exp_f32 v209, v209\n" \
    "s_waitcnt lgkmcnt(2)\n v_smfmac_f32_32x32x32_bf16 v[168:183], v[240:243], v[200:207], v251\n" \
    "v_exp_f32 v210, v210\n v_exp_f32 v211, v211\n v_exp_f32 v212, v212\n v_exp_f32 v213, v213\n v_exp_f32 v214, v214\n" \
    "v_exp_f32 v215, v215\n v_exp_f32 v216, v216\n v_exp_f32 v217, v217\n s_waitcnt lgkmcnt(0)\n v_smfmac_f32_32x32x32_bf16 v[168:183], v[244:247], v[208:215], v251\n" \
    "v_exp_f32 v218, v218\n v_exp_f32 v219, v219\n v_exp_f32 v220, v220\n v_mfma_f32_32x32x16_bf16 v[200:215], v[28:31], v[232:235], v[184:199]\n" \
    "buffer_load_dwordx4 v[124:127], v36, %[rd], s45 offen offset:1024 sc1\n" \
    "buffer_load_dwordx4 v36, %[rd], s42 offen offset:1024 sc1 lds\n" \
    "s_add_u32 s42, s42, 2048\n v_exp_f32 v221, v221\n v_exp_f32 v222, v222\n v_exp_f32 v223, v223\n v_exp_f32 v224, v224\n" \
    "v_exp_f32 v225, v225\n v_smfmac_f32_32x32x32_bf16 v[8:23], v[240:243], v[216:223], v251\n" \
    "ds_read_b128 v[240:243], v249 offset:3072\n" \
    "v_exp_f32 v226, v226\n v_exp_f32 v227, v227\n v_exp_f32 v228, v228\n v_exp_f32 v229, v229\n v_exp_f32 v230, v230\n" \
    "v_exp_f32 v231, v231\n v_exp_f32 v200, v200\n v_exp_f32 v201, v201\n v_smfmac_f32_32x32x32_bf16 v[8:23], v[244:247], v[224:231], v251\n" \
    "v_exp_f32 v202, v202\n v_exp_f32 v203, v203\n v_exp_f32 v204, v204\n v_mfma_f32_32x32x16_bf16 v[216:231], v[28:31], v[24:27], v[184:199]\n" \
    "ds_read_b128 v[244:247], v249 offset:3584\n" \
    "s_waitcnt vmcnt(9)\n s_barrier\n ds_read_b128 v[236:239], v248 offset:8192\n" \
    "buffer_load_dwordx4 v[128:131], v36, %[rd], s45 offen offset:2048 sc1\n" \
    "s_add_u32 m0, s41, 0\n s_add_u32 s40, s47, s46\n buffer_load_dwordx4 v36, %[rd], s40 offen sc1 lds\n" \
    "v_exp_f32 v205, v205\n v_exp_f32 v206, v206\n v_exp_f32 v207, v207\n v_exp_f32 v208, v208\n v_exp_f32 v209, v209\n" \
    "s_waitcnt lgkmcnt(2)\n v_smfmac_f32_32x32x32_bf16 v[168:183], v[240:243], v[200:207], v251\n" \
    "v_exp_f32 v210, v210\n v_exp_f32 v211, v211\n v_exp_f32 v212, v212\n v_exp_f32 v213, v213\n v_exp_f32 v214, v214\n" \
    "v_exp_f32 v215, v215\n v_exp_f32 v216, v216\n v_exp_f32 v217, v217\n s_waitcnt lgkmcnt(0)\n v_smfmac_f32_32x32x32_bf16 v[168:183], v[244:247], v[208:215], v251\n" \
    "v_exp_f32 v218, v218\n v_exp_f32 v219, v219\n v_exp_f32 v220, v220\n v_mfma_f32_32x32x16_bf16 v[200:215], v[236:239], v[232:235], v[184:199]\n" \
    "buffer_load_dwordx4 v[132:135], v36, %[rd], s45 offen offset:3072 sc1\n" \
    "s_add_u32 s44, s44, 8192\n s_add_u32 s45, s45, 8192\n buffer_load_dwordx4 v36, %[rd], s40 offen offset:1024 sc1 lds\n" \
    "s_add_u32 s46, s46, 8192\n s_and_b32 s46, s46, 0xffff\n v_exp_f32 v221, v221\n v_exp_f32 v222, v222\n v_exp_f32 v223, v223\n" \
    "v_exp_f32 v224, v224\n v_exp_f32 v225, v225\n v_smfmac_f32_32x32x32_bf16 v[8:23], v[240:243], v[216:223], v251\n" \
    "v_add_u32 v249, v249, v250\n ds_read_b128 v[240:243], v249\n" \
    "v_exp_f32 v226, v226\n v_exp_f32 v227, v227\n v_exp_f32 v228, v228\n v_exp_f32 v229, v229\n v_exp_f32 v230, v230\n" \
    "v_exp_f32 v231, v231\n" \
    ""
#define MQA0_5 \
    "v_exp_f32 v200, v200\n v_exp_f32 v201, v201\n v_smfmac_f32_32x32x32_bf16 v[8:23], v[244:247], v[224:231], v251\n" \
    "v_exp_f32 v202, v202\n v_exp_f32 v203, v203\n v_exp_f32 v204, v204\n v_mfma_f32_32x32x16_bf16 v[216:231], v[236:239], v[24:27], v[184:199]\n" \
    "ds_read_b128 v[244:247], v249 offset:512\n" \
    "ds_read_b128 v[28:31], v248 offset:9216\n" \
    "buffer_load_dwordx4 v[136:139], v36, %[rd], s44 offen offset:0 sc1\n" \
    "v_exp_f32 v205, v205\n v_exp_f32 v206, v206\n v_exp_f32 v207, v207\n v_exp_f32 v208, v208\n v_exp_f32 v209, v209\n" \
    "s_waitcnt lgkmcnt(2)\n v_smfmac_f32_32x32x32_bf16 v[168:183], v[240:243], v[200:207], v251\n" \
    "v_exp_f32 v210, v210\n v_exp_f32 v211, v211\n v_exp_f32 v212, v212\n v_exp_f32 v213, v213\n v_exp_f32 v214, v214\n" \
    "v_exp_f32 v215, v215\n v_exp_f32 v216, v216\n v_exp_f32 v217, v217\n s_waitcnt lgkmcnt(0)\n v_smfmac_f32_32x32x32_bf16 v[168:183], v[244:247], v[208:215], v251\n" \
    "v_exp_f32 v218, v218\n v_exp_f32 v219, v219\n v_exp_f32 v220, v220\n v_mfma_f32_32x32x16_bf16 v[200:215], v[28:31], v[232:235], v[184:199]\n" \
    "buffer_load_dwordx4 v[140:143], v36, %[rd], s44 offen offset:1024 sc1\n" \
    "v_exp_f32 v221, v221\n v_exp_f32 v222, v222\n v_exp_f32 v223, v223\n v_exp_f32 v224, v224\n v_exp_f32 v225, v225\n" \
    "v_smfmac_f32_32x32x32_bf16 v[8:23], v[240:243], v[216:223], v251\n" \
    "ds_read_b128 v[240:243], v249 offset:1024\n" \
    "v_exp_f32 v226, v226\n v_exp_f32 v227, v227\n v_exp_f32 v228, v228\n v_exp_f32 v229, v229\n v_exp_f32 v230, v230\n" \
    "v_exp_f32 v231, v231\n v_exp_f32 v200, v200\n v_exp_f32 v201, v201\n v_smfmac_f32_32x32x32_bf16 v[8:23], v[244:247], v[224:231], v251\n" \
    "v_exp_f32 v202, v202\n v_exp_f32 v203, v203\n v_exp_f32 v204, v204\n v_mfma_f32_32x32x16_bf16 v[216:231], v[28:31], v[24:27], v[184:199]\n" \
    "ds_read_b128 v[244:247], v249 offset:1536\n" \
    "ds_read_b128 v[236:239], v248 offset:10240\n" \
    "buffer_load_dwordx4 v[144:147], v36, %[rd], s44 offen offset:2048 sc1\n" \
    "buffer_store_dwordx4 v[4:7], v36, %[rd], %[r0] offen sc1\n" \
    "v_exp_f32 v205, v205\n v_exp_f32 v206, v206\n v_exp_f32 v207, v207\n v_exp_f32 v208, v208\n v_exp_f32 v209, v209\n" \
    "s_waitcnt lgkmcnt(2)\n v_smfmac_f32_32x32x32_bf16 v[168:183], v[240:243], v[200:207], v251\n" \
    "v_exp_f32 v210, v210\n v_exp_f32 v211, v211\n v_exp_f32 v212, v212\n v_exp_f32 v213, v213\n v_exp_f32 v214, v214\n" \
    "v_exp_f32 v215, v215\n v_exp_f32 v216, v216\n v_exp_f32 v217, v217\n s_waitcnt lgkmcnt(0)\n v_smfmac_f32_32x32x32_bf16 v[168:183], v[244:247], v[208:215], v251\n" \
    "v_exp_f32 v218, v218\n v_exp_f32 v219, v219\n v_exp_f32 v220, v220\n v_mfma_f32_32x32x16_bf16 v[200:215], v[236:239], v[232:235], v[184:199]\n" \
    "buffer_load_dwordx4 v[148:151], v36, %[rd], s44 offen offset:3072 sc1\n" \
    "s_mov_b32 m0, s43\n s_add_u32 s43, s43, 2048\n buffer_load_dwordx4 v36, %[rd], s42 offen sc1 lds\n" \
    "v_exp_f32 v221, v221\n v_exp_f32 v222, v222\n v_exp_f32 v223, v223\n v_exp_f32 v224, v224\n v_exp_f32 v225, v225\n" \
    "v_smfmac_f32_32x32x32_bf16 v[8:23], v[240:243], v[216:223], v251\n" \
    "ds_read_b128 v[240:243], v249 offset:2048\n" \
    "v_exp_f32 v226, v226\n v_exp_f32 v227, v227\n v_exp_f32 v228, v228\n v_exp_f32 v229, v229\n v_exp_f32 v230, v230\n" \
    "v_exp_f32 v231, v231\n v_exp_f32 v200, v200\n v_exp_f32 v201, v201\n v_smfmac_f32_32x32x32_bf16 v[8:23], v[244:247], v[224:231], v251\n" \
    "v_exp_f32 v202, v202\n v_exp_f32 v203, v203\n v_exp_f32 v204, v204\n v_mfma_f32_32x32x16_bf16 v[216:231], v[236:239], v[24:27], v[184:199]\n" \
    "ds_read_b128 v[244:247], v249 offset:2560\n" \
    "ds_read_b128 v[28:31], v248 offset:11264\n" \
    "buffer_load_dwordx4 v[152:155], v36, %[rd], s45 offen offset:0 sc1\n" \
    "v_exp_f32 v205, v205\n v_exp_f32 v206, v206\n v_exp_f32 v207, v207\n v_exp_f32 v208, v208\n v_exp_f32 v209, v209\n" \
    "s_waitcnt lgkmcnt(2)\n v_smfmac_f32_32x32x32_bf16 v[168:183], v[240:243], v[200:207], v251\n" \
    "v_exp_f32 v210, v210\n v_exp_f32 v211, v211\n v_exp_f32 v212, v212\n v_exp_f32 v213, v213\n v_exp_f32 v214, v214\n" \
    "v_exp_f32 v215, v215\n v_exp_f32 v216, v216\n v_exp_f32 v217, v217\n s_waitcnt lgkmcnt(0)\n v_smfmac_f32_32x32x32_bf16 v[168:183], v[244:247], v[208:215], v251\n" \
    "v_exp_f32 v218, v218\n v_exp_f32 v219, v219\n v_exp_f32 v220, v220\n v_mfma_f32_32x32x16_bf16 v[200:215], v[28:31], v[232:235], v[184:199]\n" \
    "buffer_load_dwordx4 v[156:159], v36, %[rd], s45 offen offset:1024 sc1\n" \
    "buffer_load_dwordx4 v36, %[rd], s42 offen offset:1024 sc1 lds\n" \
    "s_add_u32 s42, s42, 2048\n v_exp_f32 v221, v221\n v_exp_f32 v222, v222\n v_exp_f32 v223, v223\n v_exp_f32 v224, v224\n" \
    "v_exp_f32 v225, v225\n v_smfmac_f32_32x32x32_bf16 v[8:23], v[240:243], v[216:223], v251\n" \
    "ds_read_b128 v[240:243], v249 offset:3072\n" \
    "v_exp_f32 v226, v226\n v_exp_f32 v227, v227\n v_exp_f32 v228, v228\n v_exp_f32 v229, v229\n v_exp_f32 v230, v230\n" \
    "v_exp_f32 v231, v231\n v_exp_f32 v200, v200\n v_exp_f32 v201, v201\n v_smfmac_f32_32x32x32_bf16 v[8:23], v[244:247], v[224:231], v251\n" \
    "v_exp_f32 v202, v202\n v_exp_f32 v203, v203\n v_exp_f32 v204, v204\n v_mfma_f32_32x32x16_bf16 v[216:231], v[28:31], v[24:27], v[184:199]\n" \
    "ds_read_b128 v[244:247], v249 offset:3584\n" \
    "s_waitcnt vmcnt(9)\n s_barrier\n ds_read_b128 v[236:239], v248 offset:0\n" \
    "buffer_load_dwordx4 v[160:163], v36, %[rd], s45 offen offset:2048 sc1\n" \
    "s_add_u32 m0, s41, 4096\n s_add_u32 s40, s47, s46\n buffer_load_dwordx4 v36, %[rd], s40 offen sc1 lds\n" \
    "v_exp_f32 v205, v205\n v_exp_f32 v206, v206\n v_exp_f32 v207, v207\n v_exp_f32 v208, v208\n v_exp_f32 v209, v209\n" \
    "s_waitcnt lgkmcnt(2)\n v_smfmac_f32_32x32x32_bf16 v[168:183], v[240:243], v[200:207], v251\n" \
    "v_exp_f32 v210, v210\n v_exp_f32 v211, v211\n v_exp_f32 v212, v212\n v_exp_f32 v213, v213\n v_exp_f32 v214, v214\n" \
    "v_exp_f32 v215, v215\n v_exp_f32 v216, v216\n v_exp_f32 v217, v217\n s_waitcnt lgkmcnt(0)\n v_smfmac_f32_32x32x32_bf16 v[168:183], v[244:247], v[208:215], v251\n" \
    "v_exp_f32 v218, v218\n v_exp_f32 v219, v219\n v_exp_f32 v220, v220\n v_mfma_f32_32x32x16_bf16 v[200:215], v[236:239], v[232:235], v[184:199]\n" \
    "buffer_load_dwordx4 v[164:167], v36, %[rd], s45 offen offset:3072 sc1\n" \
    "s_add_u32 s44, s44, 8192\n s_add_u32 s45, s45, 8192\n buffer_load_dwordx4 v36, %[rd], s40 offen offset:1024 sc1 lds\n" \
    "s_add_u32 s46, s46, 8192\n s_and_b32 s46, s46, 0xffff\n v_exp_f32 v221, v221\n v_exp_f32 v222, v222\n v_exp_f32 v223, v223\n" \
    "v_exp_f32 v224, v224\n v_exp_f32 v225, v225\n v_smfmac_f32_32x32x32_bf16 v[8:23], v[240:243], v[216:223], v251\n" \
    "v_add_u32 v249, v249, v252\n ds_read_b128 v[240:243], v249\n" \
    "v_exp_f32 v226, v226\n v_exp_f32 v227, v227\n v_exp_f32 v228, v228\n v_exp_f32 v229, v229\n v_exp_f32 v230, v230\n" \
    "v_exp_f32 v231, v231\n" \
    ""
#define MQA0_6 \
    "v_exp_f32 v200, v200\n v_exp_f32 v201, v201\n v_smfmac_f32_32x32x32_bf16 v[8:23], v[244:247], v[224:231], v251\n" \
    "v_exp_f32 v202, v202\n v_exp_f32 v203, v203\n v_exp_f32 v204, v204\n v_mfma_f32_32x32x16_bf16 v[216:231], v[236:239], v[24:27], v[184:199]\n" \
    "ds_read_b128 v[244:247], v249 offset:512\n" \
    "ds_read_b128 v[28:31], v248 offset:1024\n" \
    "v_exp_f32 v205, v205\n v_exp_f32 v206, v206\n v_exp_f32 v207, v207\n v_exp_f32 v208, v208\n v_exp_f32 v209, v209\n" \
    "s_waitcnt lgkmcnt(2)\n v_smfmac_f32_32x32x32_bf16 v[168:183], v[240:243], v[200:207], v251\n" \
    "v_exp_f32 v210, v210\n v_exp_f32 v211, v211\n v_exp_f32 v212, v212\n v_exp_f32 v213, v213\n v_exp_f32 v214, v214\n" \
    "v_exp_f32 v215, v215\n v_exp_f32 v216, v216\n v_exp_f32 v217, v217\n s_waitcnt lgkmcnt(0)\n v_smfmac_f32_32x32x32_bf16 v[168:183], v[244:247], v[208:215], v251\n" \
    "v_exp_f32 v218, v218\n v_exp_f32 v219, v219\n v_exp_f32 v220, v220\n v_mfma_f32_32x32x16_bf16 v[200:215], v[28:31], v[232:235], v[184:199]\n" \
    "v_exp_f32 v221, v221\n v_exp_f32 v222, v222\n v_exp_f32 v223, v223\n v_exp_f32 v224, v224\n v_exp_f32 v225, v225\n" \
    "v_smfmac_f32_32x32x32_bf16 v[8:23], v[240:243], v[216:223], v251\n" \
    "ds_read_b128 v[240:243], v249 offset:1024\n" \
    "v_exp_f32 v226, v226\n v_exp_f32 v227, v227\n v_exp_f32 v228, v228\n v_exp_f32 v229, v229\n v_exp_f32 v230, v230\n" \
    "v_exp_f32 v231, v231\n v_exp_f32 v200, v200\n v_exp_f32 v201, v201\n v_smfmac_f32_32x32x32_bf16 v[8:23], v[244:247], v[224:231], v251\n" \
    "v_exp_f32 v202, v202\n v_exp_f32 v203, v203\n v_exp_f32 v204, v204\n v_mfma_f32_32x32x16_bf16 v[216:231], v[28:31], v[24:27], v[184:199]\n" \
    "ds_read_b128 v[244:247], v249 offset:1536\n" \
    "ds_read_b128 v[236:239], v248 offset:2048\n" \
    "buffer_store_dwordx4 v[4:7], v36, %[rd], %[r0] offen sc1\n" \
    "v_exp_f32 v205, v205\n v_exp_f32 v206, v206\n v_exp_f32 v207, v207\n v_exp_f32 v208, v208\n v_exp_f32 v209, v209\n" \
    "s_waitcnt lgkmcnt(2)\n v_smfmac_f32_32x32x32_bf16 v[168:183], v[240:243], v[200:207], v251\n" \
    "v_exp_f32 v210, v210\n v_exp_f32 v211, v211\n v_exp_f32 v212, v212\n v_exp_f32 v213, v213\n v_exp_f32 v214, v214\n" \
    "v_exp_f32 v215, v215\n v_exp_f32 v216, v216\n v_exp_f32 v217, v217\n s_waitcnt lgkmcnt(0)\n v_smfmac_f32_32x32x32_bf16 v[168:183], v[244:247], v[208:215], v251\n" \
    "v_exp_f32 v218, v218\n v_exp_f32 v219, v219\n v_exp_f32 v220, v220\n v_mfma_f32_32x32x16_bf16 v[200:215], v[236:239], v[232:235], v[184:199]\n" \
    "v_exp_f32 v221, v221\n v_exp_f32 v222, v222\n v_exp_f32 v223, v223\n v_exp_f32 v224, v224\n v_exp_f32 v225, v225\n" \
    "v_smfmac_f32_32x32x32_bf16 v[8:23], v[240:243], v[216:223], v251\n" \
    "ds_read_b128 v[240:243], v249 offset:2048\n" \
    "v_exp_f32 v226, v226\n v_exp_f32 v227, v227\n v_exp_f32 v228, v228\n v_exp_f32 v229, v229\n v_exp_f32 v230, v230\n" \
    "v_exp_f32 v231, v231\n v_exp_f32 v200, v200\n v_exp_f32 v201, v201\n v_smfmac_f32_32x32x32_bf16 v[8:23], v[244:247], v[224:231], v251\n" \
    "v_exp_f32 v202, v202\n v_exp_f32 v203, v203\n v_exp_f32 v204, v204\n v_mfma_f32_32x32x16_bf16 v[216:231], v[236:239], v[24:27], v[184:199]\n" \
    "ds_read_b128 v[244:247], v249 offset:2560\n" \
    "ds_read_b128 v[28:31], v248 offset:3072\n" \
    "v_exp_f32 v205, v205\n v_exp_f32 v206, v206\n v_exp_f32 v207, v207\n v_exp_f32 v208, v208\n v_exp_f32 v209, v209\n" \
    "s_waitcnt lgkmcnt(2)\n v_smfmac_f32_32x32x32_bf16 v[168:183], v[240:243], v[200:207], v251\n" \
    "v_exp_f32 v210, v210\n v_exp_f32 v211, v211\n v_exp_f32 v212, v212\n v_exp_f32 v213, v213\n v_exp_f32 v214, v214\n" \
    "v_exp_f32 v215, v215\n v_exp_f32 v216, v216\n v_exp_f32 v217, v217\n s_waitcnt lgkmcnt(0)\n v_smfmac_f32_32x32x32_bf16 v[168:183], v[244:247], v[208:215], v251\n" \
    "v_exp_f32 v218, v218\n v_exp_f32 v219, v219\n v_exp_f32 v220, v220\n v_mfma_f32_32x32x16_bf16 v[200:215], v[28:31], v[232:235], v[184:199]\n" \
    "v_exp_f32 v221, v221\n v_exp_f32 v222, v222\n v_exp_f32 v223, v223\n v_exp_f32 v224, v224\n v_exp_f32 v225, v225\n" \
    "v_smfmac_f32_32x32x32_bf16 v[8:23], v[240:243], v[216:223], v251\n" \
    "ds_read_b128 v[240:243], v249 offset:3072\n" \
    "v_exp_f32 v226, v226\n v_exp_f32 v227, v227\n v_exp_f32 v228, v228\n v_exp_f32 v229, v229\n v_exp_f32 v230, v230\n" \
    "v_exp_f32 v231, v231\n v_exp_f32 v200, v200\n v_exp_f32 v201, v201\n v_smfmac_f32_32x32x32_bf16 v[8:23], v[244:247], v[224:231], v251\n" \
    "v_exp_f32 v202, v202\n v_exp_f32 v203, v203\n v_exp_f32 v204, v204\n v_mfma_f32_32x32x16_bf16 v[216:231], v[28:31], v[24:27], v[184:199]\n" \
    "ds_read_b128 v[244:247], v249 offset:3584\n" \
    "s_waitcnt vmcnt(0)\n s_barrier\n ds_read_b128 v[236:239], v248 offset:4096\n" \
    "buffer_store_dword v32, v33, %[rd], %[to] offen sc1\n" \
    "v_exp_f32 v205, v205\n v_exp_f32 v206, v206\n v_exp_f32 v207, v207\n v_exp_f32 v208, v208\n v_exp_f32 v209, v209\n" \
    "s_waitcnt lgkmcnt(2)\n v_smfmac_f32_32x32x32_bf16 v[168:183], v[240:243], v[200:207], v251\n" \
    "v_exp_f32 v210, v210\n v_exp_f32 v211, v211\n v_exp_f32 v212, v212\n v_exp_f32 v213, v213\n v_exp_f32 v214, v214\n" \
    "v_exp_f32 v215, v215\n v_exp_f32 v216, v216\n v_exp_f32 v217, v217\n s_waitcnt lgkmcnt(0)\n v_smfmac_f32_32x32x32_bf16 v[168:183], v[244:247], v[208:215], v251\n" \
    "v_exp_f32 v218, v218\n v_exp_f32 v219, v219\n v_exp_f32 v220, v220\n v_mfma_f32_32x32x16_bf16 v[200:215], v[236:239], v[232:235], v[184:199]\n" \
    "v_exp_f32 v221, v221\n v_exp_f32 v222, v222\n v_exp_f32 v223, v223\n v_exp_f32 v224, v224\n v_exp_f32 v225, v225\n" \
    "v_smfmac_f32_32x32x32_bf16 v[8:23], v[240:243], v[216:223], v251\n" \
    "v_add_u32 v249, v249, v250\n ds_read_b128 v[240:243], v249\n" \
    "v_exp_f32 v226, v226\n v_exp_f32 v227, v227\n v_exp_f32 v228, v228\n v_exp_f32 v229, v229\n v_exp_f32 v230, v230\n" \
    "v_exp_f32 v231, v231\n" \
    ""
#define MQA0_7 \
    "v_exp_f32 v200, v200\n v_exp_f32 v201, v201\n v_smfmac_f32_32x32x32_bf16 v[8:23], v[244:247], v[224:231], v251\n" \
    "v_exp_f32 v202, v202\n v_exp_f32 v203, v203\n v_exp_f32 v204, v204\n v_mfma_f32_32x32x16_bf16 v[216:231], v[236:239], v[24:27], v[184:199]\n" \
    "ds_read_b128 v[244:247], v249 offset:512\n" \
    "ds_read_b128 v[28:31], v248 offset:5120\n" \
    "v_exp_f32 v205, v205\n v_exp_f32 v206, v206\n v_exp_f32 v207, v207\n v_exp_f32 v208, v208\n v_exp_f32 v209, v209\n" \
    "s_waitcnt lgkmcnt(2)\n v_smfmac_f32_32x32x32_bf16 v[168:183], v[240:243], v[200:207], v251\n" \
    "v_exp_f32 v210, v210\n v_exp_f32 v211, v211\n v_exp_f32 v212, v212\n v_exp_f32 v213, v213\n v_exp_f32 v214, v214\n" \
    "v_exp_f32 v215, v215\n v_exp_f32 v216, v216\n v_exp_f32 v217, v217\n s_waitcnt lgkmcnt(0)\n v_smfmac_f32_32x32x32_bf16 v[168:183], v[244:247], v[208:215], v251\n" \
    "v_exp_f32 v218, v218\n v_exp_f32 v219, v219\n v_exp_f32 v220, v220\n v_mfma_f32_32x32x16_bf16 v[200:215], v[28:31], v[232:235], v[184:199]\n" \
    "v_exp_f32 v221, v221\n v_exp_f32 v222, v222\n v_exp_f32 v223, v223\n v_exp_f32 v224, v224\n v_exp_f32 v225, v225\n" \
    "v_smfmac_f32_32x32x32_bf16 v[8:23], v[240:243], v[216:223], v251\n" \
    "ds_read_b128 v[240:243], v249 offset:1024\n" \
    "v_exp_f32 v226, v226\n v_exp_f32 v227, v227\n v_exp_f32 v228, v228\n v_exp_f32 v229, v229\n v_exp_f32 v230, v230\n" \
    "v_exp_f32 v231, v231\n v_exp_f32 v200, v200\n v_exp_f32 v201, v201\n v_smfmac_f32_32x32x32_bf16 v[8:23], v[244:247], v[224:231], v251\n" \
    "v_exp_f32 v202, v202\n v_exp_f32 v203, v203\n v_exp_f32 v204, v204\n v_mfma_f32_32x32x16_bf16 v[216:231], v[28:31], v[24:27], v[184:199]\n" \
    "ds_read_b128 v[244:247], v249 offset:1536\n" \
    "ds_read_b128 v[236:239], v248 offset:6144\n" \
    "v_exp_f32 v205, v205\n v_exp_f32 v206, v206\n v_exp_f32 v207, v207\n v_exp_f32 v208, v208\n v_exp_f32 v209, v209\n" \
    "s_waitcnt lgkmcnt(2)\n v_smfmac_f32_32x32x32_bf16 v[168:183], v[240:243], v[200:207], v251\n" \
    "v_exp_f32 v210, v210\n v_exp_f32 v211, v211\n v_exp_f32 v212, v212\n v_exp_f32 v213, v213\n v_exp_f32 v214, v214\n" \
    "v_exp_f32 v215, v215\n v_exp_f32 v216, v216\n v_exp_f32 v217, v217\n s_waitcnt lgkmcnt(0)\n v_smfmac_f32_32x32x32_bf16 v[168:183], v[244:247], v[208:215], v251\n" \
    "v_exp_f32 v218, v218\n v_exp_f32 v219, v219\n v_exp_f32 v220, v220\n v_mfma_f32_32x32x16_bf16 v[200:215], v[236:239], v[232:235], v[184:199]\n" \
    "v_exp_f32 v221, v221\n v_exp_f32 v222, v222\n v_exp_f32 v223, v223\n v_exp_f32 v224, v224\n v_exp_f32 v225, v225\n" \
    "v_smfmac_f32_32x32x32_bf16 v[8:23], v[240:243], v[216:223], v251\n" \
    "ds_read_b128 v[240:243], v249 offset:2048\n" \
    "v_exp_f32 v226, v226\n v_exp_f32 v227, v227\n v_exp_f32 v228, v228\n v_exp_f32 v229, v229\n v_exp_f32 v230, v230\n" \
    "v_exp_f32 v231, v231\n v_exp_f32 v200, v200\n v_exp_f32 v201, v201\n v_smfmac_f32_32x32x32_bf16 v[8:23], v[244:247], v[224:231], v251\n" \
    "v_exp_f32 v202, v202\n v_exp_f32 v203, v203\n v_exp_f32 v204, v204\n v_mfma_f32_32x32x16_bf16 v[216:231], v[236:239], v[24:27], v[184:199]\n" \
    "ds_read_b128 v[244:247], v249 offset:2560\n" \
    "ds_read_b128 v[28:31], v248 offset:7168\n" \
    "v_exp_f32 v205, v205\n v_exp_f32 v206, v206\n v_exp_f32 v207, v207\n v_exp_f32 v208, v208\n v_exp_f32 v209, v209\n" \
    "s_waitcnt lgkmcnt(2)\n v_smfmac_f32_32x32x32_bf16 v[168:183], v[240:243], v[200:207], v251\n" \
    "v_exp_f32 v210, v210\n v_exp_f32 v211, v211\n v_exp_f32 v212, v212\n v_exp_f32 v213, v213\n v_exp_f32 v214, v214\n" \
    "v_exp_f32 v215, v215\n v_exp_f32 v216, v216\n v_exp_f32 v217, v217\n s_waitcnt lgkmcnt(0)\n v_smfmac_f32_32x32x32_bf16 v[168:183], v[244:247], v[208:215], v251\n" \
    "v_exp_f32 v218, v218\n v_exp_f32 v219, v219\n v_exp_f32 v220, v220\n v_mfma_f32_32x32x16_bf16 v[200:215], v[28:31], v[232:235], v[184:199]\n" \
    "buffer_load_dword v38, v34, %[rd], %[fo] offen sc1\n" \
    "v_exp_f32 v221, v221\n v_exp_f32 v222, v222\n v_exp_f32 v223, v223\n v_exp_f32 v224, v224\n v_exp_f32 v225, v225\n" \
    "v_smfmac_f32_32x32x32_bf16 v[8:23], v[240:243], v[216:223], v251\n" \
    "ds_read_b128 v[240:243], v249 offset:3072\n" \
    "v_exp_f32 v226, v226\n v_exp_f32 v227, v227\n v_exp_f32 v228, v228\n v_exp_f32 v229, v229\n v_exp_f32 v230, v230\n" \
    "v_exp_f32 v231, v231\n v_exp_f32 v200, v200\n v_exp_f32 v201, v201\n v_smfmac_f32_32x32x32_bf16 v[8:23], v[244:247], v[224:231], v251\n" \
    "v_exp_f32 v202, v202\n v_exp_f32 v203, v203\n v_exp_f32 v204, v204\n v_mfma_f32_32x32x16_bf16 v[216:231], v[28:31], v[24:27], v[184:199]\n" \
    "ds_read_b128 v[244:247], v249 offset:3584\n" \
    "ds_read_b128 v[236:239], v248 offset:8192\n" \
    "v_exp_f32 v205, v205\n v_exp_f32 v206, v206\n v_exp_f32 v207, v207\n v_exp_f32 v208, v208\n v_exp_f32 v209, v209\n" \
    "s_waitcnt lgkmcnt(2)\n v_smfmac_f32_32x32x32_bf16 v[168:183], v[240:243], v[200:207], v251\n" \
    "v_exp_f32 v210, v210\n v_exp_f32 v211, v211\n v_exp_f32 v212, v212\n v_exp_f32 v213, v213\n v_exp_f32 v214, v214\n" \
    "v_exp_f32 v215, v215\n v_exp_f32 v216, v216\n v_exp_f32 v217, v217\n s_waitcnt lgkmcnt(0)\n v_smfmac_f32_32x32x32_bf16 v[168:183], v[244:247], v[208:215], v251\n" \
    "v_exp_f32 v218, v218\n v_exp_f32 v219, v219\n v_exp_f32 v220, v220\n v_mfma_f32_32x32x16_bf16 v[200:215], v[236:239], v[232:235], v[184:199]\n" \
    "v_exp_f32 v221, v221\n v_exp_f32 v222, v222\n v_exp_f32 v223, v223\n v_exp_f32 v224, v224\n v_exp_f32 v225, v225\n" \
    "v_smfmac_f32_32x32x32_bf16 v[8:23], v[240:243], v[216:223], v251\n" \
    "v_add_u32 v249, v249, v250\n ds_read_b128 v[240:243], v249\n" \
    "v_exp_f32 v226, v226\n v_exp_f32 v227, v227\n v_exp_f32 v228, v228\n v_exp_f32 v229, v229\n v_exp_f32 v230, v230\n" \
    "v_exp_f32 v231, v231\n" \
    ""
#define MQA_RESCALE \
    "s_waitcnt lgkmcnt(0)\n s_nop 7\n s_nop 7\n v_mul_f32 v39, 0x3f828f5c, v34\n v_mul_f32 v39, v39, v38\n" \
    "v_sqrt_f32 v39, v39\n s_nop 0\n v_sub_f32 v32, -v39, v184\n v_exp_f32 v33, v32\n s_nop 0\n" \
    "v_mul_f32 v168, v168, v33\n v_mul_f32 v169, v169, v33\n v_mul_f32 v170, v170, v33\n v_mul_f32 v171, v171, v33\n v_mul_f32 v172, v172, v33\n" \
    "v_mul_f32 v173, v173, v33\n v_mul_f32 v174, v174, v33\n v_mul_f32 v175, v175, v33\n v_mul_f32 v176, v176, v33\n v_mul_f32 v8, v8, v33\n" \
    "v_mul_f32 v9, v9, v33\n v_mul_f32 v10, v10, v33\n v_mul_f32 v11, v11, v33\n v_mul_f32 v12, v12, v33\n v_mul_f32 v13, v13, v33\n" \
    "v_mul_f32 v14, v14, v33\n v_mul_f32 v15, v15, v33\n v_mul_f32 v16, v16, v33\n v_mul_f32 v224, v224, v33\n v_mul_f32 v225, v225, v33\n" \
    "v_mul_f32 v226, v226, v33\n v_mul_f32 v227, v227, v33\n v_mul_f32 v228, v228, v33\n v_mul_f32 v229, v229, v33\n v_mul_f32 v230, v230, v33\n" \
    "v_mul_f32 v231, v231, v33\n v_add_f32 v200, v200, v32\n v_add_f32 v201, v201, v32\n v_add_f32 v202, v202, v32\n v_add_f32 v203, v203, v32\n" \
    "v_add_f32 v204, v204, v32\n v_add_f32 v205, v205, v32\n v_add_f32 v206, v206, v32\n v_add_f32 v207, v207, v32\n v_add_f32 v208, v208, v32\n" \
    "v_add_f32 v209, v209, v32\n v_add_f32 v210, v210, v32\n v_add_f32 v211, v211, v32\n v_add_f32 v212, v212, v32\n v_add_f32 v213, v213, v32\n" \
    "v_add_f32 v214, v214, v32\n v_add_f32 v215, v215, v32\n v_add_f32 v184, v184, v32\n v_add_f32 v185, v185, v32\n v_add_f32 v186, v186, v32\n" \
    "v_add_f32 v187, v187, v32\n v_add_f32 v188, v188, v32\n v_add_f32 v189, v189, v32\n v_add_f32 v190, v190, v32\n v_add_f32 v191, v191, v32\n" \
    "v_add_f32 v192, v192, v32\n v_add_f32 v193, v193, v32\n v_add_f32 v194, v194, v32\n v_add_f32 v195, v195, v32\n v_add_f32 v196, v196, v32\n" \
    "v_add_f32 v197, v197, v32\n v_add_f32 v198, v198, v32\n v_add_f32 v199, v199, v32\n s_nop 1\n" \
    ""
#define MQA1_0 \
    "v_exp_f32 v200, v200\n v_exp_f32 v201, v201\n v_smfmac_f32_32x32x32_bf16 v[8:23], v[244:247], v[224:231], v251\n" \
    "v_exp_f32 v202, v202\n v_exp_f32 v203, v203\n v_exp_f32 v204, v204\n v_mfma_f32_32x32x16_bf16 v[216:231], v[236:239], v[24:27], v[184:199]\n" \
    "ds_read_b128 v[244:247], v249 offset:512\n" \
    "ds_read_b128 v[28:31], v248 offset:1024\n" \
    "buffer_load_dwordx2 v[32:33], v39, %[rd], s49 offen sc1\n" \
    "v_exp_f32 v205, v205\n v_exp_f32 v206, v206\n v_exp_f32 v207, v207\n v_exp_f32 v208, v208\n v_exp_f32 v209, v209\n" \
    "s_waitcnt lgkmcnt(2)\n v_smfmac_f32_32x32x32_bf16 v[168:183], v[240:243], v[200:207], v251\n" \
    "v_exp_f32 v210, v210\n s_setprio 0\n v_exp_f32 v211, v211\n v_exp_f32 v212, v212\n v_exp_f32 v213, v213\n" \
    "s_setprio 1\n v_exp_f32 v214, v214\n v_exp_f32 v215, v215\n v_exp_f32 v216, v216\n v_exp_f32 v217, v217\n" \
    "s_waitcnt lgkmcnt(0)\n v_smfmac_f32_32x32x32_bf16 v[168:183], v[244:247], v[208:215], v251\n" \
    "v_exp_f32 v218, v218\n v_exp_f32 v219, v219\n v_exp_f32 v220, v220\n v_mfma_f32_32x32x16_bf16 v[200:215], v[28:31], v[232:235], v[184:199]\n" \
    "v_exp_f32 v221, v221\n v_exp_f32 v222, v222\n v_exp_f32 v223, v223\n v_exp_f32 v224, v224\n v_exp_f32 v225, v225\n" \
    "v_smfmac_f32_32x32x32_bf16 v[8:23], v[240:243], v[216:223], v251\n" \
    "ds_read_b128 v[240:243], v249 offset:1024\n" \
    "v_exp_f32 v226, v226\n s_setprio 0\n v_exp_f32 v227, v227\n v_exp_f32 v228, v228\n v_exp_f32 v229, v229\n" \
    "s_setprio 1\n v_exp_f32 v230, v230\n v_exp_f32 v231, v231\n v_exp_f32 v200, v200\n v_exp_f32 v201, v201\n" \
    "v_smfmac_f32_32x32x32_bf16 v[8:23], v[244:247], v[224:231], v251\n" \
    "v_exp_f32 v202, v202\n v_exp_f32 v203, v203\n v_exp_f32 v204, v204\n v_mfma_f32_32x32x16_bf16 v[216:231], v[28:31], v[24:27], v[184:199]\n" \
    "ds_read_b128 v[244:247], v249 offset:1536\n" \
    "ds_read_b128 v[236:239], v248 offset:2048\n" \
    "buffer_store_dwordx4 v[4:7], v36, %[rd], %[r0] offen sc1\n" \
    "v_exp_f32 v205, v205\n v_exp_f32 v206, v206\n v_exp_f32 v207, v207\n v_exp_f32 v208, v208\n v_exp_f32 v209, v209\n" \
    "s_waitcnt lgkmcnt(2)\n v_smfmac_f32_32x32x32_bf16 v[168:183], v[240:243], v[200:207], v251\n" \
    "v_exp_f32 v210, v210\n s_setprio 0\n v_exp_f32 v211, v211\n v_exp_f32 v212, v212\n v_exp_f32 v213, v213\n" \
    "s_setprio 1\n v_exp_f32 v214, v214\n v_exp_f32 v215, v215\n v_exp_f32 v216, v216\n v_exp_f32 v217, v217\n" \
    "s_waitcnt lgkmcnt(0)\n v_smfmac_f32_32x32x32_bf16 v[168:183], v[244:247], v[208:215], v251\n" \
    "v_exp_f32 v218, v218\n v_exp_f32 v219, v219\n v_exp_f32 v220, v220\n v_mfma_f32_32x32x16_bf16 v[200:215], v[236:239], v[232:235], v[184:199]\n" \
    "s_waitcnt vmcnt(1)\n s_movk_i32 s51, 0x2000\n 40:\n v_cmp_ne_u32 vcc, s50, v33\n s_cbranch_vccz 50f\n" \
    "s_sub_u32 s51, s51, 1\n s_cbranch_scc1 50f\n s_add_u32 s48, s48, 1\n s_sleep 4\n buffer_load_dwordx2 v[32:33], v39, %[rd], s49 offen sc1\n" \
    "s_waitcnt vmcnt(0)\n s_branch 40b\n 50:\n s_add_u32 m0, s41, 4096\n s_add_u32 s40, s47, s46\n" \
    "buffer_load_dwordx4 v36, %[rd], s40 offen sc1 lds\n" \
    "buffer_load_dwordx4 v36, %[rd], s40 offen offset:1024 sc1 lds\n" \
    "s_add_u32 s46, s46, 8192\n s_and_b32 s46, s46, 0xffff\n ds_max_u32 v35, v32\n v_exp_f32 v221, v221\n v_exp_f32 v222, v222\n" \
    "v_exp_f32 v223, v223\n v_exp_f32 v224, v224\n v_exp_f32 v225, v225\n v_smfmac_f32_32x32x32_bf16 v[8:23], v[240:243], v[216:223], v251\n" \
    "ds_read_b128 v[240:243], v249 offset:2048\n" \
    "v_exp_f32 v226, v226\n s_setprio 0\n v_exp_f32 v227, v227\n v_exp_f32 v228, v228\n v_exp_f32 v229, v229\n" \
    "s_setprio 1\n v_exp_f32 v230, v230\n v_exp_f32 v231, v231\n v_exp_f32 v200, v200\n v_exp_f32 v201, v201\n" \
    "v_smfmac_f32_32x32x32_bf16 v[8:23], v[244:247], v[224:231], v251\n" \
    "v_exp_f32 v202, v202\n v_exp_f32 v203, v203\n v_exp_f32 v204, v204\n v_mfma_f32_32x32x16_bf16 v[216:231], v[236:239], v[24:27], v[184:199]\n" \
    "ds_read_b128 v[244:247], v249 offset:2560\n" \
    "ds_read_b128 v[28:31], v248 offset:3072\n" \
    "v_exp_f32 v205, v205\n v_exp_f32 v206, v206\n v_exp_f32 v207, v207\n v_exp_f32 v208, v208\n v_exp_f32 v209, v209\n" \
    "s_waitcnt lgkmcnt(2)\n v_smfmac_f32_32x32x32_bf16 v[168:183], v[240:243], v[200:207], v251\n" \
    "v_exp_f32 v210, v210\n s_setprio 0\n v_exp_f32 v211, v211\n v_exp_f32 v212, v212\n v_exp_f32 v213, v213\n" \
    "s_setprio 1\n v_exp_f32 v214, v214\n v_exp_f32 v215, v215\n v_exp_f32 v216, v216\n v_exp_f32 v217, v217\n" \
    "s_waitcnt lgkmcnt(0)\n v_smfmac_f32_32x32x32_bf16 v[168:183], v[244:247], v[208:215], v251\n" \
    "v_exp_f32 v218, v218\n v_exp_f32 v219, v219\n v_exp_f32 v220, v220\n v_mfma_f32_32x32x16_bf16 v[200:215], v[28:31], v[232:235], v[184:199]\n" \
    "v_exp_f32 v221, v221\n v_exp_f32 v222, v222\n v_exp_f32 v223, v223\n v_exp_f32 v224, v224\n v_exp_f32 v225, v225\n" \
    "v_smfmac_f32_32x32x32_bf16 v[8:23], v[240:243], v[216:223], v251\n" \
    "ds_read_b128 v[240:243], v249 offset:3072\n" \
    "v_exp_f32 v226, v226\n s_setprio 0\n v_exp_f32 v227, v227\n v_exp_f32 v228, v228\n v_exp_f32 v229, v229\n" \
    "s_setprio 1\n v_exp_f32 v230, v230\n v_exp_f32 v231, v231\n v_exp_f32 v200, v200\n v_exp_f32 v201, v201\n" \
    "v_smfmac_f32_32x32x32_bf16 v[8:23], v[244:247], v[224:231], v251\n" \
    "v_exp_f32 v202, v202\n v_exp_f32 v203, v203\n v_exp_f32 v204, v204\n v_mfma_f32_32x32x16_bf16 v[216:231], v[28:31], v[24:27], v[184:199]\n" \
    "ds_read_b128 v[244:247], v249 offset:3584\n" \
    "s_waitcnt vmcnt(0)\n s_barrier\n ds_read_b128 v[236:239], v248 offset:4096\n" \
    "ds_read_b32 v34, v35\n" \
    "v_exp_f32 v205, v205\n v_exp_f32 v206, v206\n v_exp_f32 v207, v207\n v_exp_f32 v208, v208\n v_exp_f32 v209, v209\n" \
    "s_waitcnt lgkmcnt(2)\n v_smfmac_f32_32x32x32_bf16 v[168:183], v[240:243], v[200:207], v251\n" \
    "v_exp_f32 v210, v210\n s_setprio 0\n v_exp_f32 v211, v211\n v_exp_f32 v212, v212\n v_exp_f32 v213, v213\n" \
    "s_setprio 1\n v_exp_f32 v214, v214\n v_exp_f32 v215, v215\n v_exp_f32 v216, v216\n v_exp_f32 v217, v217\n" \
    "s_waitcnt lgkmcnt(0)\n v_smfmac_f32_32x32x32_bf16 v[168:183], v[244:247], v[208:215], v251\n" \
    "v_exp_f32 v218, v218\n v_exp_f32 v219, v219\n v_exp_f32 v220, v220\n v_mfma_f32_32x32x16_bf16 v[200:215], v[236:239], v[232:235], v[184:199]\n" \
    "s_add_u32 m0, s41, 8192\n s_add_u32 s40, s47, s46\n buffer_load_dwordx4 v36, %[rd], s40 offen sc1 lds\n" \
    "buffer_load_dwordx4 v36, %[rd], s40 offen offset:1024 sc1 lds\n" \
    "s_add_u32 s46, s46, 8192\n s_and_b32 s46, s46, 0xffff\n v_exp_f32 v221, v221\n v_exp_f32 v222, v222\n v_exp_f32 v223, v223\n" \
    "v_exp_f32 v224, v224\n v_exp_f32 v225, v225\n v_smfmac_f32_32x32x32_bf16 v[8:23], v[240:243], v[216:223], v251\n" \
    "v_add_u32 v249, v249, v250\n ds_read_b128 v[240:243], v249\n" \
    "v_exp_f32 v226, v226\n s_setprio 0\n v_exp_f32 v227, v227\n v_exp_f32 v228, v228\n v_exp_f32 v229, v229\n" \
    "s_setprio 1\n v_exp_f32 v230, v230\n v_exp_f32 v231, v231\n" \
    ""
#define MQA1_1 \
    "v_exp_f32 v200, v200\n v_exp_f32 v201, v201\n v_smfmac_f32_32x32x32_bf16 v[8:23], v[244:247], v[224:231], v251\n" \
    "v_exp_f32 v202, v202\n v_exp_f32 v203, v203\n v_exp_f32 v204, v204\n v_mfma_f32_32x32x16_bf16 v[216:231], v[236:239], v[24:27], v[184:199]\n" \
    "ds_read_b128 v[244:247], v249 offset:512\n" \
    "ds_read_b128 v[28:31], v248 offset:5120\n" \
    "v_exp_f32 v205, v205\n v_exp_f32 v206, v206\n v_exp_f32 v207, v207\n v_exp_f32 v208, v208\n v_exp_f32 v209, v209\n" \
    "s_waitcnt lgkmcnt(2)\n v_smfmac_f32_32x32x32_bf16 v[168:183], v[240:243], v[200:207], v251\n" \
    "v_exp_f32 v210, v210\n s_setprio 0\n v_exp_f32 v211, v211\n v_exp_f32 v212, v212\n v_exp_f32 v213, v213\n" \
    "s_setprio 1\n v_exp_f32 v214, v214\n v_exp_f32 v215, v215\n v_exp_f32 v216, v216\n v_exp_f32 v217, v217\n" \
    "s_waitcnt lgkmcnt(0)\n v_smfmac_f32_32x32x32_bf16 v[168:183], v[244:247], v[208:215], v251\n" \
    "v_exp_f32 v218, v218\n v_exp_f32 v219, v219\n v_exp_f32 v220, v220\n v_mfma_f32_32x32x16_bf16 v[200:215], v[28:31], v[232:235], v[184:199]\n" \
    "v_exp_f32 v221, v221\n v_exp_f32 v222, v222\n v_exp_f32 v223, v223\n v_exp_f32 v224, v224\n v_exp_f32 v225, v225\n" \
    "v_smfmac_f32_32x32x32_bf16 v[8:23], v[240:243], v[216:223], v251\n" \
    "ds_read_b128 v[240:243], v249 offset:1024\n" \
    "v_exp_f32 v226, v226\n s_setprio 0\n v_exp_f32 v227, v227\n v_exp_f32 v228, v228\n v_exp_f32 v229, v229\n" \
    "s_setprio 1\n v_exp_f32 v230, v230\n v_exp_f32 v231, v231\n v_exp_f32 v200, v200\n v_exp_f32 v201, v201\n" \
    "v_smfmac_f32_32x32x32_bf16 v[8:23], v[244:247], v[224:231], v251\n" \
    "v_exp_f32 v202, v202\n v_exp_f32 v203, v203\n v_exp_f32 v204, v204\n v_mfma_f32_32x32x16_bf16 v[216:231], v[28:31], v[24:27], v[184:199]\n" \
    "ds_read_b128 v[244:247], v249 offset:1536\n" \
    "ds_read_b128 v[236:239], v248 offset:6144\n" \
    "buffer_store_dwordx4 v[4:7], v36, %[rd], %[r0] offen sc1\n" \
    "v_exp_f32 v205, v205\n v_exp_f32 v206, v206\n v_exp_f32 v207, v207\n v_exp_f32 v208, v208\n v_exp_f32 v209, v209\n" \
    "s_waitcnt lgkmcnt(2)\n v_smfmac_f32_32x32x32_bf16 v[168:183], v[240:243], v[200:207], v251\n" \
    "v_exp_f32 v210, v210\n s_setprio 0\n v_exp_f32 v211, v211\n v_exp_f32 v212, v212\n v_exp_f32 v213, v213\n" \
    "s_setprio 1\n v_exp_f32 v214, v214\n v_exp_f32 v215, v215\n v_exp_f32 v216, v216\n v_exp_f32 v217, v217\n" \
    "s_waitcnt lgkmcnt(0)\n v_smfmac_f32_32x32x32_bf16 v[168:183], v[244:247], v[208:215], v251\n" \
    "v_exp_f32 v218, v218\n v_exp_f32 v219, v219\n v_exp_f32 v220, v220\n v_mfma_f32_32x32x16_bf16 v[200:215], v[236:239], v[232:235], v[184:199]\n" \
    "v_exp_f32 v221, v221\n v_exp_f32 v222, v222\n v_exp_f32 v223, v223\n v_exp_f32 v224, v224\n v_exp_f32 v225, v225\n" \
    "v_smfmac_f32_32x32x32_bf16 v[8:23], v[240:243], v[216:223], v251\n" \
    "ds_read_b128 v[240:243], v249 offset:2048\n" \
    "v_exp_f32 v226, v226\n s_setprio 0\n v_exp_f32 v227, v227\n v_exp_f32 v228, v228\n v_exp_f32 v229, v229\n" \
    "s_setprio 1\n v_exp_f32 v230, v230\n v_exp_f32 v231, v231\n v_exp_f32 v200, v200\n v_exp_f32 v201, v201\n" \
    "v_smfmac_f32_32x32x32_bf16 v[8:23], v[244:247], v[224:231], v251\n" \
    "v_exp_f32 v202, v202\n v_exp_f32 v203, v203\n v_exp_f32 v204, v204\n v_mfma_f32_32x32x16_bf16 v[216:231], v[236:239], v[24:27], v[184:199]\n" \
    "ds_read_b128 v[244:247], v249 offset:2560\n" \
    "ds_read_b128 v[28:31], v248 offset:7168\n" \
    "buffer_load_dword %[f], %[fv], %[rd], %[fo] offen sc1\n" \
    "v_exp_f32 v205, v205\n v_exp_f32 v206, v206\n v_exp_f32 v207, v207\n v_exp_f32 v208, v208\n v_exp_f32 v209, v209\n" \
    "s_waitcnt lgkmcnt(2)\n v_smfmac_f32_32x32x32_bf16 v[168:183], v[240:243], v[200:207], v251\n" \
    "v_exp_f32 v210, v210\n s_setprio 0\n v_exp_f32 v211, v211\n v_exp_f32 v212, v212\n v_exp_f32 v213, v213\n" \
    "s_setprio 1\n v_exp_f32 v214, v214\n v_exp_f32 v215, v215\n v_exp_f32 v216, v216\n v_exp_f32 v217, v217\n" \
    "s_waitcnt lgkmcnt(0)\n v_smfmac_f32_32x32x32_bf16 v[168:183], v[244:247], v[208:215], v251\n" \
    "v_exp_f32 v218, v218\n v_exp_f32 v219, v219\n v_exp_f32 v220, v220\n v_mfma_f32_32x32x16_bf16 v[200:215], v[28:31], v[232:235], v[184:199]\n" \
    "v_exp_f32 v221, v221\n v_exp_f32 v222, v222\n v_exp_f32 v223, v223\n v_exp_f32 v224, v224\n v_exp_f32 v225, v225\n" \
    "v_smfmac_f32_32x32x32_bf16 v[8:23], v[240:243], v[216:223], v251\n" \
    "ds_read_b128 v[240:243], v249 offset:3072\n" \
    "v_exp_f32 v226, v226\n s_setprio 0\n v_exp_f32 v227, v227\n v_exp_f32 v228, v228\n v_exp_f32 v229, v229\n" \
    "s_setprio 1\n v_exp_f32 v230, v230\n v_exp_f32 v231, v231\n v_exp_f32 v200, v200\n v_exp_f32 v201, v201\n" \
    "v_smfmac_f32_32x32x32_bf16 v[8:23], v[244:247], v[224:231], v251\n" \
    "v_exp_f32 v202, v202\n v_exp_f32 v203, v203\n v_exp_f32 v204, v204\n v_mfma_f32_32x32x16_bf16 v[216:231], v[28:31], v[24:27], v[184:199]\n" \
    "ds_read_b128 v[244:247], v249 offset:3584\n" \
    "s_waitcnt vmcnt(2)\n s_barrier\n ds_read_b128 v[236:239], v248 offset:8192\n" \
    "v_exp_f32 v205, v205\n v_exp_f32 v206, v206\n v_exp_f32 v207, v207\n v_exp_f32 v208, v208\n v_exp_f32 v209, v209\n" \
    "s_waitcnt lgkmcnt(2)\n v_smfmac_f32_32x32x32_bf16 v[168:183], v[240:243], v[200:207], v251\n" \
    "v_exp_f32 v210, v210\n s_setprio 0\n v_exp_f32 v211, v211\n v_exp_f32 v212, v212\n v_exp_f32 v213, v213\n" \
    "s_setprio 1\n v_exp_f32 v214, v214\n v_exp_f32 v215, v215\n v_exp_f32 v216, v216\n v_exp_f32 v217, v217\n" \
    "s_waitcnt lgkmcnt(0)\n v_smfmac_f32_32x32x32_bf16 v[168:183], v[244:247], v[208:215], v251\n" \
    "v_exp_f32 v218, v218\n v_exp_f32 v219, v219\n v_exp_f32 v220, v220\n v_mfma_f32_32x32x16_bf16 v[200:215], v[236:239], v[232:235], v[184:199]\n" \
    "s_add_u32 m0, s41, 0\n s_add_u32 s40, s47, s46\n buffer_load_dwordx4 v36, %[rd], s40 offen sc1 lds\n" \
    "buffer_load_dwordx4 v36, %[rd], s40 offen offset:1024 sc1 lds\n" \
    "s_add_u32 s46, s46, 8192\n s_and_b32 s46, s46, 0xffff\n v_exp_f32 v221, v221\n v_exp_f32 v222, v222\n v_exp_f32 v223, v223\n" \
    "v_exp_f32 v224, v224\n v_exp_f32 v225, v225\n v_smfmac_f32_32x32x32_bf16 v[8:23], v[240:243], v[216:223], v251\n" \
    "v_add_u32 v249, v249, v250\n ds_read_b128 v[240:243], v249\n" \
    "v_exp_f32 v226, v226\n s_setprio 0\n v_exp_f32 v227, v227\n v_exp_f32 v228, v228\n v_exp_f32 v229, v229\n" \
    "s_setprio 1\n v_exp_f32 v230, v230\n v_exp_f32 v231, v231\n" \
    ""
#define MQA1_2 \
    "v_exp_f32 v200, v200\n v_exp_f32 v201, v201\n v_smfmac_f32_32x32x32_bf16 v[8:23], v[244:247], v[224:231], v251\n" \
    "v_exp_f32 v202, v202\n v_exp_f32 v203, v203\n v_exp_f32 v204, v204\n v_mfma_f32_32x32x16_bf16 v[216:231], v[236:239], v[24:27], v[184:199]\n" \
    "ds_read_b128 v[244:247], v249 offset:512\n" \
    "ds_read_b128 v[28:31], v248 offset:9216\n" \
    "buffer_load_dwordx4 v[40:43], v36, %[rd], s44 offen offset:0 sc1\n" \
    "v_exp_f32 v205, v205\n v_exp_f32 v206, v206\n v_exp_f32 v207, v207\n v_exp_f32 v208, v208\n v_exp_f32 v209, v209\n" \
    "s_waitcnt lgkmcnt(2)\n v_smfmac_f32_32x32x32_bf16 v[168:183], v[240:243], v[200:207], v251\n" \
    "v_exp_f32 v210, v210\n s_setprio 0\n v_exp_f32 v211, v211\n v_exp_f32 v212, v212\n v_exp_f32 v213, v213\n" \
    "s_setprio 1\n v_exp_f32 v214, v214\n v_exp_f32 v215, v215\n v_exp_f32 v216, v216\n v_exp_f32 v217, v217\n" \
    "s_waitcnt lgkmcnt(0)\n v_smfmac_f32_32x32x32_bf16 v[168:183], v[244:247], v[208:215], v251\n" \
    "v_exp_f32 v218, v218\n v_exp_f32 v219, v219\n v_exp_f32 v220, v220\n v_mfma_f32_32x32x16_bf16 v[200:215], v[28:31], v[232:235], v[184:199]\n" \
    "buffer_load_dwordx4 v[44:47], v36, %[rd], s44 offen offset:1024 sc1\n" \
    "v_exp_f32 v221, v221\n v_exp_f32 v222, v222\n v_exp_f32 v223, v223\n v_exp_f32 v224, v224\n v_exp_f32 v225, v225\n" \
    "v_smfmac_f32_32x32x32_bf16 v[8:23], v[240:243], v[216:223], v251\n" \
    "ds_read_b128 v[240:243], v249 offset:1024\n" \
    "v_exp_f32 v226, v226\n s_setprio 0\n v_exp_f32 v227, v227\n v_exp_f32 v228, v228\n v_exp_f32 v229, v229\n" \
    "s_setprio 1\n v_exp_f32 v230, v230\n v_exp_f32 v231, v231\n v_exp_f32 v200, v200\n v_exp_f32 v201, v201\n" \
    "v_smfmac_f32_32x32x32_bf16 v[8:23], v[244:247], v[224:231], v251\n" \
    "v_exp_f32 v202, v202\n v_exp_f32 v203, v203\n v_exp_f32 v204, v204\n v_mfma_f32_32x32x16_bf16 v[216:231], v[28:31], v[24:27], v[184:199]\n" \
    "ds_read_b128 v[244:247], v249 offset:1536\n" \
    "ds_read_b128 v[236:239], v248 offset:10240\n" \
    "buffer_load_dwordx4 v[48:51], v36, %[rd], s44 offen offset:2048 sc1\n" \
    "buffer_store_dwordx4 v[4:7], v36, %[rd], %[r0] offen sc1\n" \
    "v_exp_f32 v205, v205\n v_exp_f32 v206, v206\n v_exp_f32 v207, v207\n v_exp_f32 v208, v208\n v_exp_f32 v209, v209\n" \
    "s_waitcnt lgkmcnt(2)\n v_smfmac_f32_32x32x32_bf16 v[168:183], v[240:243], v[200:207], v251\n" \
    "v_exp_f32 v210, v210\n s_setprio 0\n v_exp_f32 v211, v211\n v_exp_f32 v212, v212\n v_exp_f32 v213, v213\n" \
    "s_setprio 1\n v_exp_f32 v214, v214\n v_exp_f32 v215, v215\n v_exp_f32 v216, v216\n v_exp_f32 v217, v217\n" \
    "s_waitcnt lgkmcnt(0)\n v_smfmac_f32_32x32x32_bf16 v[168:183], v[244:247], v[208:215], v251\n" \
    "v_exp_f32 v218, v218\n v_exp_f32 v219, v219\n v_exp_f32 v220, v220\n v_mfma_f32_32x32x16_bf16 v[200:215], v[236:239], v[232:235], v[184:199]\n" \
    "buffer_load_dwordx4 v[52:55], v36, %[rd], s44 offen offset:3072 sc1\n" \
    "s_mov_b32 m0, s43\n s_add_u32 s43, s43, 2048\n buffer_load_dwordx4 v36, %[rd], s42 offen sc1 lds\n" \
    "v_exp_f32 v221, v221\n v_exp_f32 v222, v222\n v_exp_f32 v223, v223\n v_exp_f32 v224, v224\n v_exp_f32 v225, v225\n" \
    "v_smfmac_f32_32x32x32_bf16 v[8:23], v[240:243], v[216:223], v251\n" \
    "ds_read_b128 v[240:243], v249 offset:2048\n" \
    "v_exp_f32 v226, v226\n s_setprio 0\n v_exp_f32 v227, v227\n v_exp_f32 v228, v228\n v_exp_f32 v229, v229\n" \
    "s_setprio 1\n v_exp_f32 v230, v230\n v_exp_f32 v231, v231\n v_exp_f32 v200, v200\n v_exp_f32 v201, v201\n" \
    "v_smfmac_f32_32x32x32_bf16 v[8:23], v[244:247], v[224:231], v251\n" \
    "v_exp_f32 v202, v202\n v_exp_f32 v203, v203\n v_exp_f32 v204, v204\n v_mfma_f32_32x32x16_bf16 v[216:231], v[236:239], v[24:27], v[184:199]\n" \
    "ds_read_b128 v[244:247], v249 offset:2560\n" \
    "ds_read_b128 v[28:31], v248 offset:11264\n" \
    "buffer_load_dwordx4 v[56:59], v36, %[rd], s45 offen offset:0 sc1\n" \
    "v_exp_f32 v205, v205\n v_exp_f32 v206, v206\n v_exp_f32 v207, v207\n v_exp_f32 v208, v208\n v_exp_f32 v209, v209\n" \
    "s_waitcnt lgkmcnt(2)\n v_smfmac_f32_32x32x32_bf16 v[168:183], v[240:243], v[200:207], v251\n" \
    "v_exp_f32 v210, v210\n s_setprio 0\n v_exp_f32 v211, v211\n v_exp_f32 v212, v212\n v_exp_f32 v213, v213\n" \
    "s_setprio 1\n v_exp_f32 v214, v214\n v_exp_f32 v215, v215\n v_exp_f32 v216, v216\n v_exp_f32 v217, v217\n" \
    "s_waitcnt lgkmcnt(0)\n v_smfmac_f32_32x32x32_bf16 v[168:183], v[244:247], v[208:215], v251\n" \
    "v_exp_f32 v218, v218\n v_exp_f32 v219, v219\n v_exp_f32 v220, v220\n v_mfma_f32_32x32x16_bf16 v[200:215], v[28:31], v[232:235], v[184:199]\n" \
    "buffer_load_dwordx4 v[60:63], v36, %[rd], s45 offen offset:1024 sc1\n" \
    "buffer_load_dwordx4 v36, %[rd], s42 offen offset:1024 sc1 lds\n" \
    "s_add_u32 s42, s42, 2048\n v_exp_f32 v221, v221\n v_exp_f32 v222, v222\n v_exp_f32 v223, v223\n v_exp_f32 v224, v224\n" \
    "v_exp_f32 v225, v225\n v_smfmac_f32_32x32x32_bf16 v[8:23], v[240:243], v[216:223], v251\n" \
    "ds_read_b128 v[240:243], v249 offset:3072\n" \
    "v_exp_f32 v226, v226\n s_setprio 0\n v_exp_f32 v227, v227\n v_exp_f32 v228, v228\n v_exp_f32 v229, v229\n" \
    "s_setprio 1\n v_exp_f32 v230, v230\n v_exp_f32 v231, v231\n v_exp_f32 v200, v200\n v_exp_f32 v201, v201\n" \
    "v_smfmac_f32_32x32x32_bf16 v[8:23], v[244:247], v[224:231], v251\n" \
    "v_exp_f32 v202, v202\n v_exp_f32 v203, v203\n v_exp_f32 v204, v204\n v_mfma_f32_32x32x16_bf16 v[216:231], v[28:31], v[24:27], v[184:199]\n" \
    "ds_read_b128 v[244:247], v249 offset:3584\n" \
    "s_waitcnt vmcnt(9)\n s_barrier\n ds_read_b128 v[236:239], v248 offset:0\n" \
    "buffer_load_dwordx4 v[64:67], v36, %[rd], s45 offen offset:2048 sc1\n" \
    "v_exp_f32 v205, v205\n v_exp_f32 v206, v206\n v_exp_f32 v207, v207\n v_exp_f32 v208, v208\n v_exp_f32 v209, v209\n" \
    "s_waitcnt lgkmcnt(2)\n v_smfmac_f32_32x32x32_bf16 v[168:183], v[240:243], v[200:207], v251\n" \
    "v_exp_f32 v210, v210\n s_setprio 0\n v_exp_f32 v211, v211\n v_exp_f32 v212, v212\n v_exp_f32 v213, v213\n" \
    "s_setprio 1\n v_exp_f32 v214, v214\n v_exp_f32 v215, v215\n v_exp_f32 v216, v216\n v_exp_f32 v217, v217\n" \
    "s_waitcnt lgkmcnt(0)\n v_smfmac_f32_32x32x32_bf16 v[168:183], v[244:247], v[208:215], v251\n" \
    "v_exp_f32 v218, v218\n v_exp_f32 v219, v219\n v_exp_f32 v220, v220\n v_mfma_f32_32x32x16_bf16 v[200:215], v[236:239], v[232:235], v[184:199]\n" \
    "buffer_load_dwordx4 v[68:71], v36, %[rd], s45 offen offset:3072 sc1\n" \
    "s_add_u32 s44, s44, 8192\n s_add_u32 s45, s45, 8192\n s_add_u32 m0, s41, 4096\n s_add_u32 s40, s47, s46\n buffer_load_dwordx4 v36, %[rd], s40 offen sc1 lds\n" \
    "buffer_load_dwordx4 v36, %[rd], s40 offen offset:1024 sc1 lds\n" \
    "s_add_u32 s46, s46, 8192\n s_and_b32 s46, s46, 0xffff\n v_exp_f32 v221, v221\n v_exp_f32 v222, v222\n v_exp_f32 v223, v223\n" \
    "v_exp_f32 v224, v224\n v_exp_f32 v225, v225\n v_smfmac_f32_32x32x32_bf16 v[8:23], v[240:243], v[216:223], v251\n" \
    "v_add_u32 v249, v249, v252\n ds_read_b128 v[240:243], v249\n" \
    "v_exp_f32 v226, v226\n s_setprio 0\n v_exp_f32 v227, v227\n v_exp_f32 v228, v228\n v_exp_f32 v229, v229\n" \
    "s_setprio 1\n v_exp_f32 v230, v230\n v_exp_f32 v231, v231\n" \
    ""
#define MQA1_3 \
    "v_exp_f32 v200, v200\n v_exp_f32 v201, v201\n v_smfmac_f32_32x32x32_bf16 v[8:23], v[244:247], v[224:231], v251\n" \
    "v_exp_f32 v202, v202\n v_exp_f32 v203, v203\n v_exp_f32 v204, v204\n v_mfma_f32_32x32x16_bf16 v[216:231], v[236:239], v[24:27], v[184:199]\n" \
    "ds_read_b128 v[244:247], v249 offset:512\n" \
    "ds_read_b128 v[28:31], v248 offset:1024\n" \
    "buffer_load_dwordx4 v[72:75], v36, %[rd], s44 offen offset:0 sc1\n" \
    "v_exp_f32 v205, v205\n v_exp_f32 v206, v206\n v_exp_f32 v207, v207\n v_exp_f32 v208, v208\n v_exp_f32 v209, v209\n" \
    "s_waitcnt lgkmcnt(2)\n v_smfmac_f32_32x32x32_bf16 v[168:183], v[240:243], v[200:207], v251\n" \
    "v_exp_f32 v210, v210\n s_setprio 0\n v_exp_f32 v211, v211\n v_exp_f32 v212, v212\n v_exp_f32 v213, v213\n" \
    "s_setprio 1\n v_exp_f32 v214, v214\n v_exp_f32 v215, v215\n v_exp_f32 v216, v216\n v_exp_f32 v217, v217\n" \
    "s_waitcnt lgkmcnt(0)\n v_smfmac_f32_32x32x32_bf16 v[168:183], v[244:247], v[208:215], v251\n" \
    "v_exp_f32 v218, v218\n v_exp_f32 v219, v219\n v_exp_f32 v220, v220\n v_mfma_f32_32x32x16_bf16 v[200:215], v[28:31], v[232:235], v[184:199]\n" \
    "buffer_load_dwordx4 v[76:79], v36, %[rd], s44 offen offset:1024 sc1\n" \
    "v_exp_f32 v221, v221\n v_exp_f32 v222, v222\n v_exp_f32 v223, v223\n v_exp_f32 v224, v224\n v_exp_f32 v225, v225\n" \
    "v_smfmac_f32_32x32x32_bf16 v[8:23], v[240:243], v[216:223], v251\n" \
    "ds_read_b128 v[240:243], v249 offset:1024\n" \
    "v_exp_f32 v226, v226\n s_setprio 0\n v_exp_f32 v227, v227\n v_exp_f32 v228, v228\n v_exp_f32 v229, v229\n" \
    "s_setprio 1\n v_exp_f32 v230, v230\n v_exp_f32 v231, v231\n v_exp_f32 v200, v200\n v_exp_f32 v201, v201\n" \
    "v_smfmac_f32_32x32x32_bf16 v[8:23], v[244:247], v[224:231], v251\n" \
    "v_exp_f32 v202, v202\n v_exp_f32 v203, v203\n v_exp_f32 v204, v204\n v_mfma_f32_32x32x16_bf16 v[216:231], v[28:31], v[24:27], v[184:199]\n" \
    "ds_read_b128 v[244:247], v249 offset:1536\n" \
    "ds_read_b128 v[236:239], v248 offset:2048\n" \
    "buffer_load_dwordx4 v[80:83], v36, %[rd], s44 offen offset:2048 sc1\n" \
    "buffer_store_dwordx4 v[4:7], v36, %[rd], %[r0] offen sc1\n" \
    "v_exp_f32 v205, v205\n v_exp_f32 v206, v206\n v_exp_f32 v207, v207\n v_exp_f32 v208, v208\n v_exp_f32 v209, v209\n" \
    "s_waitcnt lgkmcnt(2)\n v_smfmac_f32_32x32x32_bf16 v[168:183], v[240:243], v[200:207], v251\n" \
    "v_exp_f32 v210, v210\n s_setprio 0\n v_exp_f32 v211, v211\n v_exp_f32 v212, v212\n v_exp_f32 v213, v213\n" \
    "s_setprio 1\n v_exp_f32 v214, v214\n v_exp_f32 v215, v215\n v_exp_f32 v216, v216\n v_exp_f32 v217, v217\n" \
    "s_waitcnt lgkmcnt(0)\n v_smfmac_f32_32x32x32_bf16 v[168:183], v[244:247], v[208:215], v251\n" \
    "v_exp_f32 v218, v218\n v_exp_f32 v219, v219\n v_exp_f32 v220, v220\n v_mfma_f32_32x32x16_bf16 v[200:215], v[236:239], v[232:235], v[184:199]\n" \
    "buffer_load_dwordx4 v[84:87], v36, %[rd], s44 offen offset:3072 sc1\n" \
    "s_mov_b32 m0, s43\n s_add_u32 s43, s43, 2048\n buffer_load_dwordx4 v36, %[rd], s42 offen sc1 lds\n" \
    "v_exp_f32 v221, v221\n v_exp_f32 v222, v222\n v_exp_f32 v223, v223\n v_exp_f32 v224, v224\n v_exp_f32 v225, v225\n" \
    "v_smfmac_f32_32x32x32_bf16 v[8:23], v[240:243], v[216:223], v251\n" \
    "ds_read_b128 v[240:243], v249 offset:2048\n" \
    "v_exp_f32 v226, v226\n s_setprio 0\n v_exp_f32 v227, v227\n v_exp_f32 v228, v228\n v_exp_f32 v229, v229\n" \
    "s_setprio 1\n v_exp_f32 v230, v230\n v_exp_f32 v231, v231\n v_exp_f32 v200, v200\n v_exp_f32 v201, v201\n" \
    "v_smfmac_f32_32x32x32_bf16 v[8:23], v[244:247], v[224:231], v251\n" \
    "v_exp_f32 v202, v202\n v_exp_f32 v203, v203\n v_exp_f32 v204, v204\n v_mfma_f32_32x32x16_bf16 v[216:231], v[236:239], v[24:27], v[184:199]\n" \
    "ds_read_b128 v[244:247], v249 offset:2560\n" \
    "ds_read_b128 v[28:31], v248 offset:3072\n" \
    "buffer_load_dwordx4 v[88:91], v36, %[rd], s45 offen offset:0 sc1\n" \
    "v_exp_f32 v205, v205\n v_exp_f32 v206, v206\n v_exp_f32 v207, v207\n v_exp_f32 v208, v208\n v_exp_f32 v209, v209\n" \
    "s_waitcnt lgkmcnt(2)\n v_smfmac_f32_32x32x32_bf16 v[168:183], v[240:243], v[200:207], v251\n" \
    "v_exp_f32 v210, v210\n s_setprio 0\n v_exp_f32 v211, v211\n v_exp_f32 v212, v212\n v_exp_f32 v213, v213\n" \
    "s_setprio 1\n v_exp_f32 v214, v214\n v_exp_f32 v215, v215\n v_exp_f32 v216, v216\n v_exp_f32 v217, v217\n" \
    "s_waitcnt lgkmcnt(0)\n v_smfmac_f32_32x32x32_bf16 v[168:183], v[244:247], v[208:215], v251\n" \
    "v_exp_f32 v218, v218\n v_exp_f32 v219, v219\n v_exp_f32 v220, v220\n v_mfma_f32_32x32x16_bf16 v[200:215], v[28:31], v[232:235], v[184:199]\n" \
    "buffer_load_dwordx4 v[92:95], v36, %[rd], s45 offen offset:1024 sc1\n" \
    "buffer_load_dwordx4 v36, %[rd], s42 offen offset:1024 sc1 lds\n" \
    "s_add_u32 s42, s42, 2048\n v_exp_f32 v221, v221\n v_exp_f32 v222, v222\n v_exp_f32 v223, v223\n v_exp_f32 v224, v224\n" \
    "v_exp_f32 v225, v225\n v_smfmac_f32_32x32x32_bf16 v[8:23], v[240:243], v[216:223], v251\n" \
    "ds_read_b128 v[240:243], v249 offset:3072\n" \
    "v_exp_f32 v226, v226\n s_setprio 0\n v_exp_f32 v227, v227\n v_exp_f32 v228, v228\n v_exp_f32 v229, v229\n" \
    "s_setprio 1\n v_exp_f32 v230, v230\n v_exp_f32 v231, v231\n v_exp_f32 v200, v200\n v_exp_f32 v201, v201\n" \
    "v_smfmac_f32_32x32x32_bf16 v[8:23], v[244:247], v[224:231], v251\n" \
    "v_exp_f32 v202, v202\n v_exp_f32 v203, v203\n v_exp_f32 v204, v204\n v_mfma_f32_32x32x16_bf16 v[216:231], v[28:31], v[24:27], v[184:199]\n" \
    "ds_read_b128 v[244:247], v249 offset:3584\n" \
    "s_waitcnt vmcnt(9)\n s_barrier\n ds_read_b128 v[236:239], v248 offset:4096\n" \
    "buffer_load_dwordx4 v[96:99], v36, %[rd], s45 offen offset:2048 sc1\n" \
    "v_exp_f32 v205, v205\n v_exp_f32 v206, v206\n v_exp_f32 v207, v207\n v_exp_f32 v208, v208\n v_exp_f32 v209, v209\n" \
    "s_waitcnt lgkmcnt(2)\n v_smfmac_f32_32x32x32_bf16 v[168:183], v[240:243], v[200:207], v251\n" \
    "v_exp_f32 v210, v210\n s_setprio 0\n v_exp_f32 v211, v211\n v_exp_f32 v212, v212\n v_exp_f32 v213, v213\n" \
    "s_setprio 1\n v_exp_f32 v214, v214\n v_exp_f32 v215, v215\n v_exp_f32 v216, v216\n v_exp_f32 v217, v217\n" \
    "s_waitcnt lgkmcnt(0)\n v_smfmac_f32_32x32x32_bf16 v[168:183], v[244:247], v[208:215], v251\n" \
    "v_exp_f32 v218, v218\n v_exp_f32 v219, v219\n v_exp_f32 v220, v220\n v_mfma_f32_32x32x16_bf16 v[200:215], v[236:239], v[232:235], v[184:199]\n" \
    "buffer_load_dwordx4 v[100:103], v36, %[rd], s45 offen offset:3072 sc1\n" \
    "s_add_u32 s44, s44, 8192\n s_add_u32 s45, s45, 8192\n s_add_u32 m0, s41, 8192\n s_add_u32 s40, s47, s46\n buffer_load_dwordx4 v36, %[rd], s40 offen sc1 lds\n" \
    "buffer_load_dwordx4 v36, %[rd], s40 offen offset:1024 sc1 lds\n" \
    "s_add_u32 s46, s46, 8192\n s_and_b32 s46, s46, 0xffff\n v_exp_f32 v221, v221\n v_exp_f32 v222, v222\n v_exp_f32 v223, v223\n" \
    "v_exp_f32 v224, v224\n v_exp_f32 v225, v225\n v_smfmac_f32_32x32x32_bf16 v[8:23], v[240:243], v[216:223], v251\n" \
    "v_add_u32 v249, v249, v250\n ds_read_b128 v[240:243], v249\n" \
    "v_exp_f32 v226, v226\n s_setprio 0\n v_exp_f32 v227, v227\n v_exp_f32 v228, v228\n v_exp_f32 v229, v229\n" \
    "s_setprio 1\n v_exp_f32 v230, v230\n v_exp_f32 v231, v231\n" \
    ""
#define MQA1_4 \
    "v_exp_f32 v200, v200\n v_exp_f32 v201, v201\n v_smfmac_f32_32x32x32_bf16 v[8:23], v[244:247], v[224:231], v251\n" \
    "v_exp_f32 v202, v202\n v_exp_f32 v203, v203\n v_exp_f32 v204, v204\n v_mfma_f32_32x32x16_bf16 v[216:231], v[236:239], v[24:27], v[184:199]\n" \
    "ds_read_b128 v[244:247], v249 offset:512\n" \
    "ds_read_b128 v[28:31], v248 offset:5120\n" \
    "buffer_load_dwordx4 v[104:107], v36, %[rd], s44 offen offset:0 sc1\n" \
    "v_exp_f32 v205, v205\n v_exp_f32 v206, v206\n v_exp_f32 v207, v207\n v_exp_f32 v208, v208\n v_exp_f32 v209, v209\n" \
    "s_waitcnt lgkmcnt(2)\n v_smfmac_f32_32x32x32_bf16 v[168:183], v[240:243], v[200:207], v251\n" \
    "v_exp_f32 v210, v210\n s_setprio 0\n v_exp_f32 v211, v211\n v_exp_f32 v212, v212\n v_exp_f32 v213, v213\n" \
    "s_setprio 1\n v_exp_f32 v214, v214\n v_exp_f32 v215, v215\n v_exp_f32 v216, v216\n v_exp_f32 v217, v217\n" \
    "s_waitcnt lgkmcnt(0)\n v_smfmac_f32_32x32x32_bf16 v[168:183], v[244:247], v[208:215], v251\n" \
    "v_exp_f32 v218, v218\n v_exp_f32 v219, v219\n v_exp_f32 v220, v220\n v_mfma_f32_32x32x16_bf16 v[200:215], v[28:31], v[232:235], v[184:199]\n" \
    "buffer_load_dwordx4 v[108:111], v36, %[rd], s44 offen offset:1024 sc1\n" \
    "v_exp_f32 v221, v221\n v_exp_f32 v222, v222\n v_exp_f32 v223, v223\n v_exp_f32 v224, v224\n v_exp_f32 v225, v225\n" \
    "v_smfmac_f32_32x32x32_bf16 v[8:23], v[240:243], v[216:223], v251\n" \
    "ds_read_b128 v[240:243], v249 offset:1024\n" \
    "v_exp_f32 v226, v226\n s_setprio 0\n v_exp_f32 v227, v227\n v_exp_f32 v228, v228\n v_exp_f32 v229, v229\n" \
    "s_setprio 1\n v_exp_f32 v230, v230\n v_exp_f32 v231, v231\n v_exp_f32 v200, v200\n v_exp_f32 v201, v201\n" \
    "v_smfmac_f32_32x32x32_bf16 v[8:23], v[244:247], v[224:231], v251\n" \
    "v_exp_f32 v202, v202\n v_exp_f32 v203, v203\n v_exp_f32 v204, v204\n v_mfma_f32_32x32x16_bf16 v[216:231], v[28:31], v[24:27], v[184:199]\n" \
    "ds_read_b128 v[244:247], v249 offset:1536\n" \
    "ds_read_b128 v[236:239], v248 offset:6144\n" \
    "buffer_load_dwordx4 v[112:115], v36, %[rd], s44 offen offset:2048 sc1\n" \
    "buffer_store_dwordx4 v[4:7], v36, %[rd], %[r0] offen sc1\n" \
    "v_exp_f32 v205, v205\n v_exp_f32 v206, v206\n v_exp_f32 v207, v207\n v_exp_f32 v208, v208\n v_exp_f32 v209, v209\n" \
    "s_waitcnt lgkmcnt(2)\n v_smfmac_f32_32x32x32_bf16 v[168:183], v[240:243], v[200:207], v251\n" \
    "v_exp_f32 v210, v210\n s_setprio 0\n v_exp_f32 v211, v211\n v_exp_f32 v212, v212\n v_exp_f32 v213, v213\n" \
    "s_setprio 1\n v_exp_f32 v214, v214\n v_exp_f32 v215, v215\n v_exp_f32 v216, v216\n v_exp_f32 v217, v217\n" \
    "s_waitcnt lgkmcnt(0)\n v_smfmac_f32_32x32x32_bf16 v[168:183], v[244:247], v[208:215], v251\n" \
    "v_exp_f32 v218, v218\n v_exp_f32 v219, v219\n v_exp_f32 v220, v220\n v_mfma_f32_32x32x16_bf16 v[200:215], v[236:239], v[232:235], v[184:199]\n" \
    "buffer_load_dwordx4 v[116:119], v36, %[rd], s44 offen offset:3072 sc1\n" \
    "s_mov_b32 m0, s43\n s_add_u32 s43, s43, 2048\n buffer_load_dwordx4 v36, %[rd], s42 offen sc1 lds\n" \
    "v_exp_f32 v221, v221\n v_exp_f32 v222, v222\n v_exp_f32 v223, v223\n v_exp_f32 v224, v224\n v_exp_f32 v225, v225\n" \
    "v_smfmac_f32_32x32x32_bf16 v[8:23], v[240:243], v[216:223], v251\n" \
    "ds_read_b128 v[240:243], v249 offset:2048\n" \
    "v_exp_f32 v226, v226\n s_setprio 0\n v_exp_f32 v227, v227\n v_exp_f32 v228, v228\n v_exp_f32 v229, v229\n" \
    "s_setprio 1\n v_exp_f32 v230, v230\n v_exp_f32 v231, v231\n v_exp_f32 v200, v200\n v_exp_f32 v201, v201\n" \
    "v_smfmac_f32_32x32x32_bf16 v[8:23], v[244:247], v[224:231], v251\n" \
    "v_exp_f32 v202, v202\n v_exp_f32 v203, v203\n v_exp_f32 v204, v204\n v_mfma_f32_32x32x16_bf16 v[216:231], v[236:239], v[24:27], v[184:199]\n" \
    "ds_read_b128 v[244:247], v249 offset:2560\n" \
    "ds_read_b128 v[28:31], v248 offset:7168\n" \
    "buffer_load_dwordx4 v[120:123], v36, %[rd], s45 offen offset:0 sc1\n" \
    "v_exp_f32 v205, v205\n v_exp_f32 v206, v206\n v_exp_f32 v207, v207\n v_exp_f32 v208, v208\n v_exp_f32 v209, v209\n" \
    "s_waitcnt lgkmcnt(2)\n v_smfmac_f32_32x32x32_bf16 v[168:183], v[240:243], v[200:207], v251\n" \
    "v_exp_f32 v210, v210\n s_setprio 0\n v_exp_f32 v211, v211\n v_exp_f32 v212, v212\n v_exp_f32 v213, v213\n" \
    "s_setprio 1\n v_exp_f32 v214, v214\n v_exp_f32 v215, v215\n v_exp_f32 v216, v216\n v_exp_f32 v217, v217\n" \
    "s_waitcnt lgkmcnt(0)\n v_smfmac_f32_32x32x32_bf16 v[168:183], v[244:247], v[208:215], v251\n" \
    "v_exp_f32 v218, v218\n v_exp_f32 v219, v219\n v_exp_f32 v220, v220\n v_mfma_f32_32x32x16_bf16 v[200:215], v[28:31], v[232:235], v[184:199]\n" \
    "buffer_load_dwordx4 v[124:127], v36, %[rd], s45 offen offset:1024 sc1\n" \
    "buffer_load_dwordx4 v36, %[rd], s42 offen offset:1024 sc1 lds\n" \
    "s_add_u32 s42, s42, 2048\n v_exp_f32 v221, v221\n v_exp_f32 v222, v222\n v_exp_f32 v223, v223\n v_exp_f32 v224, v224\n" \
    "v_exp_f32 v225, v225\n v_smfmac_f32_32x32x32_bf16 v[8:23], v[240:243], v[216:223], v251\n" \
    "ds_read_b128 v[240:243], v249 offset:3072\n" \
    "v_exp_f32 v226, v226\n s_setprio 0\n v_exp_f32 v227, v227\n v_exp_f32 v228, v228\n v_exp_f32 v229, v229\n" \
    "s_setprio 1\n v_exp_f32 v230, v230\n v_exp_f32 v231, v231\n v_exp_f32 v200, v200\n v_exp_f32 v201, v201\n" \
    "v_smfmac_f32_32x32x32_bf16 v[8:23], v[244:247], v[224:231], v251\n" \
    "v_exp_f32 v202, v202\n v_exp_f32 v203, v203\n v_exp_f32 v204, v204\n v_mfma_f32_32x32x16_bf16 v[216:231], v[28:31], v[24:27], v[184:199]\n" \
    "ds_read_b128 v[244:247], v249 offset:3584\n" \
    "s_waitcnt vmcnt(9)\n s_barrier\n ds_read_b128 v[236:239], v248 offset:8192\n" \
    "buffer_load_dwordx4 v[128:131], v36, %[rd], s45 offen offset:2048 sc1\n" \
    "v_exp_f32 v205, v205\n v_exp_f32 v206, v206\n v_exp_f32 v207, v207\n v_exp_f32 v208, v208\n v_exp_f32 v209, v209\n" \
    "s_waitcnt lgkmcnt(2)\n v_smfmac_f32_32x32x32_bf16 v[168:183], v[240:243], v[200:207], v251\n" \
    "v_exp_f32 v210, v210\n s_setprio 0\n v_exp_f32 v211, v211\n v_exp_f32 v212, v212\n v_exp_f32 v213, v213\n" \
    "s_setprio 1\n v_exp_f32 v214, v214\n v_exp_f32 v215, v215\n v_exp_f32 v216, v216\n v_exp_f32 v217, v217\n" \
    "s_waitcnt lgkmcnt(0)\n v_smfmac_f32_32x32x32_bf16 v[168:183], v[244:247], v[208:215], v251\n" \
    "v_exp_f32 v218, v218\n v_exp_f32 v219, v219\n v_exp_f32 v220, v220\n v_mfma_f32_32x32x16_bf16 v[200:215], v[236:239], v[232:235], v[184:199]\n" \
    "buffer_load_dwordx4 v[132:135], v36, %[rd], s45 offen offset:3072 sc1\n" \
    "s_add_u32 s44, s44, 8192\n s_add_u32 s45, s45, 8192\n s_add_u32 m0, s41, 0\n s_add_u32 s40, s47, s46\n buffer_load_dwordx4 v36, %[rd], s40 offen sc1 lds\n" \
    "buffer_load_dwordx4 v36, %[rd], s40 offen offset:1024 sc1 lds\n" \
    "s_add_u32 s46, s46, 8192\n s_and_b32 s46, s46, 0xffff\n v_exp_f32 v221, v221\n v_exp_f32 v222, v222\n v_exp_f32 v223, v223\n" \
    "v_exp_f32 v224, v224\n v_exp_f32 v225, v225\n v_smfmac_f32_32x32x32_bf16 v[8:23], v[240:243], v[216:223], v251\n" \
    "v_add_u32 v249, v249, v250\n ds_read_b128 v[240:243], v249\n" \
    "v_exp_f32 v226, v226\n s_setprio 0\n v_exp_f32 v227, v227\n v_exp_f32 v228, v228\n v_exp_f32 v229, v229\n" \
    "s_setprio 1\n v_exp_f32 v230, v230\n v_exp_f32 v231, v231\n" \
    ""
#define MQA1_5 \
    "v_exp_f32 v200, v200\n v_exp_f32 v201, v201\n v_smfmac_f32_32x32x32_bf16 v[8:23], v[244:247], v[224:231], v251\n" \
    "v_exp_f32 v202, v202\n v_exp_f32 v203, v203\n v_exp_f32 v204, v204\n v_mfma_f32_32x32x16_bf16 v[216:231], v[236:239], v[24:27], v[184:199]\n" \
    "ds_read_b128 v[244:247], v249 offset:512\n" \
    "ds_read_b128 v[28:31], v248 offset:9216\n" \
    "buffer_load_dwordx4 v[136:139], v36, %[rd], s44 offen offset:0 sc1\n" \
    "v_exp_f32 v205, v205\n v_exp_f32 v206, v206\n v_exp_f32 v207, v207\n v_exp_f32 v208, v208\n v_exp_f32 v209, v209\n" \
    "s_waitcnt lgkmcnt(2)\n v_smfmac_f32_32x32x32_bf16 v[168:183], v[240:243], v[200:207], v251\n" \
    "v_exp_f32 v210, v210\n s_setprio 0\n v_exp_f32 v211, v211\n v_exp_f32 v212, v212\n v_exp_f32 v213, v213\n" \
    "s_setprio 1\n v_exp_f32 v214, v214\n v_exp_f32 v215, v215\n v_exp_f32 v216, v216\n v_exp_f32 v217, v217\n" \
    "s_waitcnt lgkmcnt(0)\n v_smfmac_f32_32x32x32_bf16 v[168:183], v[244:247], v[208:215], v251\n" \
    "v_exp_f32 v218, v218\n v_exp_f32 v219, v219\n v_exp_f32 v220, v220\n v_mfma_f32_32x32x16_bf16 v[200:215], v[28:31], v[232:235], v[184:199]\n" \
    "buffer_load_dwordx4 v[140:143], v36, %[rd], s44 offen offset:1024 sc1\n" \
    "v_exp_f32 v221, v221\n v_exp_f32 v222, v222\n v_exp_f32 v223, v223\n v_exp_f32 v224, v224\n v_exp_f32 v225, v225\n" \
    "v_smfmac_f32_32x32x32_bf16 v[8:23], v[240:243], v[216:223], v251\n" \
    "ds_read_b128 v[240:243], v249 offset:1024\n" \
    "v_exp_f32 v226, v226\n s_setprio 0\n v_exp_f32 v227, v227\n v_exp_f32 v228, v228\n v_exp_f32 v229, v229\n" \
    "s_setprio 1\n v_exp_f32 v230, v230\n v_exp_f32 v231, v231\n v_exp_f32 v200, v200\n v_exp_f32 v201, v201\n" \
    "v_smfmac_f32_32x32x32_bf16 v[8:23], v[244:247], v[224:231], v251\n" \
    "v_exp_f32 v202, v202\n v_exp_f32 v203, v203\n v_exp_f32 v204, v204\n v_mfma_f32_32x32x16_bf16 v[216:231], v[28:31], v[24:27], v[184:199]\n" \
    "ds_read_b128 v[244:247], v249 offset:1536\n" \
    "ds_read_b128 v[236:239], v248 offset:10240\n" \
    "buffer_load_dwordx4 v[144:147], v36, %[rd], s44 offen offset:2048 sc1\n" \
    "buffer_store_dwordx4 v[4:7], v36, %[rd], %[r0] offen sc1\n" \
    "v_exp_f32 v205, v205\n v_exp_f32 v206, v206\n v_exp_f32 v207, v207\n v_exp_f32 v208, v208\n v_exp_f32 v209, v209\n" \
    "s_waitcnt lgkmcnt(2)\n v_smfmac_f32_32x32x32_bf16 v[168:183], v[240:243], v[200:207], v251\n" \
    "v_exp_f32 v210, v210\n s_setprio 0\n v_exp_f32 v211, v211\n v_exp_f32 v212, v212\n v_exp_f32 v213, v213\n" \
    "s_setprio 1\n v_exp_f32 v214, v214\n v_exp_f32 v215, v215\n v_exp_f32 v216, v216\n v_exp_f32 v217, v217\n" \
    "s_waitcnt lgkmcnt(0)\n v_smfmac_f32_32x32x32_bf16 v[168:183], v[244:247], v[208:215], v251\n" \
    "v_exp_f32 v218, v218\n v_exp_f32 v219, v219\n v_exp_f32 v220, v220\n v_mfma_f32_32x32x16_bf16 v[200:215], v[236:239], v[232:235], v[184:199]\n" \
    "buffer_load_dwordx4 v[148:151], v36, %[rd], s44 offen offset:3072 sc1\n" \
    "s_mov_b32 m0, s43\n s_add_u32 s43, s43, 2048\n buffer_load_dwordx4 v36, %[rd], s42 offen sc1 lds\n" \
    "v_exp_f32 v221, v221\n v_exp_f32 v222, v222\n v_exp_f32 v223, v223\n v_exp_f32 v224, v224\n v_exp_f32 v225, v225\n" \
    "v_smfmac_f32_32x32x32_bf16 v[8:23], v[240:243], v[216:223], v251\n" \
    "ds_read_b128 v[240:243], v249 offset:2048\n" \
    "v_exp_f32 v226, v226\n s_setprio 0\n v_exp_f32 v227, v227\n v_exp_f32 v228, v228\n v_exp_f32 v229, v229\n" \
    "s_setprio 1\n v_exp_f32 v230, v230\n v_exp_f32 v231, v231\n v_exp_f32 v200, v200\n v_exp_f32 v201, v201\n" \
    "v_smfmac_f32_32x32x32_bf16 v[8:23], v[244:247], v[224:231], v251\n" \
    "v_exp_f32 v202, v202\n v_exp_f32 v203, v203\n v_exp_f32 v204, v204\n v_mfma_f32_32x32x16_bf16 v[216:231], v[236:239], v[24:27], v[184:199]\n" \
    "ds_read_b128 v[244:247], v249 offset:2560\n" \
    "ds_read_b128 v[28:31], v248 offset:11264\n" \
    "buffer_load_dwordx4 v[152:155], v36, %[rd], s45 offen offset:0 sc1\n" \
    "v_exp_f32 v205, v205\n v_exp_f32 v206, v206\n v_exp_f32 v207, v207\n v_exp_f32 v208, v208\n v_exp_f32 v209, v209\n" \
    "s_waitcnt lgkmcnt(2)\n v_smfmac_f32_32x32x32_bf16 v[168:183], v[240:243], v[200:207], v251\n" \
    "v_exp_f32 v210, v210\n s_setprio 0\n v_exp_f32 v211, v211\n v_exp_f32 v212, v212\n v_exp_f32 v213, v213\n" \
    "s_setprio 1\n v_exp_f32 v214, v214\n v_exp_f32 v215, v215\n v_exp_f32 v216, v216\n v_exp_f32 v217, v217\n" \
    "s_waitcnt lgkmcnt(0)\n v_smfmac_f32_32x32x32_bf16 v[168:183], v[244:247], v[208:215], v251\n" \
    "v_exp_f32 v218, v218\n v_exp_f32 v219, v219\n v_exp_f32 v220, v220\n v_mfma_f32_32x32x16_bf16 v[200:215], v[28:31], v[232:235], v[184:199]\n" \
    "buffer_load_dwordx4 v[156:159], v36, %[rd], s45 offen offset:1024 sc1\n" \
    "buffer_load_dwordx4 v36, %[rd], s42 offen offset:1024 sc1 lds\n" \
    "s_add_u32 s42, s42, 2048\n v_exp_f32 v221, v221\n v_exp_f32 v222, v222\n v_exp_f32 v223, v223\n v_exp_f32 v224, v224\n" \
    "v_exp_f32 v225, v225\n v_smfmac_f32_32x32x32_bf16 v[8:23], v[240:243], v[216:223], v251\n" \
    "ds_read_b128 v[240:243], v249 offset:3072\n" \
    "v_exp_f32 v226, v226\n s_setprio 0\n v_exp_f32 v227, v227\n v_exp_f32 v228, v228\n v_exp_f32 v229, v229\n" \
    "s_setprio 1\n v_exp_f32 v230, v230\n v_exp_f32 v231, v231\n v_exp_f32 v200, v200\n v_exp_f32 v201, v201\n" \
    "v_smfmac_f32_32x32x32_bf16 v[8:23], v[244:247], v[224:231], v251\n" \
    "v_exp_f32 v202, v202\n v_exp_f32 v203, v203\n v_exp_f32 v204, v204\n v_mfma_f32_32x32x16_bf16 v[216:231], v[28:31], v[24:27], v[184:199]\n" \
    "ds_read_b128 v[244:247], v249 offset:3584\n" \
    "s_waitcnt vmcnt(9)\n s_barrier\n ds_read_b128 v[236:239], v248 offset:0\n" \
    "buffer_load_dwordx4 v[160:163], v36, %[rd], s45 offen offset:2048 sc1\n" \
    "v_exp_f32 v205, v205\n v_exp_f32 v206, v206\n v_exp_f32 v207, v207\n v_exp_f32 v208, v208\n v_exp_f32 v209, v209\n" \
    "s_waitcnt lgkmcnt(2)\n v_smfmac_f32_32x32x32_bf16 v[168:183], v[240:243], v[200:207], v251\n" \
    "v_exp_f32 v210, v210\n s_setprio 0\n v_exp_f32 v211, v211\n v_exp_f32 v212, v212\n v_exp_f32 v213, v213\n" \
    "s_setprio 1\n v_exp_f32 v214, v214\n v_exp_f32 v215, v215\n v_exp_f32 v216, v216\n v_exp_f32 v217, v217\n" \
    "s_waitcnt lgkmcnt(0)\n v_smfmac_f32_32x32x32_bf16 v[168:183], v[244:247], v[208:215], v251\n" \
    "v_exp_f32 v218, v218\n v_exp_f32 v219, v219\n v_exp_f32 v220, v220\n v_mfma_f32_32x32x16_bf16 v[200:215], v[236:239], v[232:235], v[184:199]\n" \
    "buffer_load_dwordx4 v[164:167], v36, %[rd], s45 offen offset:3072 sc1\n" \
    "s_add_u32 s44, s44, 8192\n s_add_u32 s45, s45, 8192\n s_add_u32 m0, s41, 4096\n s_add_u32 s40, s47, s46\n buffer_load_dwordx4 v36, %[rd], s40 offen sc1 lds\n" \
    "buffer_load_dwordx4 v36, %[rd], s40 offen offset:1024 sc1 lds\n" \
    "s_add_u32 s46, s46, 8192\n s_and_b32 s46, s46, 0xffff\n v_exp_f32 v221, v221\n v_exp_f32 v222, v222\n v_exp_f32 v223, v223\n" \
    "v_exp_f32 v224, v224\n v_exp_f32 v225, v225\n v_smfmac_f32_32x32x32_bf16 v[8:23], v[240:243], v[216:223], v251\n" \
    "v_add_u32 v249, v249, v252\n ds_read_b128 v[240:243], v249\n" \
    "v_exp_f32 v226, v226\n s_setprio 0\n v_exp_f32 v227, v227\n v_exp_f32 v228, v228\n v_exp_f32 v229, v229\n" \
    "s_setprio 1\n v_exp_f32 v230, v230\n v_exp_f32 v231, v231\n" \
    ""
#define MQA1_6 \
    "v_exp_f32 v200, v200\n v_exp_f32 v201, v201\n v_smfmac_f32_32x32x32_bf16 v[8:23], v[244:247], v[224:231], v251\n" \
    "v_exp_f32 v202, v202\n v_exp_f32 v203, v203\n v_exp_f32 v204, v204\n v_mfma_f32_32x32x16_bf16 v[216:231], v[236:239], v[24:27], v[184:199]\n" \
    "ds_read_b128 v[244:247], v249 offset:512\n" \
    "ds_read_b128 v[28:31], v248 offset:1024\n" \
    "v_exp_f32 v205, v205\n v_exp_f32 v206, v206\n v_exp_f32 v207, v207\n v_exp_f32 v208, v208\n v_exp_f32 v209, v209\n" \
    "s_waitcnt lgkmcnt(2)\n v_smfmac_f32_32x32x32_bf16 v[168:183], v[240:243], v[200:207], v251\n" \
    "v_exp_f32 v210, v210\n s_setprio 0\n v_exp_f32 v211, v211\n v_exp_f32 v212, v212\n v_exp_f32 v213, v213\n" \
    "s_setprio 1\n v_exp_f32 v214, v214\n v_exp_f32 v215, v215\n v_exp_f32 v216, v216\n v_exp_f32 v217, v217\n" \
    "s_waitcnt lgkmcnt(0)\n v_smfmac_f32_32x32x32_bf16 v[168:183], v[244:247], v[208:215], v251\n" \
    "v_exp_f32 v218, v218\n v_exp_f32 v219, v219\n v_exp_f32 v220, v220\n v_mfma_f32_32x32x16_bf16 v[200:215], v[28:31], v[232:235], v[184:199]\n" \
    "v_exp_f32 v221, v221\n v_exp_f32 v222, v222\n v_exp_f32 v223, v223\n v_exp_f32 v224, v224\n v_exp_f32 v225, v225\n" \
    "v_smfmac_f32_32x32x32_bf16 v[8:23], v[240:243], v[216:223], v251\n" \
    "ds_read_b128 v[240:243], v249 offset:1024\n" \
    "v_exp_f32 v226, v226\n s_setprio 0\n v_exp_f32 v227, v227\n v_exp_f32 v228, v228\n v_exp_f32 v229, v229\n" \
    "s_setprio 1\n v_exp_f32 v230, v230\n v_exp_f32 v231, v231\n v_exp_f32 v200, v200\n v_exp_f32 v201, v201\n" \
    "v_smfmac_f32_32x32x32_bf16 v[8:23], v[244:247], v[224:231], v251\n" \
    "v_exp_f32 v202, v202\n v_exp_f32 v203, v203\n v_exp_f32 v204, v204\n v_mfma_f32_32x32x16_bf16 v[216:231], v[28:31], v[24:27], v[184:199]\n" \
    "ds_read_b128 v[244:247], v249 offset:1536\n" \
    "ds_read_b128 v[236:239], v248 offset:2048\n" \
    "buffer_store_dwordx4 v[4:7], v36, %[rd], %[r0] offen sc1\n" \
    "v_exp_f32 v205, v205\n v_exp_f32 v206, v206\n v_exp_f32 v207, v207\n v_exp_f32 v208, v208\n v_exp_f32 v209, v209\n" \
    "s_waitcnt lgkmcnt(2)\n v_smfmac_f32_32x32x32_bf16 v[168:183], v[240:243], v[200:207], v251\n" \
    "v_exp_f32 v210, v210\n s_setprio 0\n v_exp_f32 v211, v211\n v_exp_f32 v212, v212\n v_exp_f32 v213, v213\n" \
    "s_setprio 1\n v_exp_f32 v214, v214\n v_exp_f32 v215, v215\n v_exp_f32 v216, v216\n v_exp_f32 v217, v217\n" \
    "s_waitcnt lgkmcnt(0)\n v_smfmac_f32_32x32x32_bf16 v[168:183], v[244:247], v[208:215], v251\n" \
    "v_exp_f32 v218, v218\n v_exp_f32 v219, v219\n v_exp_f32 v220, v220\n v_mfma_f32_32x32x16_bf16 v[200:215], v[236:239], v[232:235], v[184:199]\n" \
    "v_exp_f32 v221, v221\n v_exp_f32 v222, v222\n v_exp_f32 v223, v223\n v_exp_f32 v224, v224\n v_exp_f32 v225, v225\n" \
    "v_smfmac_f32_32x32x32_bf16 v[8:23], v[240:243], v[216:223], v251\n" \
    "ds_read_b128 v[240:243], v249 offset:2048\n" \
    "v_exp_f32 v226, v226\n s_setprio 0\n v_exp_f32 v227, v227\n v_exp_f32 v228, v228\n v_exp_f32 v229, v229\n" \
    "s_setprio 1\n v_exp_f32 v230, v230\n v_exp_f32 v231, v231\n v_exp_f32 v200, v200\n v_exp_f32 v201, v201\n" \
    "v_smfmac_f32_32x32x32_bf16 v[8:23], v[244:247], v[224:231], v251\n" \
    "v_exp_f32 v202, v202\n v_exp_f32 v203, v203\n v_exp_f32 v204, v204\n v_mfma_f32_32x32x16_bf16 v[216:231], v[236:239], v[24:27], v[184:199]\n" \
    "ds_read_b128 v[244:247], v249 offset:2560\n" \
    "ds_read_b128 v[28:31], v248 offset:3072\n" \
    "v_exp_f32 v205, v205\n v_exp_f32 v206, v206\n v_exp_f32 v207, v207\n v_exp_f32 v208, v208\n v_exp_f32 v209, v209\n" \
    "s_waitcnt lgkmcnt(2)\n v_smfmac_f32_32x32x32_bf16 v[168:183], v[240:243], v[200:207], v251\n" \
    "v_exp_f32 v210, v210\n s_setprio 0\n v_exp_f32 v211, v211\n v_exp_f32 v212, v212\n v_exp_f32 v213, v213\n" \
    "s_setprio 1\n v_exp_f32 v214, v214\n v_exp_f32 v215, v215\n v_exp_f32 v216, v216\n v_exp_f32 v217, v217\n" \
    "s_waitcnt lgkmcnt(0)\n v_smfmac_f32_32x32x32_bf16 v[168:183], v[244:247], v[208:215], v251\n" \
    "v_exp_f32 v218, v218\n v_exp_f32 v219, v219\n v_exp_f32 v220, v220\n v_mfma_f32_32x32x16_bf16 v[200:215], v[28:31], v[232:235], v[184:199]\n" \
    "v_exp_f32 v221, v221\n v_exp_f32 v222, v222\n v_exp_f32 v223, v223\n v_exp_f32 v224, v224\n v_exp_f32 v225, v225\n" \
    "v_smfmac_f32_32x32x32_bf16 v[8:23], v[240:243], v[216:223], v251\n" \
    "ds_read_b128 v[240:243], v249 offset:3072\n" \
    "v_exp_f32 v226, v226\n s_setprio 0\n v_exp_f32 v227, v227\n v_exp_f32 v228, v228\n v_exp_f32 v229, v229\n" \
    "s_setprio 1\n v_exp_f32 v230, v230\n v_exp_f32 v231, v231\n v_exp_f32 v200, v200\n v_exp_f32 v201, v201\n" \
    "v_smfmac_f32_32x32x32_bf16 v[8:23], v[244:247], v[224:231], v251\n" \
    "v_exp_f32 v202, v202\n v_exp_f32 v203, v203\n v_exp_f32 v204, v204\n v_mfma_f32_32x32x16_bf16 v[216:231], v[28:31], v[24:27], v[184:199]\n" \
    "ds_read_b128 v[244:247], v249 offset:3584\n" \
    "s_waitcnt vmcnt(0)\n s_barrier\n ds_read_b128 v[236:239], v248 offset:4096\n" \
    "buffer_store_dword v32, v33, %[rd], %[to] offen sc1\n" \
    "v_exp_f32 v205, v205\n v_exp_f32 v206, v206\n v_exp_f32 v207, v207\n v_exp_f32 v208, v208\n v_exp_f32 v209, v209\n" \
    "s_waitcnt lgkmcnt(2)\n v_smfmac_f32_32x32x32_bf16 v[168:183], v[240:243], v[200:207], v251\n" \
    "v_exp_f32 v210, v210\n s_setprio 0\n v_exp_f32 v211, v211\n v_exp_f32 v212, v212\n v_exp_f32 v213, v213\n" \
    "s_setprio 1\n v_exp_f32 v214, v214\n v_exp_f32 v215, v215\n v_exp_f32 v216, v216\n v_exp_f32 v217, v217\n" \
    "s_waitcnt lgkmcnt(0)\n v_smfmac_f32_32x32x32_bf16 v[168:183], v[244:247], v[208:215], v251\n" \
    "v_exp_f32 v218, v218\n v_exp_f32 v219, v219\n v_exp_f32 v220, v220\n v_mfma_f32_32x32x16_bf16 v[200:215], v[236:239], v[232:235], v[184:199]\n" \
    "v_exp_f32 v221, v221\n v_exp_f32 v222, v222\n v_exp_f32 v223, v223\n v_exp_f32 v224, v224\n v_exp_f32 v225, v225\n" \
    "v_smfmac_f32_32x32x32_bf16 v[8:23], v[240:243], v[216:223], v251\n" \
    "v_add_u32 v249, v249, v250\n ds_read_b128 v[240:243], v249\n" \
    "v_exp_f32 v226, v226\n s_setprio 0\n v_exp_f32 v227, v227\n v_exp_f32 v228, v228\n v_exp_f32 v229, v229\n" \
    "s_setprio 1\n v_exp_f32 v230, v230\n v_exp_f32 v231, v231\n" \
    ""
#define MQA1_7 \
    "v_exp_f32 v200, v200\n v_exp_f32 v201, v201\n v_smfmac_f32_32x32x32_bf16 v[8:23], v[244:247], v[224:231], v251\n" \
    "v_exp_f32 v202, v202\n v_exp_f32 v203, v203\n v_exp_f32 v204, v204\n v_mfma_f32_32x32x16_bf16 v[216:231], v[236:239], v[24:27], v[184:199]\n" \
    "ds_read_b128 v[244:247], v249 offset:512\n" \
    "ds_read_b128 v[28:31], v248 offset:5120\n" \
    "v_exp_f32 v205, v205\n v_exp_f32 v206, v206\n v_exp_f32 v207, v207\n v_exp_f32 v208, v208\n v_exp_f32 v209, v209\n" \
    "s_waitcnt lgkmcnt(2)\n v_smfmac_f32_32x32x32_bf16 v[168:183], v[240:243], v[200:207], v251\n" \
    "v_exp_f32 v210, v210\n s_setprio 0\n v_exp_f32 v211, v211\n v_exp_f32 v212, v212\n v_exp_f32 v213, v213\n" \
    "s_setprio 1\n v_exp_f32 v214, v214\n v_exp_f32 v215, v215\n v_exp_f32 v216, v216\n v_exp_f32 v217, v217\n" \
    "s_waitcnt lgkmcnt(0)\n v_smfmac_f32_32x32x32_bf16 v[168:183], v[244:247], v[208:215], v251\n" \
    "v_exp_f32 v218, v218\n v_exp_f32 v219, v219\n v_exp_f32 v220, v220\n v_mfma_f32_32x32x16_bf16 v[200:215], v[28:31], v[232:235], v[184:199]\n" \
    "v_exp_f32 v221, v221\n v_exp_f32 v222, v222\n v_exp_f32 v223, v223\n v_exp_f32 v224, v224\n v_exp_f32 v225, v225\n" \
    "v_smfmac_f32_32x32x32_bf16 v[8:23], v[240:243], v[216:223], v251\n" \
    "ds_read_b128 v[240:243], v249 offset:1024\n" \
    "v_exp_f32 v226, v226\n s_setprio 0\n v_exp_f32 v227, v227\n v_exp_f32 v228, v228\n v_exp_f32 v229, v229\n" \
    "s_setprio 1\n v_exp_f32 v230, v230\n v_exp_f32 v231, v231\n v_exp_f32 v200, v200\n v_exp_f32 v201, v201\n" \
    "v_smfmac_f32_32x32x32_bf16 v[8:23], v[244:247], v[224:231], v251\n" \
    "v_exp_f32 v202, v202\n v_exp_f32 v203, v203\n v_exp_f32 v204, v204\n v_mfma_f32_32x32x16_bf16 v[216:231], v[28:31], v[24:27], v[184:199]\n" \
    "ds_read_b128 v[244:247], v249 offset:1536\n" \
    "ds_read_b128 v[236:239], v248 offset:6144\n" \
    "v_exp_f32 v205, v205\n v_exp_f32 v206, v206\n v_exp_f32 v207, v207\n v_exp_f32 v208, v208\n v_exp_f32 v209, v209\n" \
    "s_waitcnt lgkmcnt(2)\n v_smfmac_f32_32x32x32_bf16 v[168:183], v[240:243], v[200:207], v251\n" \
    "v_exp_f32 v210, v210\n s_setprio 0\n v_exp_f32 v211, v211\n v_exp_f32 v212, v212\n v_exp_f32 v213, v213\n" \
    "s_setprio 1\n v_exp_f32 v214, v214\n v_exp_f32 v215, v215\n v_exp_f32 v216, v216\n v_exp_f32 v217, v217\n" \
    "s_waitcnt lgkmcnt(0)\n v_smfmac_f32_32x32x32_bf16 v[168:183], v[244:247], v[208:215], v251\n" \
    "v_exp_f32 v218, v218\n v_exp_f32 v219, v219\n v_exp_f32 v220, v220\n v_mfma_f32_32x32x16_bf16 v[200:215], v[236:239], v[232:235], v[184:199]\n" \
    "v_exp_f32 v221, v221\n v_exp_f32 v222, v222\n v_exp_f32 v223, v223\n v_exp_f32 v224, v224\n v_exp_f32 v225, v225\n" \
    "v_smfmac_f32_32x32x32_bf16 v[8:23], v[240:243], v[216:223], v251\n" \
    "ds_read_b128 v[240:243], v249 offset:2048\n" \
    "v_exp_f32 v226, v226\n s_setprio 0\n v_exp_f32 v227, v227\n v_exp_f32 v228, v228\n v_exp_f32 v229, v229\n" \
    "s_setprio 1\n v_exp_f32 v230, v230\n v_exp_f32 v231, v231\n v_exp_f32 v200, v200\n v_exp_f32 v201, v201\n" \
    "v_smfmac_f32_32x32x32_bf16 v[8:23], v[244:247], v[224:231], v251\n" \
    "v_exp_f32 v202, v202\n v_exp_f32 v203, v203\n v_exp_f32 v204, v204\n v_mfma_f32_32x32x16_bf16 v[216:231], v[236:239], v[24:27], v[184:199]\n" \
    "ds_read_b128 v[244:247], v249 offset:2560\n" \
    "ds_read_b128 v[28:31], v248 offset:7168\n" \
    "v_exp_f32 v205, v205\n v_exp_f32 v206, v206\n v_exp_f32 v207, v207\n v_exp_f32 v208, v208\n v_exp_f32 v209, v209\n" \
    "s_waitcnt lgkmcnt(2)\n v_smfmac_f32_32x32x32_bf16 v[168:183], v[240:243], v[200:207], v251\n" \
    "v_exp_f32 v210, v210\n s_setprio 0\n v_exp_f32 v211, v211\n v_exp_f32 v212, v212\n v_exp_f32 v213, v213\n" \
    "s_setprio 1\n v_exp_f32 v214, v214\n v_exp_f32 v215, v215\n v_exp_f32 v216, v216\n v_exp_f32 v217, v217\n" \
    "s_waitcnt lgkmcnt(0)\n v_smfmac_f32_32x32x32_bf16 v[168:183], v[244:247], v[208:215], v251\n" \
    "v_exp_f32 v218, v218\n v_exp_f32 v219, v219\n v_exp_f32 v220, v220\n v_mfma_f32_32x32x16_bf16 v[200:215], v[28:31], v[232:235], v[184:199]\n" \
    "buffer_load_dword v38, v34, %[rd], %[fo] offen sc1\n" \
    "v_exp_f32 v221, v221\n v_exp_f32 v222, v222\n v_exp_f32 v223, v223\n v_exp_f32 v224, v224\n v_exp_f32 v225, v225\n" \
    "v_smfmac_f32_32x32x32_bf16 v[8:23], v[240:243], v[216:223], v251\n" \
    "ds_read_b128 v[240:243], v249 offset:3072\n" \
    "v_exp_f32 v226, v226\n s_setprio 0\n v_exp_f32 v227, v227\n v_exp_f32 v228, v228\n v_exp_f32 v229, v229\n" \
    "s_setprio 1\n v_exp_f32 v230, v230\n v_exp_f32 v231, v231\n v_exp_f32 v200, v200\n v_exp_f32 v201, v201\n" \
    "v_smfmac_f32_32x32x32_bf16 v[8:23], v[244:247], v[224:231], v251\n" \
    "v_exp_f32 v202, v202\n v_exp_f32 v203, v203\n v_exp_f32 v204, v204\n v_mfma_f32_32x32x16_bf16 v[216:231], v[28:31], v[24:27], v[184:199]\n" \
    "ds_read_b128 v[244:247], v249 offset:3584\n" \
    "ds_read_b128 v[236:239], v248 offset:8192\n" \
    "v_exp_f32 v205, v205\n v_exp_f32 v206, v206\n v_exp_f32 v207, v207\n v_exp_f32 v208, v208\n v_exp_f32 v209, v209\n" \
    "s_waitcnt lgkmcnt(2)\n v_smfmac_f32_32x32x32_bf16 v[168:183], v[240:243], v[200:207], v251\n" \
    "v_exp_f32 v210, v210\n s_setprio 0\n v_exp_f32 v211, v211\n v_exp_f32 v212, v212\n v_exp_f32 v213, v213\n" \
    "s_setprio 1\n v_exp_f32 v214, v214\n v_exp_f32 v215, v215\n v_exp_f32 v216, v216\n v_exp_f32 v217, v217\n" \
    "s_waitcnt lgkmcnt(0)\n v_smfmac_f32_32x32x32_bf16 v[168:183], v[244:247], v[208:215], v251\n" \
    "v_exp_f32 v218, v218\n v_exp_f32 v219, v219\n v_exp_f32 v220, v220\n v_mfma_f32_32x32x16_bf16 v[200:215], v[236:239], v[232:235], v[184:199]\n" \
    "v_exp_f32 v221, v221\n v_exp_f32 v222, v222\n v_exp_f32 v223, v223\n v_exp_f32 v224, v224\n v_exp_f32 v225, v225\n" \
    "v_smfmac_f32_32x32x32_bf16 v[8:23], v[240:243], v[216:223], v251\n" \
    "v_add_u32 v249, v249, v250\n ds_read_b128 v[240:243], v249\n" \
    "v_exp_f32 v226, v226\n s_setprio 0\n v_exp_f32 v227, v227\n v_exp_f32 v228, v228\n v_exp_f32 v229, v229\n" \
    "s_setprio 1\n v_exp_f32 v230, v230\n v_exp_f32 v231, v231\n" \
    ""
#define WIN0 \
    "ds_read_b128 v[236:239], v248 offset:57344\n" \
    "ds_read_b128 v[240:243], v249\n" \
    "s_waitcnt lgkmcnt(1)\n v_mfma_f32_32x32x16_bf16 v[216:231], v[236:239], v[232:235], v[184:199]\n" \
    "ds_read_b128 v[236:239], v248 offset:49152\n" \
    "s_waitcnt vmcnt(8)\n s_movk_i32 s46, 0x2000\n 21:\n ds_write_b128 %[cv], v[120:123] offset:32768\n ds_write_b128 %[cv], v[124:127] offset:33792\n" \
    "ds_write_b128 %[cv], v[128:131] offset:34816\n ds_write_b128 %[cv], v[132:135] offset:35840\n v_max3_u32 v32, v120, v121, v122\n v_max3_u32 v33, v123, v124, v125\n v_max3_u32 v32, v32, v126, v127\n" \
    "v_max3_u32 v33, v33, v128, v129\n v_max3_u32 v32, v32, v130, v131\n v_max3_u32 v33, v33, v132, v133\n v_max3_u32 v32, v32, v134, v135\n v_max_u32 v32, v32, v33\n" \
    "v_cmp_eq_u32 vcc, -1, v32\n s_cbranch_vccz 31f\n s_sub_u32 s46, s46, 1\n s_cbranch_scc1 31f\n s_add_u32 s45, s45, 1\n" \
    "buffer_load_dwordx4 v[120:123], %[vo], %[rd], s41 offen offset:0 sc1\n" \
    "buffer_load_dwordx4 v[124:127], %[vo], %[rd], s41 offen offset:1024 sc1\n" \
    "buffer_load_dwordx4 v[128:131], %[vo], %[rd], s41 offen offset:2048 sc1\n" \
    "buffer_load_dwordx4 v[132:135], %[vo], %[rd], s41 offen offset:3072 sc1\n" \
    "s_waitcnt vmcnt(0)\n s_branch 21b\n 31:\n s_nop 7\n s_nop 2\n" \
    "v_exp_f32 v216, v216\n v_exp_f32 v217, v217\n s_cmp_lg_u32 s47, 2\n s_cbranch_scc1 2f\n v_mov_b32 v184, %[big]\n" \
    "v_mov_b32 v185, %[big]\n v_mov_b32 v186, %[big]\n v_mov_b32 v187, %[big]\n v_mov_b32 v188, %[big]\n v_mov_b32 v189, %[big]\n" \
    "v_mov_b32 v190, %[big]\n v_mov_b32 v191, %[big]\n v_mov_b32 v192, %[big]\n v_mov_b32 v193, %[big]\n v_mov_b32 v194, %[big]\n" \
    "v_mov_b32 v195, %[big]\n v_mov_b32 v196, %[big]\n v_mov_b32 v197, %[big]\n v_mov_b32 v198, %[big]\n v_mov_b32 v199, %[big]\n" \
    "2:\n v_exp_f32 v218, v218\n v_exp_f32 v219, v219\n v_exp_f32 v220, v220\n s_waitcnt lgkmcnt(0)\n" \
    "v_mfma_f32_32x32x16_bf16 v[200:215], v[236:239], v[232:235], v[184:199]\n" \
    "s_barrier\n ds_read_b128 v[236:239], v248 offset:40960\n" \
    "ds_read_b128 v[244:247], v249 offset:512\n" \
    "buffer_load_dwordx4 v[72:75], %[vo], %[rd], s44 offen offset:0 sc1\n" \
    "buffer_load_dwordx4 v[76:79], %[vo], %[rd], s44 offen offset:1024 sc1\n" \
    "v_exp_f32 v221, v221\n v_exp_f32 v222, v222\n v_exp_f32 v223, v223\n v_exp_f32 v224, v224\n v_exp_f32 v225, v225\n" \
    "s_waitcnt lgkmcnt(2)\n v_smfmac_f32_32x32x32_bf16 v[168:183], v[240:243], v[216:223], v251\n" \
    "v_add_u32 v249, v249, v250\n ds_read_b128 v[240:243], v249\n" \
    "buffer_load_dwordx4 v[80:83], %[vo], %[rd], s44 offen offset:2048 sc1\n" \
    "buffer_load_dwordx4 v[84:87], %[vo], %[rd], s44 offen offset:3072 sc1\n" \
    "s_add_u32 s44, s44, 4096\n v_exp_f32 v226, v226\n v_exp_f32 v227, v227\n v_exp_f32 v228, v228\n v_exp_f32 v229, v229\n" \
    "v_exp_f32 v230, v230\n v_exp_f32 v231, v231\n s_waitcnt vmcnt(8)\n s_movk_i32 s46, 0x2000\n 22:\n" \
    "ds_write_b128 %[cv], v[136:139] offset:16384\n ds_write_b128 %[cv], v[140:143] offset:17408\n ds_write_b128 %[cv], v[144:147] offset:18432\n ds_write_b128 %[cv], v[148:151] offset:19456\n v_max3_u32 v32, v136, v137, v138\n" \
    "v_max3_u32 v33, v139, v140, v141\n v_max3_u32 v32, v32, v142, v143\n v_max3_u32 v33, v33, v144, v145\n v_max3_u32 v32, v32, v146, v147\n v_max3_u32 v33, v33, v148, v149\n" \
    "v_max3_u32 v32, v32, v150, v151\n v_max_u32 v32, v32, v33\n v_cmp_eq_u32 vcc, -1, v32\n s_cbranch_vccz 32f\n s_sub_u32 s46, s46, 1\n" \
    "s_cbranch_scc1 32f\n s_add_u32 s45, s45, 1\n buffer_load_dwordx4 v[136:139], %[vo], %[rd], s42 offen offset:0 sc1\n" \
    "buffer_load_dwordx4 v[140:143], %[vo], %[rd], s42 offen offset:1024 sc1\n" \
    "buffer_load_dwordx4 v[144:147], %[vo], %[rd], s42 offen offset:2048 sc1\n" \
    "buffer_load_dwordx4 v[148:151], %[vo], %[rd], s42 offen offset:3072 sc1\n" \
    "s_waitcnt vmcnt(0)\n s_branch 22b\n 32:\n v_exp_f32 v200, v200\n v_exp_f32 v201, v201\n" \
    "s_waitcnt lgkmcnt(1)\n v_smfmac_f32_32x32x32_bf16 v[168:183], v[244:247], v[224:231], v251\n" \
    "s_cmp_lg_u32 s47, 3\n s_cbranch_scc1 3f\n v_mov_b32 v184, %[big]\n v_mov_b32 v185, %[big]\n v_mov_b32 v186, %[big]\n" \
    "v_mov_b32 v187, %[big]\n v_mov_b32 v188, %[big]\n v_mov_b32 v189, %[big]\n v_mov_b32 v190, %[big]\n v_mov_b32 v191, %[big]\n" \
    "v_mov_b32 v192, %[big]\n v_mov_b32 v193, %[big]\n v_mov_b32 v194, %[big]\n v_mov_b32 v195, %[big]\n v_mov_b32 v196, %[big]\n" \
    "v_mov_b32 v197, %[big]\n v_mov_b32 v198, %[big]\n v_mov_b32 v199, %[big]\n 3:\n v_exp_f32 v202, v202\n" \
    "v_exp_f32 v203, v203\n v_exp_f32 v204, v204\n v_mfma_f32_32x32x16_bf16 v[216:231], v[236:239], v[232:235], v[184:199]\n" \
    "ds_read_b128 v[236:239], v248 offset:32768\n" \
    "ds_read_b128 v[244:247], v249 offset:512\n" \
    "buffer_load_dwordx4 v[88:91], %[vo], %[rd], s44 offen offset:0 sc1\n" \
    "buffer_load_dwordx4 v[92:95], %[vo], %[rd], s44 offen offset:1024 sc1\n" \
    "v_exp_f32 v205, v205\n v_exp_f32 v206, v206\n v_exp_f32 v207, v207\n v_exp_f32 v208, v208\n v_exp_f32 v209, v209\n" \
    "s_waitcnt lgkmcnt(2)\n v_smfmac_f32_32x32x32_bf16 v[168:183], v[240:243], v[200:207], v251\n" \
    "v_add_u32 v249, v249, v250\n ds_read_b128 v[240:243], v249\n" \
    "buffer_load_dwordx4 v[96:99], %[vo], %[rd], s44 offen offset:2048 sc1\n" \
    "buffer_load_dwordx4 v[100:103], %[vo], %[rd], s44 offen offset:3072 sc1\n" \
    "s_add_u32 s44, s44, 4096\n v_exp_f32 v210, v210\n v_exp_f32 v211, v211\n v_exp_f32 v212, v212\n v_exp_f32 v213, v213\n" \
    "v_exp_f32 v214, v214\n v_exp_f32 v215, v215\n v_exp_f32 v216, v216\n v_exp_f32 v217, v217\n s_waitcnt lgkmcnt(1)\n" \
    "v_smfmac_f32_32x32x32_bf16 v[168:183], v[244:247], v[208:215], v251\n" \
    "s_cmp_lg_u32 s47, 4\n s_cbranch_scc1 4f\n v_mov_b32 v184, %[big]\n v_mov_b32 v185, %[big]\n v_mov_b32 v186, %[big]\n" \
    "v_mov_b32 v187, %[big]\n v_mov_b32 v188, %[big]\n v_mov_b32 v189, %[big]\n v_mov_b32 v190, %[big]\n v_mov_b32 v191, %[big]\n" \
    "v_mov_b32 v192, %[big]\n v_mov_b32 v193, %[big]\n v_mov_b32 v194, %[big]\n v_mov_b32 v195, %[big]\n v_mov_b32 v196, %[big]\n" \
    "v_mov_b32 v197, %[big]\n v_mov_b32 v198, %[big]\n v_mov_b32 v199, %[big]\n 4:\n v_exp_f32 v218, v218\n" \
    "v_exp_f32 v219, v219\n v_exp_f32 v220, v220\n v_mfma_f32_32x32x16_bf16 v[200:215], v[236:239], v[232:235], v[184:199]\n" \
    "s_barrier\n ds_read_b128 v[236:239], v248 offset:24576\n" \
    "ds_read_b128 v[244:247], v249 offset:512\n" \
    "buffer_load_dwordx4 v[104:107], %[vo], %[rd], s44 offen offset:0 sc1\n" \
    "buffer_load_dwordx4 v[108:111], %[vo], %[rd], s44 offen offset:1024 sc1\n" \
    "v_exp_f32 v221, v221\n v_exp_f32 v222, v222\n v_exp_f32 v223, v223\n v_exp_f32 v224, v224\n v_exp_f32 v225, v225\n" \
    "s_waitcnt lgkmcnt(2)\n v_smfmac_f32_32x32x32_bf16 v[168:183], v[240:243], v[216:223], v251\n" \
    "v_add_u32 v249, v249, v250\n ds_read_b128 v[240:243], v249\n" \
    "buffer_load_dwordx4 v[112:115], %[vo], %[rd], s44 offen offset:2048 sc1\n" \
    "buffer_load_dwordx4 v[116:119], %[vo], %[rd], s44 offen offset:3072 sc1\n" \
    "s_add_u32 s44, s44, 4096\n v_exp_f32 v226, v226\n v_exp_f32 v227, v227\n v_exp_f32 v228, v228\n v_exp_f32 v229, v229\n" \
    "v_exp_f32 v230, v230\n v_exp_f32 v231, v231\n s_waitcnt vmcnt(12)\n s_movk_i32 s46, 0x2000\n 23:\n" \
    "ds_write_b128 %[cv], v[152:155] offset:0\n ds_write_b128 %[cv], v[156:159] offset:1024\n ds_write_b128 %[cv], v[160:163] offset:2048\n ds_write_b128 %[cv], v[164:167] offset:3072\n v_max3_u32 v32, v152, v153, v154\n" \
    "v_max3_u32 v33, v155, v156, v157\n v_max3_u32 v32, v32, v158, v159\n v_max3_u32 v33, v33, v160, v161\n v_max3_u32 v32, v32, v162, v163\n v_max3_u32 v33, v33, v164, v165\n" \
    "v_max3_u32 v32, v32, v166, v167\n v_max_u32 v32, v32, v33\n v_cmp_eq_u32 vcc, -1, v32\n s_cbranch_vccz 33f\n s_sub_u32 s46, s46, 1\n" \
    "s_cbranch_scc1 33f\n s_add_u32 s45, s45, 1\n buffer_load_dwordx4 v[152:155], %[vo], %[rd], s43 offen offset:0 sc1\n" \
    "buffer_load_dwordx4 v[156:159], %[vo], %[rd], s43 offen offset:1024 sc1\n" \
    "buffer_load_dwordx4 v[160:163], %[vo], %[rd], s43 offen offset:2048 sc1\n" \
    "buffer_load_dwordx4 v[164:167], %[vo], %[rd], s43 offen offset:3072 sc1\n" \
    "s_waitcnt vmcnt(0)\n s_branch 23b\n 33:\n v_exp_f32 v200, v200\n v_exp_f32 v201, v201\n" \
    "s_waitcnt lgkmcnt(1)\n v_smfmac_f32_32x32x32_bf16 v[168:183], v[244:247], v[224:231], v251\n" \
    "s_cmp_lg_u32 s47, 5\n s_cbranch_scc1 5f\n v_mov_b32 v184, %[big]\n v_mov_b32 v185, %[big]\n v_mov_b32 v186, %[big]\n" \
    "v_mov_b32 v187, %[big]\n v_mov_b32 v188, %[big]\n v_mov_b32 v189, %[big]\n v_mov_b32 v190, %[big]\n v_mov_b32 v191, %[big]\n" \
    "v_mov_b32 v192, %[big]\n v_mov_b32 v193, %[big]\n v_mov_b32 v194, %[big]\n v_mov_b32 v195, %[big]\n v_mov_b32 v196, %[big]\n" \
    "v_mov_b32 v197, %[big]\n v_mov_b32 v198, %[big]\n v_mov_b32 v199, %[big]\n 5:\n v_exp_f32 v202, v202\n" \
    "v_exp_f32 v203, v203\n v_exp_f32 v204, v204\n v_mfma_f32_32x32x16_bf16 v[216:231], v[236:239], v[232:235], v[184:199]\n" \
    "ds_read_b128 v[236:239], v248 offset:16384\n" \
    "ds_read_b128 v[244:247], v249 offset:512\n" \
    "buffer_load_dwordx4 v[120:123], %[vo], %[rd], s44 offen offset:0 sc1\n" \
    "buffer_load_dwordx4 v[124:127], %[vo], %[rd], s44 offen offset:1024 sc1\n" \
    "v_exp_f32 v205, v205\n v_exp_f32 v206, v206\n v_exp_f32 v207, v207\n v_exp_f32 v208, v208\n v_exp_f32 v209, v209\n" \
    "s_waitcnt lgkmcnt(2)\n v_smfmac_f32_32x32x32_bf16 v[168:183], v[240:243], v[200:207], v251\n" \
    "v_add_u32 v249, v249, v250\n ds_read_b128 v[240:243], v249\n" \
    "buffer_load_dwordx4 v[128:131], %[vo], %[rd], s44 offen offset:2048 sc1\n" \
    "buffer_load_dwordx4 v[132:135], %[vo], %[rd], s44 offen offset:3072 sc1\n" \
    "s_add_u32 s44, s44, 4096\n v_exp_f32 v210, v210\n v_exp_f32 v211, v211\n v_exp_f32 v212, v212\n v_exp_f32 v213, v213\n" \
    "v_exp_f32 v214, v214\n v_exp_f32 v215, v215\n v_exp_f32 v216, v216\n v_exp_f32 v217, v217\n s_waitcnt lgkmcnt(1)\n" \
    "v_smfmac_f32_32x32x32_bf16 v[168:183], v[244:247], v[208:215], v251\n" \
    "s_cmp_lg_u32 s47, 6\n s_cbranch_scc1 6f\n v_mov_b32 v184, %[big]\n v_mov_b32 v185, %[big]\n v_mov_b32 v186, %[big]\n" \
    "v_mov_b32 v187, %[big]\n v_mov_b32 v188, %[big]\n v_mov_b32 v189, %[big]\n v_mov_b32 v190, %[big]\n v_mov_b32 v191, %[big]\n" \
    "v_mov_b32 v192, %[big]\n v_mov_b32 v193, %[big]\n v_mov_b32 v194, %[big]\n v_mov_b32 v195, %[big]\n v_mov_b32 v196, %[big]\n" \
    "v_mov_b32 v197, %[big]\n v_mov_b32 v198, %[big]\n v_mov_b32 v199, %[big]\n 6:\n v_exp_f32 v218, v218\n" \
    "v_exp_f32 v219, v219\n v_exp_f32 v220, v220\n v_mfma_f32_32x32x16_bf16 v[200:215], v[236:239], v[232:235], v[184:199]\n" \
    "s_barrier\n ds_read_b128 v[236:239], v248 offset:8192\n" \
    "ds_read_b128 v[244:247], v249 offset:512\n" \
    "buffer_load_dwordx4 v[136:139], %[vo], %[rd], s44 offen offset:0 sc1\n" \
    "buffer_load_dwordx4 v[140:143], %[vo], %[rd], s44 offen offset:1024 sc1\n" \
    "v_exp_f32 v221, v221\n v_exp_f32 v222, v222\n v_exp_f32 v223, v223\n v_exp_f32 v224, v224\n v_exp_f32 v225, v225\n" \
    "s_waitcnt lgkmcnt(2)\n v_smfmac_f32_32x32x32_bf16 v[168:183], v[240:243], v[216:223], v251\n" \
    "v_add_u32 v249, v249, v250\n ds_read_b128 v[240:243], v249\n" \
    "buffer_load_dwordx4 v[144:147], %[vo], %[rd], s44 offen offset:2048 sc1\n" \
    "buffer_load_dwordx4 v[148:151], %[vo], %[rd], s44 offen offset:3072 sc1\n" \
    "s_add_u32 s44, s44, 4096\n v_exp_f32 v226, v226\n v_exp_f32 v227, v227\n v_exp_f32 v228, v228\n v_exp_f32 v229, v229\n" \
    "v_exp_f32 v230, v230\n v_exp_f32 v231, v231\n v_exp_f32 v200, v200\n v_exp_f32 v201, v201\n s_waitcnt lgkmcnt(1)\n" \
    "v_smfmac_f32_32x32x32_bf16 v[168:183], v[244:247], v[224:231], v251\n" \
    "s_cmp_lg_u32 s47, 7\n s_cbranch_scc1 7f\n v_mov_b32 v184, %[big]\n v_mov_b32 v185, %[big]\n v_mov_b32 v186, %[big]\n" \
    "v_mov_b32 v187, %[big]\n v_mov_b32 v188, %[big]\n v_mov_b32 v189, %[big]\n v_mov_b32 v190, %[big]\n v_mov_b32 v191, %[big]\n" \
    "v_mov_b32 v192, %[big]\n v_mov_b32 v193, %[big]\n v_mov_b32 v194, %[big]\n v_mov_b32 v195, %[big]\n v_mov_b32 v196, %[big]\n" \
    "v_mov_b32 v197, %[big]\n v_mov_b32 v198, %[big]\n v_mov_b32 v199, %[big]\n 7:\n v_exp_f32 v202, v202\n" \
    "v_exp_f32 v203, v203\n v_exp_f32 v204, v204\n v_mfma_f32_32x32x16_bf16 v[216:231], v[236:239], v[232:235], v[184:199]\n" \
    "ds_read_b128 v[236:239], v248 offset:0\n" \
    "ds_read_b128 v[244:247], v249 offset:512\n" \
    "buffer_load_dwordx4 v[152:155], %[vo], %[rd], s44 offen offset:0 sc1\n" \
    "buffer_load_dwordx4 v[156:159], %[vo], %[rd], s44 offen offset:1024 sc1\n" \
    "v_exp_f32 v205, v205\n v_exp_f32 v206, v206\n v_exp_f32 v207, v207\n v_exp_f32 v208, v208\n v_exp_f32 v209, v209\n" \
    "s_waitcnt lgkmcnt(2)\n v_smfmac_f32_32x32x32_bf16 v[168:183], v[240:243], v[200:207], v251\n" \
    "v_add_u32 v249, v249, v250\n ds_read_b128 v[240:243], v249\n" \
    "buffer_load_dwordx4 v[160:163], %[vo], %[rd], s44 offen offset:2048 sc1\n" \
    "buffer_load_dwordx4 v[164:167], %[vo], %[rd], s44 offen offset:3072 sc1\n" \
    "s_add_u32 s44, s44, 4096\n s_mov_b32 vcc_lo, 0xffffffff\n s_mov_b32 vcc_hi, 0xffffff00\n v_cndmask_b32 v184, %[nf], %[big], vcc\n s_mov_b32 vcc_lo, 0xfffffffe\n" \
    "s_mov_b32 vcc_hi, 0xfffffe00\n v_cndmask_b32 v185, %[nf], %[big], vcc\n s_mov_b32 vcc_lo, 0xfffffffc\n s_mov_b32 vcc_hi, 0xfffffc00\n v_cndmask_b32 v186, %[nf], %[big], vcc\n" \
    "s_mov_b32 vcc_lo, 0xfffffff8\n s_mov_b32 vcc_hi, 0xfffff800\n v_cndmask_b32 v187, %[nf], %[big], vcc\n s_mov_b32 vcc_lo, 0xfffffff0\n s_mov_b32 vcc_hi, 0xfffff000\n" \
    "v_cndmask_b32 v188, %[nf], %[big], vcc\n s_mov_b32 vcc_lo, 0xffffffe0\n s_mov_b32 vcc_hi, 0xffffe000\n v_cndmask_b32 v189, %[nf], %[big], vcc\n s_mov_b32 vcc_lo, 0xffffffc0\n" \
    "s_mov_b32 vcc_hi, 0xffffc000\n v_cndmask_b32 v190, %[nf], %[big], vcc\n s_mov_b32 vcc_lo, 0xffffff80\n s_mov_b32 vcc_hi, 0xffff8000\n v_cndmask_b32 v191, %[nf], %[big], vcc\n" \
    "s_mov_b32 vcc_lo, 0xffff0000\n s_mov_b32 vcc_hi, 0xff000000\n v_cndmask_b32 v192, %[nf], %[big], vcc\n s_mov_b32 vcc_lo, 0xfffe0000\n s_mov_b32 vcc_hi, 0xfe000000\n" \
    "v_cndmask_b32 v193, %[nf], %[big], vcc\n s_mov_b32 vcc_lo, 0xfffc0000\n s_mov_b32 vcc_hi, 0xfc000000\n v_cndmask_b32 v194, %[nf], %[big], vcc\n s_mov_b32 vcc_lo, 0xfff80000\n" \
    "s_mov_b32 vcc_hi, 0xf8000000\n v_cndmask_b32 v195, %[nf], %[big], vcc\n s_mov_b32 vcc_lo, 0xfff00000\n s_mov_b32 vcc_hi, 0xf0000000\n v_cndmask_b32 v196, %[nf], %[big], vcc\n" \
    "s_mov_b32 vcc_lo, 0xffe00000\n s_mov_b32 vcc_hi, 0xe0000000\n v_cndmask_b32 v197, %[nf], %[big], vcc\n s_mov_b32 vcc_lo, 0xffc00000\n s_mov_b32 vcc_hi, 0xc0000000\n" \
    "v_cndmask_b32 v198, %[nf], %[big], vcc\n s_mov_b32 vcc_lo, 0xff800000\n s_mov_b32 vcc_hi, 0x80000000\n v_cndmask_b32 v199, %[nf], %[big], vcc\n v_exp_f32 v210, v210\n" \
    "v_exp_f32 v211, v211\n v_exp_f32 v212, v212\n v_exp_f32 v213, v213\n v_exp_f32 v214, v214\n v_exp_f32 v215, v215\n" \
    "v_exp_f32 v216, v216\n v_exp_f32 v217, v217\n s_waitcnt lgkmcnt(1)\n v_smfmac_f32_32x32x32_bf16 v[168:183], v[244:247], v[208:215], v251\n" \
    "v_exp_f32 v218, v218\n v_exp_f32 v219, v219\n v_exp_f32 v220, v220\n v_mfma_f32_32x32x16_bf16 v[200:215], v[236:239], v[232:235], v[184:199]\n" \
    "ds_read_b128 v[244:247], v249 offset:512\n" \
    "v_exp_f32 v221, v221\n v_exp_f32 v222, v222\n v_exp_f32 v223, v223\n v_exp_f32 v224, v224\n v_exp_f32 v225, v225\n" \
    "s_waitcnt lgkmcnt(1)\n v_smfmac_f32_32x32x32_bf16 v[168:183], v[240:243], v[216:223], v251\n" \
    "v_add_u32 v249, v249, v250\n ds_read_b128 v[240:243], v249\n" \
    "v_exp_f32 v226, v226\n v_exp_f32 v227, v227\n v_exp_f32 v228, v228\n v_exp_f32 v229, v229\n v_exp_f32 v230, v230\n" \
    "v_exp_f32 v231, v231\n v_exp_f32 v200, v200\n v_exp_f32 v201, v201\n s_waitcnt lgkmcnt(1)\n v_smfmac_f32_32x32x32_bf16 v[168:183], v[244:247], v[224:231], v251\n" \
    "v_exp_f32 v202, v202\n v_exp_f32 v203, v203\n v_exp_f32 v204, v204\n ds_read_b128 v[244:247], v249 offset:512\n" \
    "v_exp_f32 v205, v205\n v_exp_f32 v206, v206\n v_exp_f32 v207, v207\n v_exp_f32 v208, v208\n v_exp_f32 v209, v209\n" \
    "s_waitcnt lgkmcnt(1)\n v_smfmac_f32_32x32x32_bf16 v[168:183], v[240:243], v[200:207], v251\n" \
    "v_exp_f32 v210, v210\n v_exp_f32 v211, v211\n v_exp_f32 v212, v212\n v_exp_f32 v213, v213\n v_exp_f32 v214, v214\n" \
    "v_exp_f32 v215, v215\n s_waitcnt lgkmcnt(0)\n s_setprio 0\n v_smfmac_f32_32x32x32_bf16 v[168:183], v[244:247], v[208:215], v251\n" \
    "s_nop 7\n s_nop 7\n" \
    ""
#define WIN1 \
    "ds_read_b128 v[236:239], v248 offset:57344\n" \
    "ds_read_b128 v[240:243], v249\n" \
    "s_waitcnt lgkmcnt(1)\n v_mfma_f32_32x32x16_bf16 v[216:231], v[236:239], v[232:235], v[184:199]\n" \
    "ds_read_b128 v[236:239], v248 offset:49152\n" \
    "s_nop 7\n s_nop 2\n v_exp_f32 v216, v216\n v_exp_f32 v217, v217\n s_cmp_lg_u32 s47, 2\n" \
    "s_cbranch_scc1 2f\n v_mov_b32 v184, %[big]\n v_mov_b32 v185, %[big]\n v_mov_b32 v186, %[big]\n v_mov_b32 v187, %[big]\n" \
    "v_mov_b32 v188, %[big]\n v_mov_b32 v189, %[big]\n v_mov_b32 v190, %[big]\n v_mov_b32 v191, %[big]\n v_mov_b32 v192, %[big]\n" \
    "v_mov_b32 v193, %[big]\n v_mov_b32 v194, %[big]\n v_mov_b32 v195, %[big]\n v_mov_b32 v196, %[big]\n v_mov_b32 v197, %[big]\n" \
    "v_mov_b32 v198, %[big]\n v_mov_b32 v199, %[big]\n 2:\n v_exp_f32 v218, v218\n v_exp_f32 v219, v219\n" \
    "v_exp_f32 v220, v220\n s_waitcnt lgkmcnt(0)\n v_mfma_f32_32x32x16_bf16 v[200:215], v[236:239], v[232:235], v[184:199]\n" \
    "s_barrier\n ds_read_b128 v[236:239], v248 offset:40960\n" \
    "ds_read_b128 v[244:247], v249 offset:512\n" \
    "v_exp_f32 v221, v221\n v_exp_f32 v222, v222\n v_exp_f32 v223, v223\n v_exp_f32 v224, v224\n s_setprio 0\n" \
    "v_exp_f32 v225, v225\n s_waitcnt lgkmcnt(2)\n v_smfmac_f32_32x32x32_bf16 v[168:183], v[240:243], v[216:223], v251\n" \
    "v_add_u32 v249, v249, v250\n ds_read_b128 v[240:243], v249\n" \
    "v_exp_f32 v226, v226\n v_exp_f32 v227, v227\n v_exp_f32 v228, v228\n v_exp_f32 v229, v229\n s_setprio 1\n" \
    "v_exp_f32 v230, v230\n v_exp_f32 v231, v231\n v_exp_f32 v200, v200\n v_exp_f32 v201, v201\n s_waitcnt lgkmcnt(1)\n" \
    "v_smfmac_f32_32x32x32_bf16 v[168:183], v[244:247], v[224:231], v251\n" \
    "s_cmp_lg_u32 s47, 3\n s_cbranch_scc1 3f\n v_mov_b32 v184, %[big]\n v_mov_b32 v185, %[big]\n v_mov_b32 v186, %[big]\n" \
    "v_mov_b32 v187, %[big]\n v_mov_b32 v188, %[big]\n v_mov_b32 v189, %[big]\n v_mov_b32 v190, %[big]\n v_mov_b32 v191, %[big]\n" \
    "v_mov_b32 v192, %[big]\n v_mov_b32 v193, %[big]\n v_mov_b32 v194, %[big]\n v_mov_b32 v195, %[big]\n v_mov_b32 v196, %[big]\n" \
    "v_mov_b32 v197, %[big]\n v_mov_b32 v198, %[big]\n v_mov_b32 v199, %[big]\n 3:\n v_exp_f32 v202, v202\n" \
    "v_exp_f32 v203, v203\n v_exp_f32 v204, v204\n v_mfma_f32_32x32x16_bf16 v[216:231], v[236:239], v[232:235], v[184:199]\n" \
    "ds_read_b128 v[236:239], v248 offset:32768\n" \
    "ds_read_b128 v[244:247], v249 offset:512\n" \
    "v_exp_f32 v205, v205\n v_exp_f32 v206, v206\n v_exp_f32 v207, v207\n s_setprio 0\n v_exp_f32 v208, v208\n" \
    "v_exp_f32 v209, v209\n s_waitcnt lgkmcnt(2)\n v_smfmac_f32_32x32x32_bf16 v[168:183], v[240:243], v[200:207], v251\n" \
    "v_add_u32 v249, v249, v250\n ds_read_b128 v[240:243], v249\n" \
    "v_exp_f32 v210, v210\n v_exp_f32 v211, v211\n v_exp_f32 v212, v212\n s_setprio 1\n v_exp_f32 v213, v213\n" \
    "v_exp_f32 v214, v214\n v_exp_f32 v215, v215\n v_exp_f32 v216, v216\n v_exp_f32 v217, v217\n s_waitcnt lgkmcnt(1)\n" \
    "v_smfmac_f32_32x32x32_bf16 v[168:183], v[244:247], v[208:215], v251\n" \
    "s_cmp_lg_u32 s47, 4\n s_cbranch_scc1 4f\n v_mov_b32 v184, %[big]\n v_mov_b32 v185, %[big]\n v_mov_b32 v186, %[big]\n" \
    "v_mov_b32 v187, %[big]\n v_mov_b32 v188, %[big]\n v_mov_b32 v189, %[big]\n v_mov_b32 v190, %[big]\n v_mov_b32 v191, %[big]\n" \
    "v_mov_b32 v192, %[big]\n v_mov_b32 v193, %[big]\n v_mov_b32 v194, %[big]\n v_mov_b32 v195, %[big]\n v_mov_b32 v196, %[big]\n" \
    "v_mov_b32 v197, %[big]\n v_mov_b32 v198, %[big]\n v_mov_b32 v199, %[big]\n 4:\n v_exp_f32 v218, v218\n" \
    "v_exp_f32 v219, v219\n v_exp_f32 v220, v220\n v_mfma_f32_32x32x16_bf16 v[200:215], v[236:239], v[232:235], v[184:199]\n" \
    "s_barrier\n ds_read_b128 v[236:239], v248 offset:24576\n" \
    "ds_read_b128 v[244:247], v249 offset:512\n" \
    "v_exp_f32 v221, v221\n v_exp_f32 v222, v222\n v_exp_f32 v223, v223\n s_setprio 0\n v_exp_f32 v224, v224\n" \
    "v_exp_f32 v225, v225\n s_waitcnt lgkmcnt(2)\n v_smfmac_f32_32x32x32_bf16 v[168:183], v[240:243], v[216:223], v251\n" \
    "v_add_u32 v249, v249, v250\n ds_read_b128 v[240:243], v249\n" \
    "v_exp_f32 v226, v226\n v_exp_f32 v227, v227\n v_exp_f32 v228, v228\n s_setprio 1\n v_exp_f32 v229, v229\n" \
    "v_exp_f32 v230, v230\n v_exp_f32 v231, v231\n v_exp_f32 v200, v200\n v_exp_f32 v201, v201\n s_waitcnt lgkmcnt(1)\n" \
    "v_smfmac_f32_32x32x32_bf16 v[168:183], v[244:247], v[224:231], v251\n" \
    "s_cmp_lg_u32 s47, 5\n s_cbranch_scc1 5f\n v_mov_b32 v184, %[big]\n v_mov_b32 v185, %[big]\n v_mov_b32 v186, %[big]\n" \
    "v_mov_b32 v187, %[big]\n v_mov_b32 v188, %[big]\n v_mov_b32 v189, %[big]\n v_mov_b32 v190, %[big]\n v_mov_b32 v191, %[big]\n" \
    "v_mov_b32 v192, %[big]\n v_mov_b32 v193, %[big]\n v_mov_b32 v194, %[big]\n v_mov_b32 v195, %[big]\n v_mov_b32 v196, %[big]\n" \
    "v_mov_b32 v197, %[big]\n v_mov_b32 v198, %[big]\n v_mov_b32 v199, %[big]\n 5:\n v_exp_f32 v202, v202\n" \
    "v_exp_f32 v203, v203\n v_exp_f32 v204, v204\n v_mfma_f32_32x32x16_bf16 v[216:231], v[236:239], v[232:235], v[184:199]\n" \
    "ds_read_b128 v[236:239], v248 offset:16384\n" \
    "ds_read_b128 v[244:247], v249 offset:512\n" \
    "v_exp_f32 v205, v205\n v_exp_f32 v206, v206\n v_exp_f32 v207, v207\n s_setprio 0\n v_exp_f32 v208, v208\n" \
    "v_exp_f32 v209, v209\n s_waitcnt lgkmcnt(2)\n v_smfmac_f32_32x32x32_bf16 v[168:183], v[240:243], v[200:207], v251\n" \
    "v_add_u32 v249, v249, v250\n ds_read_b128 v[240:243], v249\n" \
    "v_exp_f32 v210, v210\n v_exp_f32 v211, v211\n v_exp_f32 v212, v212\n s_setprio 1\n v_exp_f32 v213, v213\n" \
    "v_exp_f32 v214, v214\n v_exp_f32 v215, v215\n v_exp_f32 v216, v216\n v_exp_f32 v217, v217\n s_waitcnt lgkmcnt(1)\n" \
    "v_smfmac_f32_32x32x32_bf16 v[168:183], v[244:247], v[208:215], v251\n" \
    "s_cmp_lg_u32 s47, 6\n s_cbranch_scc1 6f\n v_mov_b32 v184, %[big]\n v_mov_b32 v185, %[big]\n v_mov_b32 v186, %[big]\n" \
    "v_mov_b32 v187, %[big]\n v_mov_b32 v188, %[big]\n v_mov_b32 v189, %[big]\n v_mov_b32 v190, %[big]\n v_mov_b32 v191, %[big]\n" \
    "v_mov_b32 v192, %[big]\n v_mov_b32 v193, %[big]\n v_mov_b32 v194, %[big]\n v_mov_b32 v195, %[big]\n v_mov_b32 v196, %[big]\n" \
    "v_mov_b32 v197, %[big]\n v_mov_b32 v198, %[big]\n v_mov_b32 v199, %[big]\n 6:\n v_exp_f32 v218, v218\n" \
    "v_exp_f32 v219, v219\n v_exp_f32 v220, v220\n v_mfma_f32_32x32x16_bf16 v[200:215], v[236:239], v[232:235], v[184:199]\n" \
    "s_barrier\n ds_read_b128 v[236:239], v248 offset:8192\n" \
    "ds_read_b128 v[244:247], v249 offset:512\n" \
    "v_exp_f32 v221, v221\n v_exp_f32 v222, v222\n v_exp_f32 v223, v223\n s_setprio 0\n v_exp_f32 v224, v224\n" \
    "v_exp_f32 v225, v225\n s_waitcnt lgkmcnt(2)\n v_smfmac_f32_32x32x32_bf16 v[168:183], v[240:243], v[216:223], v251\n" \
    "v_add_u32 v249, v249, v250\n ds_read_b128 v[240:243], v249\n" \
    "v_exp_f32 v226, v226\n v_exp_f32 v227, v227\n v_exp_f32 v228, v228\n s_setprio 1\n v_exp_f32 v229, v229\n" \
    "v_exp_f32 v230, v230\n v_exp_f32 v231, v231\n v_exp_f32 v200, v200\n v_exp_f32 v201, v201\n s_waitcnt lgkmcnt(1)\n" \
    "v_smfmac_f32_32x32x32_bf16 v[168:183], v[244:247], v[224:231], v251\n" \
    "s_cmp_lg_u32 s47, 7\n s_cbranch_scc1 7f\n v_mov_b32 v184, %[big]\n v_mov_b32 v185, %[big]\n v_mov_b32 v186, %[big]\n" \
    "v_mov_b32 v187, %[big]\n v_mov_b32 v188, %[big]\n v_mov_b32 v189, %[big]\n v_mov_b32 v190, %[big]\n v_mov_b32 v191, %[big]\n" \
    "v_mov_b32 v192, %[big]\n v_mov_b32 v193, %[big]\n v_mov_b32 v194, %[big]\n v_mov_b32 v195, %[big]\n v_mov_b32 v196, %[big]\n" \
    "v_mov_b32 v197, %[big]\n v_mov_b32 v198, %[big]\n v_mov_b32 v199, %[big]\n 7:\n v_exp_f32 v202, v202\n" \
    "v_exp_f32 v203, v203\n v_exp_f32 v204, v204\n v_mfma_f32_32x32x16_bf16 v[216:231], v[236:239], v[232:235], v[184:199]\n" \
    "ds_read_b128 v[236:239], v248 offset:0\n" \
    "ds_read_b128 v[244:247], v249 offset:512\n" \
    "v_exp_f32 v205, v205\n v_exp_f32 v206, v206\n v_exp_f32 v207, v207\n s_setprio 0\n v_exp_f32 v208, v208\n" \
    "v_exp_f32 v209, v209\n s_waitcnt lgkmcnt(2)\n v_smfmac_f32_32x32x32_bf16 v[168:183], v[240:243], v[200:207], v251\n" \
    "v_add_u32 v249, v249, v250\n ds_read_b128 v[240:243], v249\n" \
    "s_mov_b32 vcc_lo, 0xffffffff\n s_mov_b32 vcc_hi, 0xffffff00\n v_cndmask_b32 v184, %[nf], %[big], vcc\n s_mov_b32 vcc_lo, 0xfffffffe\n s_mov_b32 vcc_hi, 0xfffffe00\n" \
    "v_cndmask_b32 v185, %[nf], %[big], vcc\n s_mov_b32 vcc_lo, 0xfffffffc\n s_mov_b32 vcc_hi, 0xfffffc00\n v_cndmask_b32 v186, %[nf], %[big], vcc\n s_mov_b32 vcc_lo, 0xfffffff8\n" \
    "s_mov_b32 vcc_hi, 0xfffff800\n v_cndmask_b32 v187, %[nf], %[big], vcc\n s_mov_b32 vcc_lo, 0xfffffff0\n s_mov_b32 vcc_hi, 0xfffff000\n v_cndmask_b32 v188, %[nf], %[big], vcc\n" \
    "s_mov_b32 vcc_lo, 0xffffffe0\n s_mov_b32 vcc_hi, 0xffffe000\n v_cndmask_b32 v189, %[nf], %[big], vcc\n s_mov_b32 vcc_lo, 0xffffffc0\n s_mov_b32 vcc_hi, 0xffffc000\n" \
    "v_cndmask_b32 v190, %[nf], %[big], vcc\n s_mov_b32 vcc_lo, 0xffffff80\n s_mov_b32 vcc_hi, 0xffff8000\n v_cndmask_b32 v191, %[nf], %[big], vcc\n s_mov_b32 vcc_lo, 0xffff0000\n" \
    "s_mov_b32 vcc_hi, 0xff000000\n v_cndmask_b32 v192, %[nf], %[big], vcc\n s_mov_b32 vcc_lo, 0xfffe0000\n s_mov_b32 vcc_hi, 0xfe000000\n v_cndmask_b32 v193, %[nf], %[big], vcc\n" \
    "s_mov_b32 vcc_lo, 0xfffc0000\n s_mov_b32 vcc_hi, 0xfc000000\n v_cndmask_b32 v194, %[nf], %[big], vcc\n s_mov_b32 vcc_lo, 0xfff80000\n s_mov_b32 vcc_hi, 0xf8000000\n" \
    "v_cndmask_b32 v195, %[nf], %[big], vcc\n s_mov_b32 vcc_lo, 0xfff00000\n s_mov_b32 vcc_hi, 0xf0000000\n v_cndmask_b32 v196, %[nf], %[big], vcc\n s_mov_b32 vcc_lo, 0xffe00000\n" \
    "s_mov_b32 vcc_hi, 0xe0000000\n v_cndmask_b32 v197, %[nf], %[big], vcc\n s_mov_b32 vcc_lo, 0xffc00000\n s_mov_b32 vcc_hi, 0xc0000000\n v_cndmask_b32 v198, %[nf], %[big], vcc\n" \
    "s_mov_b32 vcc_lo, 0xff800000\n s_mov_b32 vcc_hi, 0x80000000\n v_cndmask_b32 v199, %[nf], %[big], vcc\n v_exp_f32 v210, v210\n v_exp_f32 v211, v211\n" \
    "v_exp_f32 v212, v212\n s_setprio 1\n v_exp_f32 v213, v213\n v_exp_f32 v214, v214\n v_exp_f32 v215, v215\n" \
    "v_exp_f32 v216, v216\n v_exp_f32 v217, v217\n s_waitcnt lgkmcnt(1)\n v_smfmac_f32_32x32x32_bf16 v[168:183], v[244:247], v[208:215], v251\n" \
    "v_exp_f32 v218, v218\n v_exp_f32 v219, v219\n v_exp_f32 v220, v220\n v_mfma_f32_32x32x16_bf16 v[200:215], v[236:239], v[232:235], v[184:199]\n" \
    "ds_read_b128 v[244:247], v249 offset:512\n" \
    "v_exp_f32 v221, v221\n v_exp_f32 v222, v222\n v_exp_f32 v223, v223\n s_setprio 0\n v_exp_f32 v224, v224\n" \
    "v_exp_f32 v225, v225\n s_waitcnt lgkmcnt(1)\n v_smfmac_f32_32x32x32_bf16 v[168:183], v[240:243], v[216:223], v251\n" \
    "v_add_u32 v249, v249, v250\n ds_read_b128 v[240:243], v249\n" \
    "v_exp_f32 v226, v226\n v_exp_f32 v227, v227\n v_exp_f32 v228, v228\n s_setprio 1\n v_exp_f32 v229, v229\n" \
    "v_exp_f32 v230, v230\n v_exp_f32 v231, v231\n v_exp_f32 v200, v200\n v_exp_f32 v201, v201\n s_waitcnt lgkmcnt(1)\n" \
    "v_smfmac_f32_32x32x32_bf16 v[168:183], v[244:247], v[224:231], v251\n" \
    "v_exp_f32 v202, v202\n v_exp_f32 v203, v203\n v_exp_f32 v204, v204\n ds_read_b128 v[244:247], v249 offset:512\n" \
    "v_exp_f32 v205, v205\n v_exp_f32 v206, v206\n v_exp_f32 v207, v207\n v_exp_f32 v208, v208\n s_setprio 0\n" \
    "v_exp_f32 v209, v209\n s_waitcnt lgkmcnt(1)\n v_smfmac_f32_32x32x32_bf16 v[168:183], v[240:243], v[200:207], v251\n" \
    "v_exp_f32 v210, v210\n v_exp_f32 v211, v211\n v_exp_f32 v212, v212\n v_exp_f32 v213, v213\n s_setprio 1\n" \
    "v_exp_f32 v214, v214\n v_exp_f32 v215, v215\n s_waitcnt lgkmcnt(0)\n s_setprio 0\n v_smfmac_f32_32x32x32_bf16 v[168:183], v[244:247], v[208:215], v251\n" \
    "s_nop 7\n s_nop 7\n" \
    ""
#define WIND0 \
    "ds_read_b128 v[232:235], %[qa]\n" \
    "ds_read_b128 v[236:239], %[k8]\n" \
    "ds_read_b128 v[240:243], v249\n" \
    "ds_read_b128 v[244:247], v249 offset:512\n" \
    "s_waitcnt lgkmcnt(2)\n v_mfma_f32_32x32x16_bf16 v[200:215], v[236:239], v[232:235], v[216:231]\n" \
    "v_add_u32 v249, v249, v250\n v_mov_b32 v168, 0\n v_mov_b32 v169, 0\n v_mov_b32 v170, 0\n v_mov_b32 v171, 0\n" \
    "v_mov_b32 v172, 0\n v_mov_b32 v173, 0\n v_mov_b32 v174, 0\n v_mov_b32 v175, 0\n v_mov_b32 v176, 0\n" \
    "s_nop 1\n v_exp_f32 v200, v200\n v_exp_f32 v201, v201\n v_exp_f32 v202, v202\n v_exp_f32 v203, v203\n" \
    "v_exp_f32 v204, v204\n v_exp_f32 v205, v205\n v_exp_f32 v206, v206\n v_exp_f32 v207, v207\n v_exp_f32 v208, v208\n" \
    "v_exp_f32 v209, v209\n s_waitcnt lgkmcnt(0)\n v_smfmac_f32_32x32x32_bf16 v[168:183], v[240:243], v[200:207], v251\n" \
    "v_exp_f32 v210, v210\n v_exp_f32 v211, v211\n v_exp_f32 v212, v212\n v_exp_f32 v213, v213\n v_exp_f32 v214, v214\n" \
    "v_exp_f32 v215, v215\n s_setprio 0\n s_nop 0\n v_smfmac_f32_32x32x32_bf16 v[168:183], v[244:247], v[208:215], v251\n" \
    "s_waitcnt vmcnt(12)\n s_movk_i32 s46, 0x2000\n 20:\n ds_write_b128 %[cv], v[104:107] offset:49152\n ds_write_b128 %[cv], v[108:111] offset:50176\n" \
    "ds_write_b128 %[cv], v[112:115] offset:51200\n ds_write_b128 %[cv], v[116:119] offset:52224\n v_max3_u32 v32, v104, v105, v106\n v_max3_u32 v33, v107, v108, v109\n v_max3_u32 v32, v32, v110, v111\n" \
    "v_max3_u32 v33, v33, v112, v113\n v_max3_u32 v32, v32, v114, v115\n v_max3_u32 v33, v33, v116, v117\n v_max3_u32 v32, v32, v118, v119\n v_max_u32 v32, v32, v33\n" \
    "v_cmp_eq_u32 vcc, -1, v32\n s_cbranch_vccz 30f\n s_sub_u32 s46, s46, 1\n s_cbranch_scc1 30f\n s_add_u32 s45, s45, 1\n" \
    "buffer_load_dwordx4 v[104:107], %[vo], %[rd], s40 offen offset:0 sc1\n" \
    "buffer_load_dwordx4 v[108:111], %[vo], %[rd], s40 offen offset:1024 sc1\n" \
    "buffer_load_dwordx4 v[112:115], %[vo], %[rd], s40 offen offset:2048 sc1\n" \
    "buffer_load_dwordx4 v[116:119], %[vo], %[rd], s40 offen offset:3072 sc1\n" \
    "s_waitcnt vmcnt(0)\n s_branch 20b\n 30:\n s_nop 7\n s_nop 7\n" \
    ""
#define WIND1 \
    "ds_read_b128 v[232:235], %[qa]\n" \
    "ds_read_b128 v[236:239], %[k8]\n" \
    "ds_read_b128 v[240:243], v249\n" \
    "ds_read_b128 v[244:247], v249 offset:512\n" \
    "s_waitcnt lgkmcnt(2)\n v_mfma_f32_32x32x16_bf16 v[200:215], v[236:239], v[232:235], v[216:231]\n" \
    "v_add_u32 v249, v249, v250\n v_mov_b32 v168, 0\n v_mov_b32 v169, 0\n v_mov_b32 v170, 0\n v_mov_b32 v171, 0\n" \
    "v_mov_b32 v172, 0\n v_mov_b32 v173, 0\n v_mov_b32 v174, 0\n v_mov_b32 v175, 0\n v_mov_b32 v176, 0\n" \
    "s_nop 1\n v_exp_f32 v200, v200\n v_exp_f32 v201, v201\n v_exp_f32 v202, v202\n v_exp_f32 v203, v203\n" \
    "v_exp_f32 v204, v204\n s_setprio 0\n v_exp_f32 v205, v205\n v_exp_f32 v206, v206\n v_exp_f32 v207, v207\n" \
    "v_exp_f32 v208, v208\n v_exp_f32 v209, v209\n s_waitcnt lgkmcnt(0)\n s_setprio 1\n v_smfmac_f32_32x32x32_bf16 v[168:183], v[240:243], v[200:207], v251\n" \
    "v_exp_f32 v210, v210\n v_exp_f32 v211, v211\n v_exp_f32 v212, v212\n v_exp_f32 v213, v213\n v_exp_f32 v214, v214\n" \
    "v_exp_f32 v215, v215\n s_setprio 0\n s_nop 0\n v_smfmac_f32_32x32x32_bf16 v[168:183], v[244:247], v[208:215], v251\n" \
    "s_nop 7\n s_nop 7\n" \
    ""
#define WIN_LOADS \
    "buffer_load_dwordx4 v[104:107], %[vo], %[rd], s40 offen offset:0 sc1\n" \
    "buffer_load_dwordx4 v[108:111], %[vo], %[rd], s40 offen offset:1024 sc1\n" \
    "buffer_load_dwordx4 v[112:115], %[vo], %[rd], s40 offen offset:2048 sc1\n" \
    "buffer_load_dwordx4 v[116:119], %[vo], %[rd], s40 offen offset:3072 sc1\n" \
    "buffer_load_dwordx4 v[120:123], %[vo], %[rd], s41 offen offset:0 sc1\n" \
    "buffer_load_dwordx4 v[124:127], %[vo], %[rd], s41 offen offset:1024 sc1\n" \
    "buffer_load_dwordx4 v[128:131], %[vo], %[rd], s41 offen offset:2048 sc1\n" \
    "buffer_load_dwordx4 v[132:135], %[vo], %[rd], s41 offen offset:3072 sc1\n" \
    "buffer_load_dwordx4 v[136:139], %[vo], %[rd], s42 offen offset:0 sc1\n" \
    "buffer_load_dwordx4 v[140:143], %[vo], %[rd], s42 offen offset:1024 sc1\n" \
    "buffer_load_dwordx4 v[144:147], %[vo], %[rd], s42 offen offset:2048 sc1\n" \
    "buffer_load_dwordx4 v[148:151], %[vo], %[rd], s42 offen offset:3072 sc1\n" \
    "buffer_load_dwordx4 v[152:155], %[vo], %[rd], s43 offen offset:0 sc1\n" \
    "buffer_load_dwordx4 v[156:159], %[vo], %[rd], s43 offen offset:1024 sc1\n" \
    "buffer_load_dwordx4 v[160:163], %[vo], %[rd], s43 offen offset:2048 sc1\n" \
    "buffer_load_dwordx4 v[164:167], %[vo], %[rd], s43 offen offset:3072 sc1\n" \
    ""
#define MASK_FILL \
    "s_mov_b32 vcc_lo, 0xffffffff\n s_mov_b32 vcc_hi, 0xffffff00\n v_cndmask_b32 v216, %2, %1, vcc\n s_mov_b32 vcc_lo, 0xfffffffe\n s_mov_b32 vcc_hi, 0xfffffe00\n" \
    "v_cndmask_b32 v217, %2, %1, vcc\n s_mov_b32 vcc_lo, 0xfffffffc\n s_mov_b32 vcc_hi, 0xfffffc00\n v_cndmask_b32 v218, %2, %1, vcc\n s_mov_b32 vcc_lo, 0xfffffff8\n" \
    "s_mov_b32 vcc_hi, 0xfffff800\n v_cndmask_b32 v219, %2, %1, vcc\n s_mov_b32 vcc_lo, 0xfffffff0\n s_mov_b32 vcc_hi, 0xfffff000\n v_cndmask_b32 v220, %2, %1, vcc\n" \
    "s_mov_b32 vcc_lo, 0xffffffe0\n s_mov_b32 vcc_hi, 0xffffe000\n v_cndmask_b32 v221, %2, %1, vcc\n s_mov_b32 vcc_lo, 0xffffffc0\n s_mov_b32 vcc_hi, 0xffffc000\n" \
    "v_cndmask_b32 v222, %2, %1, vcc\n s_mov_b32 vcc_lo, 0xffffff80\n s_mov_b32 vcc_hi, 0xffff8000\n v_cndmask_b32 v223, %2, %1, vcc\n s_mov_b32 vcc_lo, 0xffff0000\n" \
    "s_mov_b32 vcc_hi, 0xff000000\n v_cndmask_b32 v224, %2, %1, vcc\n s_mov_b32 vcc_lo, 0xfffe0000\n s_mov_b32 vcc_hi, 0xfe000000\n v_cndmask_b32 v225, %2, %1, vcc\n" \
    "s_mov_b32 vcc_lo, 0xfffc0000\n s_mov_b32 vcc_hi, 0xfc000000\n v_cndmask_b32 v226, %2, %1, vcc\n s_mov_b32 vcc_lo, 0xfff80000\n s_mov_b32 vcc_hi, 0xf8000000\n" \
    "v_cndmask_b32 v227, %2, %1, vcc\n s_mov_b32 vcc_lo, 0xfff00000\n s_mov_b32 vcc_hi, 0xf0000000\n v_cndmask_b32 v228, %2, %1, vcc\n s_mov_b32 vcc_lo, 0xffe00000\n" \
    "s_mov_b32 vcc_hi, 0xe0000000\n v_cndmask_b32 v229, %2, %1, vcc\n s_mov_b32 vcc_lo, 0xffc00000\n s_mov_b32 vcc_hi, 0xc0000000\n v_cndmask_b32 v230, %2, %1, vcc\n" \
    "s_mov_b32 vcc_lo, 0xff800000\n s_mov_b32 vcc_hi, 0x80000000\n v_cndmask_b32 v231, %2, %1, vcc\n" \
    ""
#define MQA_ZERO \
    "v_mov_b32 v224, 0\n v_mov_b32 v225, 0\n v_mov_b32 v226, 0\n v_mov_b32 v227, 0\n v_mov_b32 v228, 0\n" \
    "v_mov_b32 v229, 0\n v_mov_b32 v230, 0\n v_mov_b32 v231, 0\n v_mov_b32 v8, 0\n v_mov_b32 v9, 0\n" \
    "v_mov_b32 v10, 0\n v_mov_b32 v11, 0\n v_mov_b32 v12, 0\n v_mov_b32 v13, 0\n v_mov_b32 v14, 0\n" \
    "v_mov_b32 v15, 0\n v_mov_b32 v16, 0\n v_mov_b32 v168, 0\n v_mov_b32 v169, 0\n v_mov_b32 v170, 0\n" \
    "v_mov_b32 v171, 0\n v_mov_b32 v172, 0\n v_mov_b32 v173, 0\n v_mov_b32 v174, 0\n v_mov_b32 v175, 0\n" \
    "v_mov_b32 v176, 0\n" \
    ""
#define MQA_CINIT \
    "v_sub_f32 v184, 0, v32\n v_sub_f32 v185, 0, v32\n v_sub_f32 v186, 0, v32\n v_sub_f32 v187, 0, v32\n v_sub_f32 v188, 0, v32\n" \
    "v_sub_f32 v189, 0, v32\n v_sub_f32 v190, 0, v32\n v_sub_f32 v191, 0, v32\n v_sub_f32 v192, 0, v32\n v_sub_f32 v193, 0, v32\n" \
    "v_sub_f32 v194, 0, v32\n v_sub_f32 v195, 0, v32\n v_sub_f32 v196, 0, v32\n v_sub_f32 v197, 0, v32\n v_sub_f32 v198, 0, v32\n" \
    "v_sub_f32 v199, 0, v32\n" \
    ""
typedef unsigned u32x8 __attribute__((ext_vector_type(8)));
struct AttState { f32x16 o, o2, c, a, b; u32x4 q, q2, k, k2, v0, v1; unsigned ka, va, vs, vs2, idx; };
#define ATT_STATE_OPS(s) "+{v[168:183]}"(s.o), "+{v[200:215]}"(s.a), "+{v[216:231]}"(s.b), "+{v[236:239]}"(s.k), "+{v[240:243]}"(s.v0), "+{v[244:247]}"(s.v1), "+{v249}"(s.va)
typedef unsigned u32x16 __attribute__((ext_vector_type(16)));
typedef unsigned u32x32 __attribute__((ext_vector_type(32)));
#define MQA_OUT ATT_STATE_OPS(s), "+{v[8:23]}"(s.o2), "+{v[28:31]}"(s.k2), "+{s[40:55]}"(sc), "+{v[32:39]}"(ax), "+{v[184:199]}"(s.c)
#define MQA_IN "{v[232:235]}"(s.q), "{v[24:27]}"(s.q2), "{v248}"(s.ka), "{v250}"(s.vs), "{v252}"(s.vs2), "{v251}"(s.idx), [rd] "s"(rd)
#define MQA_TAIL "s_waitcnt lgkmcnt(0)\n\ts_nop 7\n\ts_nop 7"
#define MQA_FILL "{v[4:7]}"(fill), [r0] "s"(UNI(r0))
#define MQA_WORD [fv] "v"(fv), [fo] "s"(UNI(fo))
#define MQA_CLOB "vcc", "scc", "memory"
#define BYG(M, I) do { if constexpr (G0) M(MQA0_##I); else M(MQA1_##I); } while (0)
#define MQ0(B) asm volatile("ds_read2_b32 v[32:33], v35 offset0:2 offset1:4\n\tds_read_b128 v[236:239], v248\n\tds_read_b128 v[244:247], v249 offset:512\n\tds_read_b128 v[240:243], v249\n\t" \
                            "s_mov_b32 s50, %[tag]\n\ts_waitcnt lgkmcnt(3)\n\tv_max_f32 v37, v32, v33\n\tv_mul_f32 v37, %[slack], v37\n\tv_mul_f32 v32, v37, v38\n\tv_sqrt_f32 v32, v32\n\ts_nop 0\n" \
                            MQA_CINIT "s_waitcnt lgkmcnt(0)\n\t" \
                            "v_mfma_f32_32x32x16_bf16 v[200:215], v[236:239], v[232:235], v[184:199]\n" MQA_ZERO B : MQA_OUT : MQA_IN, MQA_FILL, [tag] "s"(tag), [slack] "i"(__builtin_bit_cast(unsigned, KSLACK)) : MQA_CLOB)
#define MQ1(B) asm volatile("v_cmp_gt_f32 vcc, v34, v37\n\ts_cbranch_vccz 6f\n" MQA_RESCALE "6:\n" B : MQA_OUT, [f] "=&v"(f) : MQA_IN, MQA_FILL, MQA_WORD : MQA_CLOB)
#define MQ2(B) asm volatile(B : MQA_OUT, "=&{v[40:71]}"(X) : MQA_IN, MQA_FILL : MQA_CLOB)
#define MQ3(B) asm volatile(B : MQA_OUT, "=&{v[72:103]}"(X) : MQA_IN, MQA_FILL : MQA_CLOB)
#define MQ4(B) asm volatile(B : MQA_OUT, "=&{v[104:135]}"(X) : MQA_IN, MQA_FILL : MQA_CLOB)
#define MQ5(B) asm volatile(B : MQA_OUT, "=&{v[136:167]}"(X) : MQA_IN, MQA_FILL : MQA_CLOB)
#define MQ6(B) asm volatile(B : MQA_OUT : MQA_IN, MQA_FILL, [to] "s"(UNI(to)) : MQA_CLOB)
#define MQ7(B) asm volatile(B "s_waitcnt vmcnt(0) lgkmcnt(0)\n\ts_setprio 0\n\tv_smfmac_f32_32x32x32_bf16 v[8:23], v[244:247], v[224:231], v251\n\ts_nop 7\n\ts_nop 7\n\t" EXW \
                            : MQA_OUT : MQA_IN, [fo] "s"(UNI(fo)) : MQA_CLOB)
template <bool G0> DEV void mqa_0(AttState& s, u32x16& sc, u32x8& ax, u32x4 rd, u32x4 fill, int r0, unsigned tag) { BYG(MQ0, 0); }
template <bool G0> DEV void mqa_1(AttState& s, u32x16& sc, u32x8& ax, u32x4 rd, u32x4 fill, int r0, unsigned& f, unsigned fv, int fo) { BYG(MQ1, 1); }
template <bool G0, int I> DEV void mqa_w(AttState& s, u32x16& sc, u32x8& ax, u32x32& X, u32x4 rd, u32x4 fill, int r0) {
    if constexpr (I == 2) BYG(MQ2, 2); else if constexpr (I == 3) BYG(MQ3, 3); else if constexpr (I == 4) BYG(MQ4, 4); else BYG(MQ5, 5); }
template <bool G0> DEV void mqa_6(AttState& s, u32x16& sc, u32x8& ax, u32x4 rd, u32x4 fill, int r0, int to) { BYG(MQ6, 6); }
template <bool G0> DEV void mqa_7(AttState& s, u32x16& sc, u32x8& ax, u32x4 rd, int fo) {
#define EXW "ds_write_b128 v35, v[8:11]\n\tds_write_b128 v35, v[12:15] offset:1024\n\tds_write_b32 v37, v16"
    if constexpr (G0) MQ7(MQA0_7);
#undef EXW
#define EXW "ds_write_b128 v35, v[168:171]\n\tds_write_b128 v35, v[172:175] offset:1024\n\tds_write_b32 v37, v176"
    else MQ7(MQA1_7);
#undef EXW
}
#define WIN_OUT ATT_STATE_OPS(s), "+{v[184:199]}"(s.c)
#define WIN_IN "{v248}"(s.ka), "{v[232:235]}"(s.q), "{v250}"(s.vs), "{v251}"(s.idx), [big] "v"(big), [nf] "v"(nf)
#define WTS
#define WTP
#define WTA
template <int I> DEV void unpack8(u32x4* W, const u32x32& x) { W[I] = __builtin_shufflevector(x, x, 4 * I, 4 * I + 1, 4 * I + 2, 4 * I + 3); if constexpr (I < 7) unpack8<I + 1>(W, x); }
DEV void window_loads(u32x32& x2, u32x32& x3, const u32x8& sc, unsigned vo, u32x4 rd) {
    asm volatile("s_nop 4\n" WIN_LOADS : "=&{v[104:135]}"(x2), "=&{v[136:167]}"(x3) : "{s[40:47]}"(sc), [vo] "v"(vo), [rd] "s"(rd) : "memory"); }
template <bool G0> DEV void att_diag(AttState& s, unsigned qa, unsigned k8, u32x8& sc, u32x32& xa, unsigned vo, unsigned cv, u32x4 rd) {
#define DIAG_IN "{v250}"(s.vs), "{v251}"(s.idx), [k8] "v"(k8), [qa] "v"(qa)
    if constexpr (G0) asm volatile(WIND0 : ATT_STATE_OPS(s), "=&{v[232:235]}"(s.q), "+{v[104:135]}"(xa), "+{s[40:47]}"(sc) : DIAG_IN, [rd] "s"(rd), [vo] "v"(vo), [cv] "v"(cv) : "vcc", "scc", "memory", "v32", "v33");
    else asm volatile(WIND1 : ATT_STATE_OPS(s), "=&{v[232:235]}"(s.q) : DIAG_IN : "memory");
}
template <bool G0> DEV void att_window(AttState& s, float big, float nf, u32x8& sc, u32x32& x1, u32x32& xa, u32x32& xb, unsigned vo, unsigned cv, u32x4 rd WTP) {
    if constexpr (G0) asm volatile("s_nop 4\n" WIN0 ".Lil_hot_end:\n\ts_waitcnt vmcnt(0)" : WIN_OUT, "=&{v[72:103]}"(x1), "+{v[104:135]}"(xa), "+{v[136:167]}"(xb), "+{s[40:47]}"(sc) WTS
                                   : WIN_IN, [rd] "s"(rd), [vo] "v"(vo), [cv] "v"(cv) : "vcc", "scc", "memory", "v32", "v33");
    else asm volatile("s_nop 1\n" WIN1 : WIN_OUT WTS : WIN_IN, "{s[40:47]}"(sc) : "vcc", "scc", "memory");
}
DEV f32x16 splat16(float v) { f32x16 r; for (int i = 0; i < 16; ++i) r[i] = v; return r; }
DEV float rowmax16(f32x16 s, float m) { for (int i = 0; i < 16; ++i) m = fmax_(m, s[i]); return m; }
DEV u32x4 norm_pack(f32x16 o) { const float r = __builtin_amdgcn_rcpf(o[8]); return (u32x4){pk2(o[0] * r, o[1] * r), pk2(o[2] * r, o[3] * r), pk2(o[4] * r, o[5] * r), pk2(o[6] * r, o[7] * r)}; }
DEV void mask_fill(f32x16& c, float on, float off) { asm volatile(MASK_FILL : "=&{v[216:231]}"(c) : "v"(on), "v"(off) : "vcc"); }

DEV void fold_t(const f32x16& acc, float r, u32x4& f0, u32x4& f1) {
    f0 = (u32x4){pk2(acc[0] * r, acc[1] * r), pk2(acc[2] * r, acc[3] * r), pk2(acc[4] * r, acc[5] * r), pk2(acc[6] * r, acc[7] * r)};
    f1 = (u32x4){pk2(acc[8] * r, acc[9] * r), pk2(acc[10] * r, acc[11] * r), pk2(acc[12] * r, acc[13] * r), pk2(acc[14] * r, acc[15] * r)};
}
struct RowStats { f32x4 r[4]; };
DEV void fold_n(const f32x16& a, const RowStats& st, u32x4& f0, u32x4& f1) {
    f0 = (u32x4){pk2(a[0] * st.r[0][0], a[1] * st.r[0][1]), pk2(a[2] * st.r[0][2], a[3] * st.r[0][3]), pk2(a[4] * st.r[1][0], a[5] * st.r[1][1]), pk2(a[6] * st.r[1][2], a[7] * st.r[1][3])};
    f1 = (u32x4){pk2(a[8] * st.r[2][0], a[9] * st.r[2][1]), pk2(a[10] * st.r[2][2], a[11] * st.r[2][3]), pk2(a[12] * st.r[3][0], a[13] * st.r[3][1]), pk2(a[14] * st.r[3][2], a[15] * st.r[3][3])};
}

DEV void kv_partial(const f32x16& acc, bool isV, lptr red, int lane) {
    const int n = lane & 31, h = lane >> 5;
    if (!isV) {
        lptr p = red + (h * 32 + slot_of(n)) * 32;
        *(LAS f32x4*)p = (f32x4){acc[0], acc[1], acc[2], acc[3]}; *(LAS f32x4*)(p + 16) = (f32x4){acc[4], acc[5], acc[6], acc[7]};
    } else {
#pragma unroll
        for (int s = 0; s < 2; ++s) { lptr p = red + ((s * 2 + h) * 16 + (n & 15)) * 32;
            *(LAS f32x4*)p = (f32x4){acc[8 * s], acc[8 * s + 1], acc[8 * s + 2], acc[8 * s + 3]}; *(LAS f32x4*)(p + 16) = (f32x4){acc[8 * s + 4], acc[8 * s + 5], acc[8 * s + 6], acc[8 * s + 7]}; }
    }
}
DEV float kv_final(lptr red0, lptr lds, bool isV, int tb, rsrc_t rw, int dst, int lane, float z) {
    lptr rp = lds + L_RSTD + (tb * 32 + (isV ? (lane >> 5) * 16 + ((lane >> 4) & 1) * 4 : slot_of(lane & 31))) * 4;
    const float r1 = *(LAS float*)rp; const f32x4 ra = ldsrf((lptr)((size_t)rp & ~(size_t)15)), rb = ldsrf((lptr)((size_t)rp & ~(size_t)15) + 32);
    f32x4 a = ldsrf(red0 + lane * 32), b = ldsrf(red0 + lane * 32 + 16), c = ldsrf(red0 + 4096 + lane * 32), d = ldsrf(red0 + 4096 + lane * 32 + 16);
    f32x4 e = ldsrf(red0 + 8192 + lane * 32), f_ = ldsrf(red0 + 8192 + lane * 32 + 16), g = ldsrf(red0 + 12288 + lane * 32), h = ldsrf(red0 + 12288 + lane * 32 + 16);
    a = (a + c) + (e + g); b = (b + d) + (f_ + h);
    if (isV) { a *= ra; b *= rb; } else { a *= r1; b *= r1; }
    const u32x4 f = {pk2(a[0], a[1]), pk2(a[2], a[3]), pk2(b[0], b[1]), pk2(b[2], b[3])};
    st16(rw, lane * 16, dst, f);
    asm volatile("" ::: "memory");
    return max32(addx32(sq4(f, z)));
}

template <bool g0>
DEV void program(const char* __restrict__ x0, const char* __restrict__ wq, const char* __restrict__ wk, const char* __restrict__ wv, const char* __restrict__ woa,
                 const char* __restrict__ wqkv, const char* __restrict__ wob, char* __restrict__ out, char* ws, lptr lds_, const int w_) {
#define FRESH() lptr lds = lds_; asm volatile("" : "+s"(lds)); const unsigned lds0 = (unsigned)(size_t)lds; (void)lds0; int w = w_, b = __builtin_amdgcn_workgroup_id_x(), lane; \
    asm volatile("" : "+s"(w), "+s"(b)); asm volatile("v_mbcnt_lo_u32_b32 %0, -1, 0\n\tv_mbcnt_hi_u32_b32 %0, -1, %0" : "=v"(lane)); \
    const int wi = w & 3, tid = w * 64 + lane, rb = 4 * (b & 7) + (b >> 6), cg = (b >> 3) & 7, n = lane & 31, h = lane >> 5, tbo = cg & 1, fg = cg >> 1; \
    unsigned one_, tag = tag_; float zero_; asm volatile("" : "+s"(tag)); asm volatile("v_mov_b32 %0, 0x3f803f80\n\tv_mov_b32 %1, 0" : "=v"(one_), "=v"(zero_)); const u32x4 ONES = {one_, one_, one_, one_}; (void)ONES; \
    const bool prod = cg < 4, isV = (cg >> 1) & 1; (void)wi; (void)tid; (void)rb; (void)n; (void)h; (void)tbo; (void)fg; (void)prod; (void)isV
    const rsrc_t rw = __builtin_amdgcn_make_buffer_rsrc(ws, 0, WS_END, 0x00020000);
    const u32x4 rd = {(unsigned)(size_t)ws, (unsigned)((size_t)ws >> 32) & 0xFFFFu, (unsigned)WS_END, 0x00020000u};
    unsigned ev, tag_ = 0;
    asm volatile("global_load_dword %0, %1, %2 sc1" : "=&v"(ev) : "v"(0), "s"(ws + WS_E) : "memory");
    const f32x16 ZERO = splat16(0.f);
    u32x4 W[32];
    unsigned nre = 0; (void)nre;
    unsigned tst = 0;
    u32x4 xr0, xr1;

#define MQA_SETUP(P) do { const int x_ = b & 7, gb_ = WS_KVA + (P) * 131072 + (w & 1) * 65536 + ((w >> 1) & 1) * 4096 + (w >> 2) * 2048; \
        mq.ka = lds0 + L_RING + (g0 ? 0 : 24576) + lane * 16; mq.va = lds0 + (n < 16 ? L_RING + (g0 ? 12288 : 36864) + h * 256 + n * 16 : L_CONST + h * 256 + (n - 16) * 16); \
        mq.vs = n < 16 ? 4096u : 0u; mq.vs2 = n < 16 ? -8192 : 0; mq.idx = 0xDDDDu; mq.F = (u32x4){SENT, SENT, SENT, SENT}; \
          \
        mq.r0 = WS_O + 2 * (P) * 2097152 + (rb * 64 + (w >> 2) * 32 + 4 * cg + wi) * 1024; \
        mq.r2 = WS_KVB + (P) * 4194304 + (rb * 8 + cg) * 16384 + w * 2048; \
        mq.r3 = WS_X + 2 * (P) * 2097152 + (rb * 64 + tbo * 32 + 8 * fg + w) * 1024; \
        mq.r5 = w < 2 - (P) ? WS_ST + (2 * (P) + w) * 262144 + rb * 8192 + tbo * 4096 + fg * 1024 : (w == 2 && (P) == 0 && prod) ? WS_KVA + 131072 + (isV ? 65536 : 0) + (rb * 2 + tbo) * 1024 : 0x7FFF0000; \
        mq.fo = WS_RDY + (P) * 32768 + 64 + w * 4096; \
        mq.sc[0] = gb_; mq.sc[1] = lds0 + L_RING + ((w >> 1) & 1) * 24576 + (w & 1) * 12288 + (w >> 2) * 2048; mq.sc[2] = WS_WT + (WT_QB + 2 * cg * 32 + 8 * w) * 1024; mq.sc[3] = lds0 + L_PARK + w * 8192; \
        mq.sc[4] = WS_WT + (g0 ? WT_KVB + (cg * 4 + wi) * 32 : WT_WOA + (4 * fg + wi) * 32) * 1024; mq.sc[5] = mq.sc[4] + 4096; mq.sc[6] = ((x_ + 1) & 7) * 8192; mq.sc[7] = gb_; \
        mq.sc[8] = 0; mq.sc[9] = WS_KAF + (P) * 16384; mq.sc[10] = 0; mq.sc[11] = 0; mq.sc[12] = 0; mq.sc[13] = 0; mq.sc[14] = 0; mq.sc[15] = 0; \
        mq.a7 = (lane & 15) * 128 + ((x_ + ((lane & 16) ? 2 + (w < 6 ? w : w - 6) : 1)) & 7) * 2048; mq.fv = n * 128; mq.qa = lds0 + L_QX + 2 * wi * 1024 + lane * 16; \
        mq.ro = only(tid == 0, WS_RDY + (P) * 32768 + b * 128 + 64); \
        asm volatile("" : "+{s[40:55]}"(mq.sc), "+s"(mq.r0), "+s"(mq.r2), "+s"(mq.r3), "+s"(mq.r5), "+s"(mq.fo)); \
        asm volatile("" : "+{v248}"(mq.ka), "+{v249}"(mq.va), "+{v250}"(mq.vs), "+{v252}"(mq.vs2), "+{v251}"(mq.idx), "+{v[4:7]}"(mq.F), "+{v39}"(mq.a7), "+v"(mq.fv), "+v"(mq.qa), "+v"(mq.ro)); } while (0)
    struct { u32x16 sc; u32x4 F; int r0, r2, r3, r5, fo; unsigned ka, va, vs, vs2, idx, a7, fv, qa, ro; } mq;

    {
        FRESH();
        STAMP(0); STAMPC(84);
        if (g0 && prod) {
            const char* src = (isV ? wv : wk) + wi * 2048;
#pragma unroll
            for (int j = 0; j < 4; ++j) dma1(lane * 16, src + (j >> 1) * 8192 + (j & 1) * 1024, lds0 + L_WK + wi * 4096 + j * 1024);
        }
        if constexpr (g0) st16(rw, only(prod && w == 2, lane * 16), WS_KVA + (isV ? 65536 : 0) + (rb * 2 + tbo) * 1024, (u32x4){SENT, SENT, SENT, SENT});
        u32x4 xv[8];
        const int row = 8 * w + (lane >> 3);
        {
            const char* src = x0 + (size_t)(rb * 64 + 8 * w) * 1024; const unsigned vo = (lane >> 3) * 1024 + (lane & 7) * 16;
            asm volatile("s_nop 4\n\tglobal_load_dwordx4 %0, %8, %9\n\tglobal_load_dwordx4 %1, %8, %9 offset:128\n\tglobal_load_dwordx4 %2, %8, %9 offset:256\n\tglobal_load_dwordx4 %3, %8, %9 offset:384\n\t"
                         "global_load_dwordx4 %4, %8, %9 offset:512\n\tglobal_load_dwordx4 %5, %8, %9 offset:640\n\tglobal_load_dwordx4 %6, %8, %9 offset:768\n\tglobal_load_dwordx4 %7, %8, %9 offset:896"
                         : "=&v"(xv[0]), "=&v"(xv[1]), "=&v"(xv[2]), "=&v"(xv[3]), "=&v"(xv[4]), "=&v"(xv[5]), "=&v"(xv[6]), "=&v"(xv[7]) : "v"(vo), "s"(uptr(src)) : "memory");
        }
        if constexpr (!g0) MQA_SETUP(0); else __builtin_amdgcn_s_sleep(HQ_D);
        u32x4 qv[8];
        {
            const unsigned vo = (lane >> 3) * 1024 + (((lane & 7) ^ (((lane >> 4) & 1) << 2)) * 16);
            const char* src = wq + cg * 128 + (size_t)w * 65536;
#define QLD(m, R) asm volatile("s_nop 4\n\tglobal_load_dwordx4 %0, %1, %2" : "=&{" R "}"(qv[m]) : "v"(vo), "s"(uptr(src + m * 8192)) : "memory")
            QLD(0, "v[200:203]"); QLD(1, "v[204:207]"); QLD(2, "v[208:211]"); QLD(3, "v[212:215]"); QLD(4, "v[216:219]"); QLD(5, "v[220:223]"); QLD(6, "v[224:227]"); QLD(7, "v[228:231]");
        }
        if constexpr (g0) { const unsigned v = ((tid & 11) == 0) ? 0x3F803F80u : 0u; ldsw(lds + L_CONST + tid * 16, (u32x4){v, v, v, v}); }
        asm volatile("s_waitcnt vmcnt(%9)" : "+v"(xv[0]), "+v"(xv[1]), "+v"(xv[2]), "+v"(xv[3]), "+v"(xv[4]), "+v"(xv[5]), "+v"(xv[6]), "+v"(xv[7]), "+v"(ev) : "i"(8) : "memory");
        tag = tag_ = __builtin_amdgcn_readfirstlane(ev) + 1u;
        STAMP(1);
        if constexpr (g0) __builtin_amdgcn_raw_buffer_store_b32(tag, rw, only(tid == 128, 72), WS_RDY + b * 128, 16);
        float s = zero_, q = zero_, s2 = zero_, q2 = zero_;
        lptr dst = lds + L_AT + (row >> 5) * HTS + ((lane & 7) >> 1) * HFS + (row & 31) * 16 + (lane & 1) * 8;
#pragma unroll
        for (int m = 0; m < 8; ++m) {
            *(LAS u32x2*)(dst + m * 4 * HFS) = (u32x2){xv[m][0], xv[m][1]};
            *(LAS u32x2*)(dst + m * 4 * HFS + HHS) = (u32x2){xv[m][2], xv[m][3]};
            if (m & 1) { s2 = sm4(xv[m], one_, s2); q2 = sq4(xv[m], q2); } else { s = sm4(xv[m], one_, s); q = sq4(xv[m], q); }
        }
        put_stats<false>(lds, row, sum8(s + s2), sum8(q + q2));
        asm volatile("s_waitcnt vmcnt(0)" : "+{v[200:203]}"(qv[0]), "+{v[204:207]}"(qv[1]), "+{v[208:211]}"(qv[2]), "+{v[212:215]}"(qv[3]),
                                            "+{v[216:219]}"(qv[4]), "+{v[220:223]}"(qv[5]), "+{v[224:227]}"(qv[6]), "+{v[228:231]}"(qv[7]) :: "memory");
#pragma unroll
        for (int m = 0; m < 8; ++m) ldsw(lds + L_WQ + w * 8192 + m * 1024 + lane * 16, qv[m]);
        BAR();
        const int gw = (rb * 4 + (cg & 3)) * 4 + wi;
        u32x4 pt[8]; unsigned pe[8]; int pid[2] = {0, 0}; bool pkv = false;
        if (g0 && !prod) {
            const int ks = gw & 31, gi = gw >> 5, rowo = 16 * ks + (lane >> 4);
            const char* src = gi < 4 ? woa + gi * 256 : wqkv + (gi - 4) * 256; const int ld = gi < 4 ? 1024 : 3072;
            pid[0] = ks + (gi < 4 ? WT_WOA + gi * 128 : gi < 8 ? WT_QB + (gi - 4) * 128 : WT_KVB + (gi & 3) * 256 + ((gi - 8) >> 2) * 64); pkv = gi >= 8;
#pragma unroll
            for (int m = 0; m < 4; ++m) pt[m] = *(const u32x4*)(src + (size_t)(rowo + 4 * m) * ld + (lane & 15) * 16);
            if (gw < 256) {
                const char* s2 = (gi < 4 ? wob : wq) + (gi & 3) * 256; pid[1] = ks + (gi < 4 ? WT_WOB : WT_QA) + (gi & 3) * 128;
#pragma unroll
                for (int m = 0; m < 4; ++m) pt[4 + m] = *(const u32x4*)(s2 + (size_t)(rowo + 4 * m) * 1024 + (lane & 15) * 16);
            } else if (gw < 320) {
                const unsigned short* s2 = (const unsigned short*)((gi & 1) ? wv : wk) + (16 * ks + 4 * h) * 16 + (lane & 15);
#pragma unroll
                for (int j = 0; j < 8; ++j) pe[j] = s2[((j >> 2) * 8 + (j & 3)) * 16];
            }
        }

        STAMP(2);
        if constexpr (g0) {
            if (prod) {
                lptr wp = lds + L_WK + wi * 4096 + (4 * h + ((lane & 15) >> 2)) * 32 + (lane & 3) * 8;
                f32x16 acc = ZERO;
                lptr ap = lds + L_AT + tbo * HTS + h * HHS + n * 16 + wi * 4 * HFS;
                u32x4 xf[8], Wj[8];
#pragma unroll
                for (int j = 0; j < 8; ++j) { Wj[j] = trfrag(wp + j * 512, 256); xf[j] = ldsr(ap + ((j >> 2) * 16 + (j & 3)) * HFS); }
                const u32x4 mf = mean_frag(lds, tbo * 32 + n);
                float cs = colsum8(Wj, ONES); asm volatile("" : "+v"(cs));
                const u32x4 cf = cs_frag(cs, h, zero_);
                if (isV) {
#pragma unroll
                    for (int j = 0; j < 8; ++j) acc = mfma(xf[j], Wj[j], acc);
                    acc = mfma(mf, cf, acc);
                } else {
#pragma unroll
                    for (int j = 0; j < 8; ++j) acc = mfma(Wj[j], xf[j], acc);
                    acc = mfma(cf, mf, acc);
                }
                kv_partial(acc, isV, lds + L_WK + wi * 4096, lane);
            }
            BAR();
            STAMP(73);
            tst = (unsigned)__builtin_amdgcn_s_memrealtime();
            float km = zero_;
            if (prod && w == 0) km = kv_final(lds + L_WK, lds, isV, tbo, rw, WS_KVA + (isV ? 65536 : 0) + (rb * 2 + tbo) * 1024, lane, zero_);
            if (prod && w == 0) VM0();
            st8(rw, only(prod && tid == 0, WS_KAF + (rb * 4 + cg) * 128), (u32x2){isV ? 0u : f2u(km), tag});
            if (!prod) {
                lptr sc = lds + L_WK + wi * 4096;
                lptr tp = sc + (4 * h + ((lane & 15) >> 2)) * 256 + ((lane >> 4) & 1) * 32 + (lane & 3) * 8;
#pragma unroll
                for (int m = 0; m < 4; ++m) ldsw(sc + m * 1024 + lane * 16, pt[m]);
#pragma unroll
                for (int j = 0; j < 4; ++j) st16(rw, WS_WT + (pid[0] + (pkv ? (j >> 1) * 128 + (j & 1) * 32 : j * 32)) * 1024 + lane * 16, trfrag(tp + j * 64, 2048));
                if (gw < 256) {
#pragma unroll
                    for (int m = 0; m < 4; ++m) ldsw(sc + m * 1024 + lane * 16, pt[4 + m]);
#pragma unroll
                    for (int j = 0; j < 4; ++j) st16(rw, WS_WT + (pid[1] + j * 32) * 1024 + lane * 16, trfrag(tp + j * 64, 2048));
                } else if (gw < 320) {
                    st16(rw, WS_WT + (WT_KA + gw - 256) * 1024 + lane * 16, (u32x4){pe[0] | (pe[1] << 16), pe[2] | (pe[3] << 16), pe[4] | (pe[5] << 16), pe[6] | (pe[7] << 16)});
                }
            }
        } else {
            const int f = wi & 1, tb = wi >> 1, qq = (lane & 15) >> 2;
            lptr wp = lds + L_WQ + (4 * h + qq) * 128 + ((f * 64 + ((lane >> 4) & 1) * 32 + (lane & 3) * 8) ^ ((qq >> 1) << 6));
            const unsigned ap = lds0 + L_AT + tb * HTS + h * HHS + n * 16;
            f32x16 acc;
#pragma unroll
            for (int ks = 0; ks < 32; ++ks) W[ks] = trfrag(wp + ks * 2048, 1024);
            const u32x4 mf = mean_frag(lds, tb * 32 + n); const float rs = *(LAS float*)(lds + L_RSTD + (tb * 32 + n) * 4) * QSCALE;
            lptr rp = lds + L_AT + tbo * HTS + (8 * fg + 2 * wi) * HFS + h * HHS + n * 16;
            xr0 = ldsr(rp); xr1 = ldsr(rp + HFS);
            f32x16 cs; colsum_a(cs, W, ONES);
            BAR();
            STAMP(73);
            const u32x4 cf = cs_frag(colsum_b(cs, W, ONES), h, zero_);
            gs32<HFS>(acc, W, ap); mean_step(acc, cf, mf);
            u32x4 q0, q1; fold_t(acc, rs, q0, q1);
            ldsw(lds + L_QX + ((2 * f) * 2 + tb) * 1024 + lane * 16, q0); ldsw(lds + L_QX + ((2 * f + 1) * 2 + tb) * 1024 + lane * 16, q1);
        }
    }

    _Pragma("clang loop unroll(disable)") for (int p = 0; p < 2; ++p) {
        u32x4 Wj[8];
        struct { u32x8 sc; int oo; unsigned ka, va, vs, idx, k8, qa, wv, cv, fv; float big; } wn;
        f32x16 oA; bool exact; unsigned filled;
        {
            FRESH();
            STAMP(3 + 20 * p);
            const unsigned vo = lane * 16;
            u32x4 c0, c1, c2, c3; u32x8 ax; AttState s;
            if constexpr (g0) MQA_SETUP(p);
            s.ka = mq.ka; s.va = mq.va; s.vs = mq.vs; s.vs2 = mq.vs2; s.idx = mq.idx;
            u32x16 sc = mq.sc; const u32x4 F = mq.F; const int r0 = mq.r0, r2 = mq.r2, r3 = mq.r3, r5 = mq.r5, fo = mq.fo, x = b & 7;
            const unsigned a7 = mq.a7, fv = mq.fv, qa = mq.qa, ro = mq.ro;
            const int gb = WS_KVA + p * 131072 + (w & 1) * 65536 + ((w >> 1) & 1) * 4096, lb = L_RING + ((w >> 1) & 1) * 24576 + (w & 1) * 12288;
            if constexpr (g0) {
                if (p == 0) gate((x | ((lane & 3) << 3) | (((lane >> 2) & 3) << 6)) * 128 + 72, rd, WS_RDY, tag);
                _Pragma("clang loop unroll(disable)") while ((int)((unsigned)__builtin_amdgcn_s_memrealtime() - tst) < (p == 0 ? KA_TICKS0 : KA_TICKS) + (prod ? 0 : p == 0 ? KA_NP0 : KA_NP)) {}
                STAMP0(82);
                gld4(c0, c1, c2, c3, vo, rd, gb + x * 8192);
                valid4<0>(c0, c1, c2, c3, vo, rd, gb + x * 8192 NRETRY);
                float k0 = zero_, k1 = zero_, k2 = zero_, k3 = zero_;
#pragma unroll
                for (int e = 0; e < 4; ++e) {
                    k0 = __builtin_fmaf(bflo(c0[e]), bflo(c0[e]), __builtin_fmaf(bfhi(c0[e]), bfhi(c0[e]), k0)); k1 = __builtin_fmaf(bflo(c1[e]), bflo(c1[e]), __builtin_fmaf(bfhi(c1[e]), bfhi(c1[e]), k1));
                    k2 = __builtin_fmaf(bflo(c2[e]), bflo(c2[e]), __builtin_fmaf(bfhi(c2[e]), bfhi(c2[e]), k2)); k3 = __builtin_fmaf(bflo(c3[e]), bflo(c3[e]), __builtin_fmaf(bfhi(c3[e]), bfhi(c3[e]), k3));
                }
                *(LAS float*)(lds + L_KMA + 8 + w * 4) = max32(fmax_(fmax_(addx32(k0), addx32(k1)), fmax_(addx32(k2), addx32(k3))));
                if (w == 0) *(LAS float*)(lds + L_KMA) = zero_;
            }
            BAR();
            STAMP(4 + 20 * p);
            if constexpr (g0) { lptr dst = lds + lb + lane * 16; ldsw(dst, c0); ldsw(dst + 1024, c1); ldsw(dst + 2048, c2); ldsw(dst + 3072, c3); }
            else { ldsw(lds + L_XR + wi * 2048 + lane * 16, xr0); ldsw(lds + L_XR + wi * 2048 + 1024 + lane * 16, xr1); }
            s.q = ldsr((lptr)(size_t)qa); s.q2 = ldsr((lptr)(size_t)qa + 1024);
            const float qn = fmax_(addx32(sq4(s.q, zero_)), addx32(sq4(s.q2, zero_)));
            ax[0] = vo; ax[1] = vo; ax[2] = vo; ax[3] = lds0 + L_KMA; ax[4] = vo; ax[5] = vo; ax[6] = f2u(qn); ax[7] = a7;
            asm volatile("" : "={v[168:183]}"(s.o), "={v[8:23]}"(s.o2), "={v[216:231]}"(s.b), "={v[200:215]}"(s.a), "={v[236:239]}"(s.k), "={v[28:31]}"(s.k2), "={v[240:243]}"(s.v0), "={v[244:247]}"(s.v1),
                              "={v[184:199]}"(s.c), "+{v[232:235]}"(s.q), "+{v[24:27]}"(s.q2), "+{v[32:39]}"(ax));
            BAR();
            STAMP(5 + 20 * p);
            unsigned f; u32x32 X0, X1, X2, X3;
            __builtin_amdgcn_raw_buffer_store_b32(tag, rw, ro, 0, 16);
            mqa_0<g0>(s, sc, ax, rd, F, r0, tag);
            mqa_1<g0>(s, sc, ax, rd, F, r0 + 2097152, f, fv, fo);
            asm volatile("s_waitcnt vmcnt(2)" : "+v"(f) :: "memory");
            if (__builtin_expect(!all_(f == tag), 0)) gate(fv, rd, fo, tag);
            ax[0] = tag; ax[1] = only(ax[4] == 0, 0); ax[2] = filled_word<g0>(b, ax[4] >> 4); ax[3] = lds0 + L_EX + w * 2304 + ax[4]; ax[5] = lds0 + L_EX + w * 2304 + 2048 + (ax[4] >> 2);
            asm volatile("s_barrier" : "+{v[32:39]}"(ax) :: "memory");
            mqa_w<g0, 2>(s, sc, ax, X0, rd, F, r2);
            mqa_w<g0, 3>(s, sc, ax, X1, rd, F, r2 + 1024);
            mqa_w<g0, 4>(s, sc, ax, X2, rd, F, r3);
            mqa_w<g0, 5>(s, sc, ax, X3, rd, F, p == 0 ? r3 + 2097152 : 0x7FFF0000);
            mqa_6<g0>(s, sc, ax, rd, F, r5, WS_RDY + p * 32768 + b * 128 + w * 4);
            mqa_7<g0>(s, sc, ax, rd, WS_RDY + p * 32768);
            filled = ax[6];
            unpack8<0>(W, X0); unpack8<0>(W + 8, X1); unpack8<0>(W + 16, X2); unpack8<0>(W + 24, X3);
            exact = !all_(s.c[0] >= -MBOUND);
            STAMP(11 + 20 * p); STAMPV(77 + p, sc[8]);
            oA = g0 ? s.o : s.o2;
        }
        {
            FRESH();
            if (__builtin_expect(exact, 0)) {
                const int kv = WS_KVA + p * 131072; const unsigned ko = lane * 16, vv = only(n < 16, 65536 + h * 256 + n * 16), om = (n == 16 || n == 20) ? one_ : 0u;
                _Pragma("clang loop unroll(disable)") for (int j = 0; j < 2; ++j) {
                    u32x4 q = ldsr(lds + L_QX + (2 * wi + j) * 1024 + lane * 16);
                    asm volatile("" : "+{v[232:235]}"(q));
                    float mx = NEG_BIG;
                    _Pragma("clang loop unroll(disable)") for (int t = 0; t < 64; ++t) {
                        f32x16 sc = mfma(ld16(rw, ko, kv + t * 1024), q, ZERO); asm volatile("" : "+{v[200:215]}"(sc)); mx = rowmax16(sc, mx); }
                    f32x16 c = splat16(-maxx32(mx)), o = splat16(zero_);
                    _Pragma("clang loop unroll(disable)") for (int t = (g0 ? 0 : 32); t < (g0 ? 32 : 64); ++t) {
                        asm volatile("" : "+{v[184:199]}"(c));
                        f32x16 sc = mfma(ld16(rw, ko, kv + t * 1024), q, c);
#pragma unroll
                        for (int i = 0; i < 16; ++i) sc[i] = __builtin_amdgcn_exp2f(sc[i]);
                        asm volatile("" : "+{v[200:215]}"(sc));
                        o = smf(ld16(rw, vv, kv + t * 1024) | om, lo8(sc), o); o = smf(ld16(rw, vv, kv + t * 1024 + 512) | om, hi8(sc), o);
                        asm volatile("" : "+{v[216:231]}"(o));
                    }
                    if ((j == 0) == g0) oA = o;
                    else { lptr ex = lds + L_EX + w * 2304 + lane * 16; *(LAS f32x4*)ex = (f32x4){o[0], o[1], o[2], o[3]}; *(LAS f32x4*)(ex + 1024) = (f32x4){o[4], o[5], o[6], o[7]}; *(LAS float*)(ex + 2048 - lane * 12) = o[8]; }
                }
            }
            if (__builtin_expect(!all_(filled == tag), 0)) gate(filled_word<g0>(b, lane), rd, WS_RDY + p * 32768, tag);
            lptr ey = lds + L_EX + (w ^ 4) * 2304 + lane * 16, el = lds + L_EX + (w ^ 4) * 2304 + 2048 + lane * 4;
            asm volatile("" : "+v"(ey), "+v"(el));
            BAR();
            f32x16 o = oA;
            const f32x4 x = ldsrf(ey), y = ldsrf(ey + 1024); o[8] += *(LAS float*)el;
#pragma unroll
            for (int e = 0; e < 4; ++e) { o[e] += x[e]; o[4 + e] += y[e]; }
            st16(rw, lane * 16, WS_O + 2 * p * 2097152 + (rb * 64 + (w >> 2) * 32 + 4 * cg + wi) * 1024, norm_pack(o));
            if constexpr (!g0) { xr0 = ldsr(lds + L_XR + wi * 2048 + lane * 16); xr1 = ldsr(lds + L_XR + wi * 2048 + 1024 + lane * 16); }
        }
#define OUT_ISSUE(OBUF, SB, SLP) \
            const int osrc = WS_O + (OBUF) * 2097152 + (rb * 64 + tbo * 32 + 4 * w) * 1024; lptr odst = lds + L_AT + 4 * w * 1024 + lane * 16; \
            u32x4 o_[4]; \
            __builtin_amdgcn_s_sleep(SLP); STAMP0(SB); nre = 0; \
            gld4(o_[0], o_[1], o_[2], o_[3], lane * 16, rd, osrc)
#define OUT_STAGES(SB) do { stage4<0>(o_[0], o_[1], o_[2], o_[3], lane * 16, rd, osrc, odst NRETRY); STAMP0(SB + 4); if (p == 0) STAMPV(SB + 5, nre); BAR(); } while (0)
#define WIN_SETUP() do { const int tb_ = w & 1, hh_ = w >> 1, nbk = rb - 1 - ((lane >> 2) & 3); \
            _Pragma("unroll") for (int i = 0; i < 4; ++i) wn.sc[i] = WS_KVB + p * 4194304 + ((rb > i ? rb - 1 - i : rb) * 8 + cg) * 16384 + wi * 4096; \
            wn.sc[4] = WS_WT + (WT_WOB + (4 * fg + wi) * 32 + 8) * 1024; wn.sc[5] = 0; wn.oo = WS_O + (2 * p + 1) * 2097152 + (rb * 64 + tb_ * 32 + 4 * cg + hh_) * 1024; wn.sc[6] = tag; wn.sc[7] = 2 * rb + tb_ + 1; \
            wn.k8 = lds0 + L_KVB + (8 + tb_) * 8192 + hh_ * 2048 + lane * 16; wn.ka = wn.k8 - 65536; \
            wn.va = lds0 + (n < 16 ? L_KVB + (8 + tb_) * 8192 + hh_ * 2048 + 1024 + h * 256 + n * 16 : L_CONST + h * 256 + (n - 16) * 16); \
            wn.vs = n < 16 ? -8192 : 0; wn.idx = 0xDDDDu; wn.big = NEG_BIG; wn.qa = lds0 + L_QX + w * 1024 + lane * 16; wn.wv = lane * 16; wn.cv = lds0 + L_KVB + wi * 4096 + lane * 16; \
            wn.fv = ((nbk >= 0 ? nbk : rb) * 8 + cg) * 128 + (lane & 3) * 8; \
            asm volatile("" : "+{s[40:47]}"(wn.sc), "+s"(wn.oo)); \
            asm volatile("" : "+{v248}"(wn.ka), "+{v249}"(wn.va), "+{v250}"(wn.vs), "+{v251}"(wn.idx), "+v"(wn.k8), "+v"(wn.big), "+v"(wn.qa), "+v"(wn.wv), "+v"(wn.cv), "+v"(wn.fv)); } while (0)
#define OUT_MMA(PK, OBUF, SLP) do { \
            OUT_ISSUE(OBUF, 83, SLP); \
            f32x16 acc; \
            _Pragma("unroll") for (int j = 0; j < 4; ++j) { acc[2 * j] = bflo(xr0[j]); acc[2 * j + 1] = bfhi(xr0[j]); acc[8 + 2 * j] = bflo(xr1[j]); acc[9 + 2 * j] = bfhi(xr1[j]); } \
            const unsigned ap_ = lds0 + L_AT + lane * 16; \
            asm volatile("" : "+{v[168:183]}"(acc)); \
            stage4<0>(o_[0], o_[1], o_[2], o_[3], lane * 16, rd, osrc, odst NRETRY); BAR(); gs32_res(acc, W, ap_); \
            settle(acc); \
            _Pragma("unroll") for (int j = 0; j < 8; ++j) PK[j] = pk2(acc[2 * j], acc[2 * j + 1]); } while (0)
#define OUT_PUBLISH(PK, XBUF) do { \
            const u32x4 f0 = {PK[0], PK[1], PK[2], PK[3]}, f1 = {PK[4], PK[5], PK[6], PK[7]}; \
            const int xo = WS_X + (XBUF) * 2097152 + (rb * 64 + tbo * 32 + 8 * fg + 2 * wi) * 1024; \
            st16(rw, lane * 16, xo, f0); st16(rw, lane * 16 + 1024, xo, f1); \
            const float s1 = addx32(sm4(f1, one_, sm4(f0, one_, zero_))), s2 = addx32(sq4(f1, sq4(f0, zero_))); \
            st8(rw, only(lane < 32, lane * 8), WS_ST + (XBUF) * 262144 + rb * 8192 + tbo * 4096 + (4 * fg + wi) * 256, (u32x2){f2u(s1), f2u(s2)}); } while (0)

        if constexpr (g0) {
            FRESH();
            STAMP(12 + 20 * p);
            { OUT_ISSUE(2 * p, 44, HS_OA); OUT_STAGES(44); }
            STAMP(13 + 20 * p);
            f32x16 a0, a1; Tmp t;
            lptr ap = lds + L_AT + lane * 16;
            const unsigned aa = lds0 + L_AT + lane * 16, ab = aa + 32768;
            const int kvo = WS_KVB + p * 4194304 + (rb * 8 + cg) * 16384;
            int isK = UNI((wi >> 1) ^ 1); asm volatile("" : "+s"(isK));
            u32x4 cf;
            int o = isK ? 2 * wi * 2048 + h * 512 + slot_of(n) * 16 : (2 * (wi - 2) + (n >> 4)) * 2048 + 1024 + h * 256 + (n & 15) * 16;
            int go = kvo + o, go2 = go + (isK ? 2048 : 512); unsigned lo = lds0 + L_KVB + 65536 + o, lo2 = lo + (isK ? 2048 : 512);
            const unsigned ra = lds0 + (isK ? L_RSTD4 + n * 16 : L_RSTD + h * 16), rb1 = ra + (isK ? 512 : 128), rs = isK ? 0 : 32;
            asm volatile("" : "+v"(go), "+v"(go2), "+v"(lo), "+v"(lo2));
            __builtin_amdgcn_raw_buffer_store_b32(tag, rw, only(tid == 0 && b == 0, WS_E), 0, 16);
            WIN_SETUP();
            if (p == 0) gate(((b & 7) | ((lane & 3) << 3) | (((lane >> 2) & 3) << 6)) * 128 + 8, rd, WS_RDY, tag);
            BAR();
            STAMP(14 + 20 * p);
            {
                const int src = WS_X + 2 * p * 2097152 + (rb * 64 + (wi >> 1) * 32 + (wi & 1) * 4) * 1024; lptr dst = ap + ((wi >> 1) * 32 + (wi & 1) * 4) * 1024;
                u32x4 r[8];
                f32x16 cs; colsum_a(cs, W, ONES); if (HS_X) __builtin_amdgcn_s_sleep(HS_X); STAMP0(64); nre = 0;
                gld4(r[0], r[1], r[2], r[3], lane * 16, rd, src); gld4(r[4], r[5], r[6], r[7], lane * 16, rd, src + 8192);
                cf = cs_frag(colsum_b(cs, W, ONES), h, zero_);
                stage4<4>(r[0], r[1], r[2], r[3], lane * 16, rd, src, dst NRETRY);
                stage4<0>(r[4], r[5], r[6], r[7], lane * 16, rd, src + 8192, dst + 8192 NRETRY); STAMP0(65);
            }
            BAR(); gd<0>(isK, a0, a1, t, W, aa, ab); gd<1>(isK, a0, a1, t, W, aa, ab); gd<2>(isK, a0, a1, t, W, aa, ab);
            asm volatile("s_barrier" ::: "memory");
            RowStats st0, st1;
#pragma unroll
            for (int g = 0; g < 4; ++g) { st0.r[g] = ldsrf((lptr)(size_t)(ra + g * rs)); st1.r[g] = ldsrf((lptr)(size_t)(rb1 + g * rs)); }
            const u32x4 m0 = mean_frag(lds, n), m1 = mean_frag(lds, 32 + n);
            gd<3>(isK, a0, a1, t, W, aa, ab);
            asm volatile("s_barrier" ::: "memory");
            mean_step2(isK, a0, a1, cf, m0, m1); STAMP0(56);
            u32x4 f0, f1, f2, f3;
            fold_n(a0, st0, f0, f1); st16(rw, go, f0); st16(rw, go2, f1);
            asm volatile("" ::: "memory");
            fold_n(a1, st1, f2, f3); st16(rw, go + 8192, f2); st16(rw, go2 + 8192, f3);
            tst = (unsigned)__builtin_amdgcn_s_memrealtime();
            asm volatile("" ::: "memory");
            STAMP0(63);
            ldsw((lptr)(size_t)lo, f0); ldsw((lptr)(size_t)lo2, f1); ldsw((lptr)(size_t)lo + 8192, f2); ldsw((lptr)(size_t)lo2 + 8192, f3);
            STAMP(15 + 20 * p); nre = 0;
            {
                FRESH();
                const int wo = WS_WT + (WT_WOB + (4 * fg + wi) * 32) * 1024;
                float q0 = zero_, q1 = zero_, q2 = zero_, q3 = zero_;
#pragma unroll
                for (int e = 0; e < 8; ++e) { q0 = __builtin_fmaf(a0[e], a0[e], q0); q1 = __builtin_fmaf(a0[8 + e], a0[8 + e], q1); q2 = __builtin_fmaf(a1[e], a1[e], q2); q3 = __builtin_fmaf(a1[8 + e], a1[8 + e], q3); }
                { const float s0 = st0.r[0][0] * st0.r[0][0], s1 = st1.r[0][0] * st1.r[0][0]; q0 *= s0; q1 *= s0; q2 *= s1; q3 *= s1; }
                lptr ap = lds + L_AT + lane * 16;
                xr0 = ldsr(ap + (tbo * 32 + 8 * fg + 2 * wi) * 1024); xr1 = ldsr(ap + (tbo * 32 + 8 * fg + 2 * wi + 1) * 1024);
                const float k0 = fmax_(addx32(q0), addx32(q2)), k1 = fmax_(addx32(q1), addx32(q3));
                const float kown = max32(h ? k1 : k0);
                *(LAS float*)(lds + L_KMOWN + (2 * wi + h) * 4) = kown;
                st8(rw, only(n == 0 && wi < 2, WS_KBF + p * 32768 + (rb * 8 + cg) * 128 + (2 * wi + h) * 8), (u32x2){f2u(kown), tag});
                STAMP0(57);
                wld<0>(W, lane * 16, rd, wo);
                STAMP(16 + 20 * p);
            }
        } else {
            FRESH();
            unsigned pk[8];
            STAMP(12 + 20 * p);
            OUT_MMA(pk, 2 * p, HS_OA);
            STAMP(13 + 20 * p);
            OUT_PUBLISH(pk, 2 * p);
            const unsigned tx = (unsigned)__builtin_amdgcn_s_memrealtime();
            if (w == 4) st8(rw, only(lane == 0, 64), WS_KBF + p * 32768 + (rb * 8 + cg) * 128, (u32x2){tx, tag});
            BAR();
            STAMP(14 + 20 * p);
            const int f = wi & 1, tb = wi >> 1;
            __builtin_amdgcn_s_sleep(7 + HS_X); STAMP0(56); nre = 0;
            f32x16 acc;
            {
                const int src = WS_X + 2 * p * 2097152 + (rb * 64 + tb * 32 + f * 4 + 16) * 1024; lptr dst = lds + L_AT + (tb * 32 + f * 4 + 16) * 1024 + lane * 16;
                const int ssrc = WS_ST + 2 * p * 262144 + rb * 8192 + tb * 4096 + f * 128; const unsigned svo = (lane >> 4) * 1024 + (lane & 15) * 8;
                u32x4 r[8]; StatLd sv;
                gld4(r[0], r[1], r[2], r[3], lane * 16, rd, src); gld4(r[4], r[5], r[6], r[7], lane * 16, rd, src + 8192);
                stats_load(sv, svo, rd, ssrc);
                WIN_SETUP();
                stage4<8>(r[0], r[1], r[2], r[3], lane * 16, rd, src, dst NRETRY); STAMP0(59);
                stage4<4>(r[4], r[5], r[6], r[7], lane * 16, rd, src + 8192, dst + 8192 NRETRY); STAMP0(60); if (p == 0) STAMPV(61, nre); BAR();
                stage_stats<0>(sv, svo, rd, ssrc, lds, wi * 16 + (lane & 15)); STAMP0(62);
                const unsigned wv = p == 0 ? lane * 16 : 0x7FFFF000; const int wo = WS_WT + (WT_QA + 2 * cg * 32 + 16 * wi - 8) * 1024;
                u32x2 an; asm volatile("buffer_load_dwordx2 %0, %1, %2, %3 offen offset:64 sc1" : "=&v"(an) : "v"(wn.fv & ~127u), "s"(rd), "s"(UNI(WS_KBF + p * 32768)) : "memory");
                wld<1>(W, wv, rd, wo); wld<2>(W, wv, rd, wo);
                asm volatile("s_waitcnt vmcnt(16)" : "+v"(an) :: "memory");
                int d = an[1] == tag ? (int)(an[0] - tx) : 150; d = d < 0 ? 0 : d > 150 ? 150 : d;
                *(LAS float*)(lds + L_LATE) = max16((float)d);
                BAR();
            }
            STAMP(15 + 20 * p);
            const unsigned ap = lds0 + L_AT + tb * 32768 + lane * 16;
            const u32x4 mf = mean_frag(lds, tb * 32 + n); const float rs = *(LAS float*)(lds + L_RSTD + (tb * 32 + n) * 4) * QSCALE;
            asm volatile("s_barrier" ::: "memory");
            mean_step(acc, cs_frag(gs32_ll(acc, lds0 + L_PARK + f * 32768 + lane * 16, ap, ONES), h, zero_), mf); STAMP0(64);
            asm volatile("s_waitcnt vmcnt(0)" : WIO_Q1 :: "memory"); asm volatile("" : WIO_Q2);
            u32x4 q0, q1; fold_t(acc, rs, q0, q1);
            ldsw(lds + L_QX + ((2 * f) * 2 + tb) * 1024 + lane * 16, q0); ldsw(lds + L_QX + ((2 * f + 1) * 2 + tb) * 1024 + lane * 16, q1);
            STAMP(16 + 20 * p);
        }
        {
            FRESH();
            const int hh = w >> 1, kt = 2 * rb + (w & 1);
            u32x32 x1, xa, xb; u32x2 kw; AttState s; const int fo = UNI(WS_KBF + p * 32768);
            s.ka = wn.ka; s.va = wn.va; s.vs = wn.vs; s.idx = wn.idx;
            u32x8 sc = wn.sc; const unsigned qa = wn.qa, wv = wn.wv, cv = wn.cv, fv = wn.fv, k8 = wn.k8; const float big = wn.big;
            mask_fill(s.b, zero_, big);
            asm volatile("" : "={v[168:183]}"(s.o), "={v[200:215]}"(s.a), "={v[236:239]}"(s.k), "={v[240:243]}"(s.v0), "={v[244:247]}"(s.v1));
            if constexpr (g0) {
                const unsigned until = tst + KV_TICKS + UNI((unsigned)*(LAS float*)(lds + L_LATE));
                _Pragma("clang loop unroll(disable)") while ((int)((unsigned)__builtin_amdgcn_s_memrealtime() - until) < 0) {}
                STAMP0(58);
                window_loads(xa, xb, sc, wv, rd);
                STAMP0(59);
            }
            BAR();
            STAMP(17 + 20 * p);
            if constexpr (!g0) asm volatile("buffer_load_dwordx2 %0, %1, %2, %3 offen sc1" : "=&{v[34:35]}"(kw) : "v"(fv), "s"(rd), "s"(fo) : "memory");
            att_diag<g0>(s, qa, k8, sc, xa, wv, cv, rd);
            const float kown = *(LAS float*)(lds + L_KMOWN + hh * 4), qn = addx32(sq4(s.q, zero_)) * 1.02f;
            if constexpr (!g0) {
                asm volatile("s_waitcnt vmcnt(0)" : "+{v[34:35]}"(kw) :: "memory");
                _Pragma("clang loop unroll(disable)") for (unsigned sp = 0; !all_(kw[1] == tag) && sp < SPIN_CAP; ++sp)
                    asm volatile("s_sleep 4\n\tbuffer_load_dwordx2 %0, %1, %2, %3 offen sc1\n\ts_waitcnt vmcnt(0)" : "=&{v[34:35]}"(kw) : "v"(fv), "s"(rd), "s"(fo) : "memory");
                float kn = u2f(kw[0]); kn = fmax_(kn, dpp<0x124>(kn)); kn = fmax_(kn, dpp<0x128>(kn));
                *(LAS float*)(lds + L_KMNB + (lane & 3) * 4) = kn;
            }
            STAMP0(60);
            BAR();
            const float m = __builtin_amdgcn_sqrtf(qn * fmax_(kown, *(LAS float*)(lds + L_KMNB + hh * 4))), fs = __builtin_amdgcn_exp2f(-m);
            const bool redo = !all_(m <= MBOUND);
#pragma unroll
            for (int i = 0; i < 9; ++i) s.o[i] *= fs;
            s.c = splat16(kt >= 1 ? -m : big);
            att_window<g0>(s, big, kt >= 8 ? -m : big, sc, x1, xa, xb, wv, cv, rd WTA);
            if constexpr (g0) { unpack8<0>(W + 8, x1); unpack8<0>(W + 16, xa); unpack8<0>(W + 24, xb); }
            if (__builtin_expect(redo, 0)) {
                FRESH();
                const int tb = w & 1, hh = w >> 1, kt = 2 * rb + tb; const float big = NEG_BIG;
                lptr kp = lds + L_KVB + (8 + tb) * 8192 + hh * 2048 + lane * 16;
                const u32x4 q = ldsr(lds + L_QX + w * 1024 + lane * 16);
                lptr vp = lds + (n < 16 ? L_KVB + (8 + tb) * 8192 + hh * 2048 + 1024 + h * 256 + n * 16 : L_CONST + h * 256 + (n - 16) * 16);
                f32x16 o = splat16(zero_); float mx = zero_, rm = big;
                _Pragma("clang loop unroll(disable)") for (int pass = 0; pass < 2; ++pass) {
                    _Pragma("clang loop unroll(disable)") for (int t = 0; t <= (kt < 8 ? kt : 8); ++t) {
                        f32x16 c; mask_fill(c, t == 8 ? big : mx, t == 0 ? big : mx);
                        asm volatile("s_nop 1" : "+{v[216:231]}"(c));
                        f32x16 sc = mfma(ldsr(kp - t * 8192), q, c);
                        if (pass == 0) rm = rowmax16(sc, rm);
                        else {
#pragma unroll
                            for (int i = 0; i < 16; ++i) sc[i] = __builtin_amdgcn_exp2f(sc[i]);
                            asm volatile("" : "+{v[200:215]}"(sc));
                            o = smf(ldsr(vp), lo8(sc), o); o = smf(ldsr(vp + 512), hi8(sc), o); vp -= n < 16 ? 8192 : 0;
                            asm volatile("" : "+{v[168:183]}"(o));
                        }
                    }
                    mx = -maxx32(rm);
                }
                s.o = o;
            }
            st16(rw, wv, wn.oo, norm_pack(s.o));
            STAMP(18 + 20 * p); STAMPV(75 + p, sc[5]);
            if (p == 0 && g0 && prod) {
                const int wo = WS_WT + ((isV ? WT_VA : WT_KA) + 4 * wi) * 1024 + lane * 16;
#pragma unroll
                for (int j = 0; j < 8; ++j) Wj[j] = ld16(rw, wo + ((j >> 2) * 16 + (j & 3)) * 1024);
            } else if (g0) {
#pragma unroll
                for (int j = 0; j < 8; ++j) asm volatile("" : "=v"(Wj[j]));
            }
            BAR();
        }
        if constexpr (g0) {
            FRESH();
            unsigned pk[8];
            STAMP(19 + 20 * p);
            OUT_MMA(pk, 2 * p + 1, HS_OB);
            STAMP(20 + 20 * p);
            if (p == 1) {
                const rsrc_t ro = __builtin_amdgcn_make_buffer_rsrc(out, 0, 2097152, 0x00020000);
                const int dst = (rb * 64 + tbo * 32 + n) * 1024 + (128 * fg + 32 * wi) * 2 + h * 16;
#pragma unroll
                for (int g = 0; g < 2; ++g) {
                    auto r0 = __builtin_amdgcn_permlane32_swap(pk[4 * g], pk[4 * g + 2], false, false), r1 = __builtin_amdgcn_permlane32_swap(pk[4 * g + 1], pk[4 * g + 3], false, false);
                    __builtin_amdgcn_raw_buffer_store_b128((u32x4){r0[0], r1[0], r0[1], r1[1]}, ro, dst + g * 32, 0, OUT_AUX);
                }
            } else {
                OUT_PUBLISH(pk, 1);
                BAR();
                f32x16 acc = ZERO;
#define JOB(J0, J1, DST, XF) do { if (isV) { _Pragma("unroll") for (int j = J0; j < J1; ++j) DST = mfma(XF, Wj[j], DST); } \
                                  else { _Pragma("unroll") for (int j = J0; j < J1; ++j) DST = mfma(Wj[j], XF, DST); } asm volatile("" : "+v"(DST)); } while (0)
                u32x4 cf = ONES;
                if (prod) cf = cs_frag(colsum8(Wj, ONES), h, zero_);
                __builtin_amdgcn_s_sleep(HS_M);
                STAMP0(66); nre = 0;
                const int s0 = WS_X + 2097152 + (rb * 64 + tbo * 32 + wi * 4) * 1024;
                lptr d0 = lds + L_AT + (tbo * 32 + wi * 4) * 1024 + lane * 16;
                const int ssrc = WS_ST + 262144 + rb * 8192 + (wi >> 1) * 4096 + (wi & 1) * 128; const unsigned svo = (lane >> 4) * 1024 + (lane & 15) * 8;
                u32x4 r[8]; StatLd sv;
                stats_load(sv, svo, rd, ssrc);
                gld4(r[0], r[1], r[2], r[3], lane * 16, rd, s0); gld4(r[4], r[5], r[6], r[7], lane * 16, rd, s0 + 16384);
                stage_stats<8>(sv, svo, rd, ssrc, lds, wi * 16 + (lane & 15)); STAMP0(69);
                valid4<4>(r[0], r[1], r[2], r[3], lane * 16, rd, s0 NRETRY); valid4<0>(r[4], r[5], r[6], r[7], lane * 16, rd, s0 + 16384 NRETRY); STAMP0(67);
                if (prod) JOB(0, 8, acc, r[j]);
                BAR();
                if (prod) { const u32x4 mf = mean_frag(lds, tbo * 32 + n); acc = isV ? mfma(mf, cf, acc) : mfma(cf, mf, acc); kv_partial(acc, isV, lds + L_KVB + wi * 4096, lane); }
                STAMP(21);
                BAR();
                tst = (unsigned)__builtin_amdgcn_s_memrealtime();
                float km = zero_;
                if (prod && w == 0) km = kv_final(lds + L_KVB, lds, isV, tbo, rw, WS_KVA + 131072 + (isV ? 65536 : 0) + (rb * 2 + tbo) * 1024, lane, zero_);
                STAMP0(70); STAMPV(71, nre);
#pragma unroll
                for (int j = 0; j < 8; ++j) ldsw(d0 + (j >> 2) * 16384 + (j & 3) * 1024, r[j]);
                BAR();
                if (prod && w == 0) { VM0(); st8(rw, only(lane == 0, WS_KAF + 16384 + (rb * 4 + cg) * 128), (u32x2){isV ? 0u : f2u(km), tag}); }
                STAMP0(72);
            }
        } else {
            FRESH();
            STAMP(19 + 20 * p);
            if (p == 1) { OUT_ISSUE(3, 50, HS_OB); OUT_STAGES(50); asm volatile("" : "=v"(xr0), "=v"(xr1)); MQA_SETUP(1); }
            else {
                const int f = wi & 1, tb = wi >> 1;
                { OUT_ISSUE(1, 50, HS_OB);
#pragma unroll
                  for (int j = 0; j < 16; ++j) ldsw(lds + L_PARK + (16 * wi + j) * 1024 + lane * 16, W[8 + j]);
                  OUT_STAGES(50); }
                STAMP(20);
#pragma unroll
                for (int i = 0; i < 32; ++i) W[i] = ldsr(lds + L_PARK + (f * 32 + i) * 1024 + lane * 16);
                f32x16 acc;
                const unsigned ap = lds0 + L_AT + tb * 32768 + lane * 16;
                BAR();
                const u32x4 cf = cs_frag(colsum32(W, ONES), h, zero_);
                const int s1 = WS_X + 2097152 + (rb * 64 + (tbo ^ 1) * 32 + wi * 4) * 1024; lptr d1 = lds + L_AT + ((tbo ^ 1) * 32 + wi * 4) * 1024 + lane * 16;
                u32x4 r[8];
                gld4(r[0], r[1], r[2], r[3], lane * 16, rd, s1); gld4(r[4], r[5], r[6], r[7], lane * 16, rd, s1 + 16384);
                MQA_SETUP(1);
                BAR();
                stage4<4>(r[0], r[1], r[2], r[3], lane * 16, rd, s1, d1 NRETRY);
                BAR();
                stage4<0>(r[4], r[5], r[6], r[7], lane * 16, rd, s1 + 16384, d1 + 16384 NRETRY);
                BAR();
                const u32x4 mf = mean_frag(lds, tb * 32 + n); const float rs = *(LAS float*)(lds + L_RSTD + (tb * 32 + n) * 4) * QSCALE;
                gs32<1024>(acc, W, ap); mean_step(acc, cf, mf);
                u32x4 q0, q1; fold_t(acc, rs, q0, q1);
                ldsw(lds + L_QX + ((2 * f) * 2 + tb) * 1024 + lane * 16, q0); ldsw(lds + L_QX + ((2 * f + 1) * 2 + tb) * 1024 + lane * 16, q1);
                xr0 = ldsr(lds + L_AT + (tbo * 32 + 8 * fg + 2 * wi) * 1024 + lane * 16); xr1 = ldsr(lds + L_AT + (tbo * 32 + 8 * fg + 2 * wi + 1) * 1024 + lane * 16);
                STAMP(21);
            }
        }
    }
}

NOPK __global__ void __launch_bounds__(512, 2) il_fwd(const char* __restrict__ x0, const char* __restrict__ wq, const char* __restrict__ wk, const char* __restrict__ wv,
                                                 const char* __restrict__ woa, const char* __restrict__ wqkv, const char* __restrict__ wob, char* __restrict__ out, char* ws) {
    extern __shared__ __attribute__((aligned(16))) char smem[];
    const int w = __builtin_amdgcn_readfirstlane(__builtin_amdgcn_workitem_id_x() >> 6);
    {
        unsigned long long pc; unsigned len; u32x4 junk;
        asm volatile(".Lil_here:\n\ts_getpc_b64 %0\n\ts_mov_b32 %1, .Lil_hot_end-.Lil_here" : "=s"(pc), "=s"(len));
        const unsigned off = umin_((((__builtin_amdgcn_workgroup_id_x() >> 3) & 7) * 512 + __builtin_amdgcn_workitem_id_x()) * 16, len & ~15u);
        asm volatile("global_load_dwordx4 %0, %1, %2" : "=&{v[244:247]}"(junk) : "v"(off), "s"(pc & ~15ull) : "memory");
    }
    if (w < 4) program<true>(x0, wq, wk, wv, woa, wqkv, wob, out, ws, (lptr)smem, w);
    else program<false>(x0, wq, wk, wv, woa, wqkv, wob, out, ws, (lptr)smem, w);
}

extern "C" void kernel_launch(void* const* d_in, const int* in_sizes, int n_in, void* d_out, int out_size, void* d_ws, size_t ws_size, hipStream_t stream) {
    (void)in_sizes; (void)n_in; (void)out_size; (void)ws_size;
    (void)hipFuncSetAttribute((const void*)il_fwd, hipFuncAttributeMaxDynamicSharedMemorySize, LDS_BYTES);
    hipLaunchKernelGGL(il_fwd, dim3(256), dim3(512), LDS_BYTES, stream, (const char*)d_in[0], (const char*)d_in[1], (const char*)d_in[2], (const char*)d_in[3], (const char*)d_in[4],
                       (const char*)d_in[6], (const char*)d_in[7], (char*)d_out, (char*)d_ws);
}
```
